```python
import torch

def _softmax(x, axis=-1):
    return torch.softmax(x, dim=axis)
def _gelu(x):
    return torch.nn.functional.gelu(x, approximate="tanh")
def _silu(x):
    return torch.nn.functional.silu(x)
def _sigmoid(x):
    return torch.sigmoid(x)
def _layernorm(x, axis=-1, eps=1e-5):
    m = x.mean(dim=axis, keepdim=True)
    v = x.var(dim=axis, keepdim=True, unbiased=False)
    return (x - m) / torch.sqrt(v + eps)
def _scatter_add(tgt, idx, src, axis=0):
    return tgt.clone().index_add_(axis, idx, src)
def _segment_sum(src, seg, n):
    out = torch.zeros((n,) + src.shape[1:], dtype=src.dtype)
    return out.index_add_(0, seg, src)
def _pad(x, pw):
    flat = tuple(v for pair in reversed(pw) for v in pair)
    return torch.nn.functional.pad(x, flat)

kernel_name = 'gated_mix__ssm_conv_s5__mixer_channel_mlp__prompt'

def setup_inputs():

    _w = (lambda a: (lambda t: t.to(torch.bfloat16) if t.is_floating_point() else t)(torch.as_tensor(a)))
    def _A_setup():
        B, S, D = 1, 2048, 1024
        P = D
        Lambda_re = -0.5 * (lambda _x: torch.exp(torch.as_tensor(_x)))((lambda *s: torch.randn(*s, dtype=torch.bfloat16) * 0.4)(P))
        Lambda_im = (lambda *s: torch.randn(*s, dtype=torch.bfloat16) * 0.8)(P) * 1.0
        B_re = (lambda *s: torch.randn(*s, dtype=torch.bfloat16) * 0.8)(P, D) * (D ** -0.5)
        B_im = (lambda *s: torch.randn(*s, dtype=torch.bfloat16) * 0.4)(P, D) * (D ** -0.5)
        C_re = (lambda *s: torch.randn(*s, dtype=torch.bfloat16) * 0.8)(D, P) * (P ** -0.5)
        C_im = (lambda *s: torch.randn(*s, dtype=torch.bfloat16) * 0.8)(D, P) * (P ** -0.5)
        log_dt = (lambda *s: torch.randn(*s, dtype=torch.bfloat16) * 0.4)(P) * 0.3 - 2.0
        Dskip = (lambda *s: torch.randn(*s, dtype=torch.bfloat16) * 0.1)(D)
        return {
            "x": _w((lambda *s: torch.randn(*s, dtype=torch.bfloat16) * 0.05)(B, S, D)),
            "Lambda_re": _w(Lambda_re),
            "Lambda_im": _w(Lambda_im),
            "B_re": _w(B_re),
            "B_im": _w(B_im),
            "C_re": _w(C_re),
            "C_im": _w(C_im),
            "log_dt": _w(log_dt),
            "Dskip": _w(Dskip),
        }
    _A_in = _A_setup()

    def _B_setup():
        B, S, D, F = 1, 2048, 1024, 3584
        return {
            "x": _w((lambda *s: torch.randn(*s, dtype=torch.bfloat16) * 0.05)(B, S, D)),
            "w1": _w((lambda *s: torch.randn(*s, dtype=torch.bfloat16) * 0.8)(D, F) * (D ** -0.5)),
            "b1": _w((lambda *s: torch.randn(*s, dtype=torch.bfloat16) * 0.2)(F)),
            "w2": _w((lambda *s: torch.randn(*s, dtype=torch.bfloat16) * 0.05)(F, D) * (F ** -0.5)),
            "b2": _w((lambda *s: torch.randn(*s, dtype=torch.bfloat16) * 0.05)(D)),
        }
    _B_in = _B_setup()

    return {
        "A_x": _A_in["x"],
        "A_Lambda_re": _A_in["Lambda_re"],
        "A_Lambda_im": _A_in["Lambda_im"],
        "A_B_re": _A_in["B_re"],
        "A_B_im": _A_in["B_im"],
        "A_C_re": _A_in["C_re"],
        "A_C_im": _A_in["C_im"],
        "A_log_dt": _A_in["log_dt"],
        "A_Dskip": _A_in["Dskip"],
        "B_x": _B_in["x"],
        "B_w1": _B_in["w1"],
        "B_b1": _B_in["b1"],
        "B_w2": _B_in["w2"],
        "B_b2": _B_in["b2"],
        "gate_w": _w((lambda *s: torch.randn(*s, dtype=torch.bfloat16) * 0.05)(1024) * 16.0),
    }

def reference(A_x, A_Lambda_re, A_Lambda_im, A_B_re, A_B_im, A_C_re, A_C_im, A_log_dt, A_Dskip, B_x, B_w1, B_b1, B_w2, B_b2, gate_w):
    def _A_ref(x, Lambda_re, Lambda_im, B_re, B_im, C_re, C_im, log_dt, Dskip):
        Bsz, L, H = x.shape
        Lam = Lambda_re + 1j * Lambda_im
        Bmat = B_re + 1j * B_im
        Cmat = C_re + 1j * C_im
        dt = torch.exp(log_dt)
        Lam_bar = torch.exp(dt * Lam)
        B_bar = ((Lam_bar - 1.0) / Lam)[:, None] * Bmat
        Bu = torch.einsum("bld,pd->blp", x + 0j, B_bar)
        h = torch.zeros_like(Bu[:, 0, :])
        hs = []
        for t in range(L):
            h = Lam_bar[None, :] * h + Bu[:, t, :]
            hs.append(h)
        hseq = torch.stack(hs, 1)
        y = 2.0 * torch.einsum("blp,dp->bld", hseq, Cmat).real + x * Dskip[None, None, :]
        return y

    def _B_ref(x, w1, b1, w2, b2):
        y = _layernorm(x, axis=-1)
        h = _gelu(torch.einsum("bsd,df->bsf", y, w1) + b1)
        return torch.einsum("bsf,fd->bsd", h, w2) + b2

    x = A_x
    g = _sigmoid((x * gate_w).sum(-1, keepdims=True))
    a = _A_ref(x, A_Lambda_re, A_Lambda_im, A_B_re, A_B_im, A_C_re, A_C_im, A_log_dt, A_Dskip)
    b = _B_ref(x, B_w1, B_b1, B_w2, B_b2)
    return g * a + (1 - g) * b
```

```cpp
#include <hip/hip_runtime.h>
#include <hip/hip_bf16.h>

namespace gm {
#define LAS __attribute__((address_space(3)))
#define GAS __attribute__((address_space(1)))
typedef unsigned short u16;
typedef short bf16x8 __attribute__((ext_vector_type(8)));
typedef short s16x4 __attribute__((ext_vector_type(4)));
typedef float f32x4 __attribute__((ext_vector_type(4)));
typedef float f32x2 __attribute__((ext_vector_type(2)));
typedef unsigned u32x4 __attribute__((ext_vector_type(4)));
typedef unsigned u32x2 __attribute__((ext_vector_type(2)));
typedef LAS char* ldsp;
typedef GAS unsigned gu32;

#define BAR() __builtin_amdgcn_s_barrier()
#define WAITV(n) asm volatile("s_waitcnt vmcnt(%0)" ::"i"(n) : "memory")
#define SCHED0() __builtin_amdgcn_sched_barrier(0)
#define RLX_AGENT __ATOMIC_RELAXED, __HIP_MEMORY_SCOPE_AGENT

constexpr unsigned WS_CNTY = 0;
constexpr unsigned WS_CNTS = 2048;
constexpr unsigned WS_CNTM = 6144;
constexpr unsigned WS_XFLAG = 16384;
constexpr unsigned WS_GRAN = 81920;
constexpr unsigned WS_SYNC_BYTES = 81920 + 262144;
constexpr unsigned WS_G = 1u << 20;
constexpr unsigned WS_Y = 2u << 20;
constexpr unsigned WS_A2 = 8u << 20;
constexpr unsigned WS_XB = 32u << 20;
constexpr unsigned WS_END = 40u << 20;

constexpr int SLOT = 32768, SLOTM = 45056;
constexpr int LDS_E = 135168;
constexpr int LDS_COEF = LDS_E + 4096;
constexpr int LDS_B1 = LDS_COEF + 2048;
constexpr int LDS_BYTES = LDS_B1 + 512;

enum { PH_S1 = 0, PH_M1 = 1, PH_GS = 2, PH_GM = 3 };

struct Args {
    const u16 *x, *lre, *lim, *bre, *bim, *cre, *cim, *ldt, *dsk, *w1, *b1, *w2, *b2, *gw;
    float* out;
    char* ws;
};

__device__ __forceinline__ bf16x8 lds128(ldsp p) { return *(const LAS bf16x8*)p; }
__device__ __forceinline__ s16x4 ldstr(ldsp p) { return __builtin_amdgcn_ds_read_tr16_b64_v4i16((LAS s16x4*)p); }
__device__ __forceinline__ bf16x8 cat(s16x4 a, s16x4 b) { return __builtin_shufflevector(a, b, 0, 1, 2, 3, 4, 5, 6, 7); }
__device__ __forceinline__ void dma16(const void* g, ldsp l) { __builtin_amdgcn_global_load_lds((const unsigned*)g, (LAS unsigned*)l, 16, 0, 0); }
__device__ __forceinline__ float bflo(unsigned w) { return __uint_as_float(w << 16); }
__device__ __forceinline__ float bfhi(unsigned w) { return __uint_as_float(w & 0xffff0000u); }
__device__ __forceinline__ float bf1(u16 b) { return __uint_as_float((unsigned)b << 16); }
__device__ __forceinline__ unsigned pkbf(float lo, float hi) {
    typedef __bf16 bf2_t __attribute__((ext_vector_type(2)));
    bf2_t v = {(__bf16)lo, (__bf16)hi};
    return __builtin_bit_cast(unsigned, v);
}
__device__ __forceinline__ bool deadline(unsigned long long t0) { return __builtin_amdgcn_s_memrealtime() - t0 > 400000ull; }

struct LaneC { unsigned a0, a1, b0, b1; };
template <int PH>
__device__ __forceinline__ LaneC lane_consts(int wm, int wn, int lane) {
    const int r = lane & 15, q = lane >> 4, rr = r >> 2, p = lane & 3;
    const unsigned rl = r * 128 + ((q ^ (r >> 1)) * 16);
    LaneC c;
    if constexpr (PH == PH_S1) { c.a0 = wm * 8192 + rl; c.a1 = c.a0 ^ 64; c.b0 = 16384 + wn * 8192 + rl; c.b1 = c.b0 ^ 64; }
    if constexpr (PH == PH_M1) {
        c.a0 = wm * 8192 + rl; c.a1 = c.a0 ^ 64;
        const unsigned t0 = 16384 + q * 7 * 512 + rr * 64 + ((q & 1) * 32) + p * 8;
        c.b0 = wn ? (t0 ^ 32) + 3 * 512 : t0;
        c.b1 = wn ? t0 + 4 * 512 : (t0 ^ 32);
    }
    if constexpr (PH == PH_GS) {
        c.a0 = (q * 8 + wm * 4) * 256 + (p >> 1) * 128 + (4 * (q & 1) + rr) * 16 + (p & 1) * 8; c.a1 = c.a0 ^ 64;
        c.b0 = 16384 + wn * 8192 + rl; c.b1 = c.b0 ^ 64;
    }
    if constexpr (PH == PH_GM) {
        c.a0 = wm * 8192 + lane * 16; c.a1 = c.a0;
        c.b0 = 16384 + q * 4 * 512 + wn * 1024 + rr * 64 + ((q & 1) * 32) + p * 8; c.b1 = c.b0 ^ 32;
    }
    return c;
}
template <int PH, int KK, int NB>
__device__ __forceinline__ void load_frags(ldsp slot, const LaneC& c, bf16x8 (&A)[4], bf16x8 (&B)[NB]) {
    if constexpr (PH == PH_S1 || PH == PH_M1) {
        ldsp pa = slot + (KK ? c.a1 : c.a0);
#pragma unroll
        for (int m = 0; m < 4; ++m) A[m] = lds128(pa + m * 2048);
    } else if constexpr (PH == PH_GS) {
        ldsp p0 = slot + c.a0, p1 = slot + c.a1;
#pragma unroll
        for (int m = 0; m < 4; ++m) A[m] = cat(ldstr(p0 + (KK * 32 + m) * 256), ldstr(p1 + (KK * 32 + m) * 256));
    } else {
        ldsp pa = slot + c.a0;
#pragma unroll
        for (int m = 0; m < 4; ++m) A[m] = lds128(pa + (m * 2 + KK) * 1024);
    }
    if constexpr (PH == PH_S1 || PH == PH_GS) {
        ldsp pb = slot + (KK ? c.b1 : c.b0);
#pragma unroll
        for (int n = 0; n < NB; ++n) B[n] = lds128(pb + n * 2048);
    } else {
        constexpr int NS2 = PH == PH_M1 ? 7 : 4;
        ldsp p0 = slot + c.b0, p1 = slot + c.b1;
#pragma unroll
        for (int n = 0; n < NB; ++n) { ldsp p = ((n & 1) ? p1 : p0) + KK * 4 * NS2 * 512 + (n >> 1) * 512; B[n] = cat(ldstr(p), ldstr(p + 256)); }
    }
}
template <int PH, int NB>
__device__ __forceinline__ void mma(f32x4 (&acc)[NB][4], const bf16x8 (&A)[4], const bf16x8 (&B)[NB]) {
#pragma unroll
    for (int n = 0; n < NB; ++n)
#pragma unroll
        for (int m = 0; m < 4; ++m) {
            if constexpr (PH == PH_S1) acc[n][m] = __builtin_amdgcn_mfma_f32_16x16x32_bf16(A[m], B[n], acc[n][m], 0, 0, 0);
            else acc[n][m] = __builtin_amdgcn_mfma_f32_16x16x32_bf16(B[n], A[m], acc[n][m], 0, 0, 0);
        }
}
template <int PH> constexpr int nreads() { return PH == PH_S1 ? 8 : PH == PH_M1 ? 18 : 12; }
template <int NR, int NM>
__device__ __forceinline__ void sched_half() {
#pragma unroll
    for (int i = 0; i < NR; ++i) { __builtin_amdgcn_sched_group_barrier(0x8, 1, 0); __builtin_amdgcn_sched_group_barrier(0x100, 1, 0); }
    __builtin_amdgcn_sched_group_barrier(0x8, NM - NR, 0);
}
template <int PC, int PN, bool LAST, int NB>
__device__ __forceinline__ void cstep(ldsp cur, ldsp nxt, const LaneC& cc, const LaneC& cn, f32x4 (&acc)[NB][4], bf16x8 (&A0)[4], bf16x8 (&B0)[NB], bf16x8 (&A1)[4], bf16x8 (&B1)[NB]) {
    load_frags<PC, 1, NB>(cur, cc, A1, B1);
    mma<PC, NB>(acc, A0, B0);
    sched_half<nreads<PC>(), 4 * NB>();
    SCHED0();
    __builtin_amdgcn_s_waitcnt(0xC07F);
    BAR();
    SCHED0();
    if constexpr (!LAST) load_frags<PN, 0, NB>(nxt, cn, A0, B0);
    mma<PC, NB>(acc, A1, B1);
    if constexpr (!LAST) sched_half<nreads<PN>(), 4 * NB>();
    SCHED0();
}

__device__ __forceinline__ void wait_count(gu32* cnt, unsigned v, unsigned need) {
    if ((unsigned)__builtin_amdgcn_readfirstlane(v) < need) {
        const unsigned long long t0 = __builtin_amdgcn_s_memrealtime();
        do { __builtin_amdgcn_s_sleep(2); v = __hip_atomic_load(cnt, RLX_AGENT); } while ((unsigned)__builtin_amdgcn_readfirstlane(v) < need && !deadline(t0));
    }
    __builtin_amdgcn_fence(__ATOMIC_ACQUIRE, "agent");
}
__device__ __forceinline__ void signal_add(gu32* cnt, int lane) {
    WAITV(0);
    if (lane == 0) __hip_atomic_fetch_add(cnt, 1u, RLX_AGENT);
}
#define PREFETCH(dummy, p) asm volatile("global_load_dword %0, %1, off" : "+v"(dummy) : "v"(p) : "memory")

__device__ __forceinline__ float gelu_tanh(float v) {
    const float u = v * (1.5957691216f + 0.0713548163f * v * v);
    const float e = __builtin_amdgcn_exp2f(u * -1.4426950409f);
    return v * __builtin_amdgcn_rcpf(1.0f + e);
}

template <int WN>
__device__ __forceinline__ void m1_epilogue(f32x4 (&acc)[7][4], ldsp smem, __amdgpu_buffer_rsrc_t rs, const float* gptr, int rb, int cb, int wm, int lane) {
    const int r = lane & 15, q = lane >> 4;
    float og[4];
#pragma unroll
    for (int m = 0; m < 4; ++m) og[m] = 1.0f - gptr[rb * 128 + wm * 64 + m * 16 + r];
    u32x2 pk[7][4];
#pragma unroll
    for (int n = 0; n < 7; ++n) {
        const u32x2 bw = *(const LAS u32x2*)(smem + LDS_B1 + (WN * 112 + n * 16 + q * 4) * 2);
        const float b0 = bflo(bw.x), b1 = bfhi(bw.x), b2 = bflo(bw.y), b3 = bfhi(bw.y);
#pragma unroll
        for (int m = 0; m < 4; ++m) {
            const f32x4 a = acc[n][m];
            pk[n][m].x = pkbf(gelu_tanh(a[0] + b0) * og[m], gelu_tanh(a[1] + b1) * og[m]);
            pk[n][m].y = pkbf(gelu_tanh(a[2] + b2) * og[m], gelu_tanh(a[3] + b3) * og[m]);
        }
        constexpr int dummy = 0; (void)dummy;
        const int nbt = 7 * WN + n;
        if ((nbt & 1) == 1 || (WN == 0 && n == 6)) {
            const int pair = 7 * cb + (nbt >> 1);
            const unsigned base = WS_A2 + (unsigned)(rb * 88 + 32 + (pair >> 1)) * 16384u + (unsigned)(pair & 1) * 1024u + (unsigned)(wm * 4) * 2048u;
#pragma unroll
            for (int m = 0; m < 4; ++m) {
                if (WN == 0 && n == 6) __builtin_amdgcn_raw_buffer_store_b64(pk[n][m], rs, lane * 16 + m * 2048, base, 16);
                else if (WN == 1 && n == 0) __builtin_amdgcn_raw_buffer_store_b64(pk[n][m], rs, lane * 16 + 8 + m * 2048, base, 16);
                else { const u32x4 v = {pk[n > 0 ? n - 1 : 0][m].x, pk[n > 0 ? n - 1 : 0][m].y, pk[n][m].x, pk[n][m].y};
                       __builtin_amdgcn_raw_buffer_store_b128(v, rs, lane * 16 + m * 2048, base, 16); }
            }
        }
    }
}

__global__ __launch_bounds__(512, 2) void fused_kernel(Args a) {
    extern __shared__ __attribute__((aligned(16))) char smem_[];
    ldsp smem = (ldsp)smem_;
    const int wid = __builtin_amdgcn_readfirstlane(threadIdx.x >> 6), lane = threadIdx.x & 63;
    const int bid = blockIdx.x, xcd = bid & 7, rk = bid >> 3;
    const int kh = xcd & 1, rb = (xcd >> 1) * 4 + (rk >> 3), c8 = rk & 7, cb = kh * 8 + c8;
    char* const ws = a.ws;

    if (wid >= 4) {
        const int w = wid - 4;
        const unsigned swz = (unsigned)(((lane & 7) ^ ((4 * (w & 1) + (lane >> 4)) & 7)) * 16);
        {
            const unsigned va = (unsigned)((rb * 128 + 16 * (lane >> 5) + ((lane >> 3) & 3) + 32 * (w & 1) + 4 * (w >> 1)) * 2048) + swz;
            const unsigned vb = (unsigned)((cb * 64 + 16 * (w >> 1) + 8 * (w & 1) + (lane >> 3)) * 2048) + swz;
            auto issue = [&](int t) {
                ldsp s = smem + (t & 3) * SLOT + w * 1024;
                const char* pa = (const char*)a.x + t * 128;
#pragma unroll
                for (int n = 0; n < 4; ++n) dma16(pa + (64 * (n >> 1) + 8 * (n & 1)) * 2048 + va, s + n * 4096);
#pragma unroll
                for (int n = 0; n < 4; ++n) dma16((const char*)((n & 1) ? a.bim : a.bre) + t * 128 + (n >> 1) * 32 * 2048 + vb, s + 16384 + n * 4096);
            };
_Pragma("unroll 1")
            for (int t = 0; t < 4; ++t) issue(t);
            WAITV(24); BAR();
_Pragma("unroll 1")
            for (int t = 0; t < 12; ++t) { WAITV(16); BAR(); issue(t + 4); }
            WAITV(16); BAR();
            WAITV(8); BAR();
            WAITV(0); BAR();
            BAR();
        }
        {
            const unsigned va = (unsigned)((rb * 128 + 8 * w + (lane >> 3)) * 2048) + swz;
            unsigned vb[7];
#pragma unroll
            for (int i = 0; i < 7; ++i) {
                const int sl = 2 * i + (lane >> 5), kq = 2 * w + (sl >= 7 ? 1 : 0), nb2 = sl >= 7 ? sl - 7 : sl;
                vb[i] = (unsigned)((8 * kq + ((lane >> 2) & 7)) * 7168 + cb * 448 + nb2 * 64 + (((lane & 3) ^ (2 * (kq & 1))) * 16));
            }
            int slot = 0;
            auto issue = [&](int t) {
                ldsp s = smem + slot * SLOTM;
                slot = slot == 2 ? 0 : slot + 1;
                const char* pa = ws + WS_Y + t * 128;
#pragma unroll
                for (int n = 0; n < 4; ++n) dma16(pa + n * 32 * 2048 + va, s + w * 1024 + n * 4096);
                const char* pb = (const char*)a.w1 + (size_t)t * 64 * 7168;
#pragma unroll
                for (int i = 0; i < 7; ++i) dma16(pb + vb[i], s + 16384 + (w * 7 + i) * 1024);
            };
_Pragma("unroll 1")
            for (int t = 0; t < 3; ++t) issue(t);
            BAR();
            WAITV(22); BAR();
_Pragma("unroll 1")
            for (int t = 0; t < 13; ++t) { WAITV(11); BAR(); issue(t + 3); }
            WAITV(11); BAR();
            WAITV(0); BAR();
            BAR();
        }
        {
            const char* a2 = ws + WS_A2 + (size_t)rb * 88 * 16384 + w * 4096 + lane * 16;
            const unsigned vr = (unsigned)((c8 * 128 + 8 * w + (lane >> 3)) * 2048) + swz;
            const int rho = (lane >> 2) & 7;
            unsigned vt[2];
#pragma unroll
            for (int qq = 0; qq < 2; ++qq) { const int q = 2 * (w & 1) + qq; const int k = 32 * (w >> 1) + (rho < 4 ? 4 * q + rho : 16 + 4 * q + rho - 4);
                vt[qq] = (unsigned)(k * 2048 + c8 * 256 + (lane >> 5) * 64 + (((lane & 3) ^ (2 * qq)) * 16)); }
            auto issue = [&](int t) {
                ldsp s = smem + (t & 3) * SLOT;
                if (t < 16) {
                    const int ks = kh * 16 + t;
                    const char* pa = a2 + (size_t)ks * 16384;
#pragma unroll
                    for (int n = 0; n < 4; ++n) dma16(pa + n * 1024, s + w * 4096 + n * 1024);
                    const char* pb = (const char*)((ks & 1) ? a.cim : a.cre) + (ks >> 1) * 128;
#pragma unroll
                    for (int n = 0; n < 4; ++n) dma16(pb + n * 32 * 2048 + vr, s + 16384 + (w + 4 * n) * 1024);
                } else {
                    const int km = kh * 28 + (t - 16);
                    const char* pa = a2 + (size_t)(32 + km) * 16384;
#pragma unroll
                    for (int n = 0; n < 4; ++n) dma16(pa + n * 1024, s + w * 4096 + n * 1024);
                    const char* pb = (const char*)a.w2 + (size_t)km * 64 * 2048;
#pragma unroll
                    for (int n = 0; n < 4; ++n) dma16(pb + (n & 1) * 128 + vt[n >> 1], s + 16384 + (w * 4 + n) * 1024);
                }
            };
_Pragma("unroll 1")
            for (int t = 0; t < 4; ++t) issue(t);
            WAITV(24); BAR();
_Pragma("unroll 1")
            for (int t = 0; t < 40; ++t) { WAITV(16); BAR(); issue(t + 4); }
            WAITV(16); BAR();
            WAITV(8); BAR();
            WAITV(0); BAR();
            BAR();
        }
        return;
    }

    const int wm = wid >> 1, wn = wid & 1;
    const __amdgpu_buffer_rsrc_t rs = __builtin_amdgcn_make_buffer_rsrc(ws, 0, WS_END, 0x00020000);
    gu32* const cntY = (gu32*)(ws + WS_CNTY) + rb * 32;
    gu32* const cntS = (gu32*)(ws + WS_CNTS) + (rb * 2 + kh) * 32;
    gu32* const cntM = (gu32*)(ws + WS_CNTM) + (rb * 2 + kh) * 32;
    const float* const gptr = (const float*)(ws + WS_G);
    unsigned pf = 0;
    {
        if (wid < 2) { const char* p = (const char*)(wid ? a.bim : a.bre) + bid * 8192 + lane * 128; PREFETCH(pf, p); }
        const int row = bid * 8 + wid * 2;
        u32x4 xv[2][2], gv[2];
#pragma unroll
        for (int i = 0; i < 2; ++i)
#pragma unroll
            for (int h = 0; h < 2; ++h) xv[i][h] = *(const u32x4*)(a.x + (size_t)(row + i) * 1024 + h * 512 + lane * 8);
#pragma unroll
        for (int h = 0; h < 2; ++h) gv[h] = *(const u32x4*)(a.gw + h * 512 + lane * 8);
        if (wid == 3) {
            const int p = cb * 64 + lane;
            const float lr = bf1(a.lre[p]), li = bf1(a.lim[p]);
            const float dt = (float)(__bf16)expf(bf1(a.ldt[p]));
            const float zr = dt * lr, zi = dt * li;
            float sn, cs; sincosf(zi, &sn, &cs);
            const float er = expf(zr), Lr = er * cs, Li = er * sn;
            const float den = 1.0f / (lr * lr + li * li), nr = Lr - 1.0f;
            const float cr = (nr * lr + Li * li) * den, ci = (Li * lr - nr * li) * den;
            float pr = Lr, pi = Li;
#pragma unroll
            for (int i = 0; i < 4; ++i) { const float t = pr * pr - pi * pi; pi = 2.0f * pr * pi; pr = t; }
            const float p16r = pr, p16i = pi;
#pragma unroll
            for (int i = 0; i < 3; ++i) { const float t = pr * pr - pi * pi; pi = 2.0f * pr * pi; pr = t; }
            *(LAS f32x4*)(smem + LDS_COEF + lane * 32) = (f32x4){Lr, Li, cr, ci};
            *(LAS f32x4*)(smem + LDS_COEF + lane * 32 + 16) = (f32x4){p16r, p16i, pr, pi};
        }
        if (wid == 2 && lane < 56) *(LAS u32x2*)(smem + LDS_B1 + lane * 8) = *(const u32x2*)(a.b1 + cb * 224 + lane * 4);
        float xf[2][16], s[2], gd[2];
#pragma unroll
        for (int i = 0; i < 2; ++i) {
            s[i] = 0.f; gd[i] = 0.f;
#pragma unroll
            for (int h = 0; h < 2; ++h)
#pragma unroll
                for (int j = 0; j < 4; ++j) {
                    const float lo = bflo(xv[i][h][j]), hi = bfhi(xv[i][h][j]);
                    xf[i][h * 8 + 2 * j] = lo; xf[i][h * 8 + 2 * j + 1] = hi;
                    s[i] += lo + hi; gd[i] += lo * bflo(gv[h][j]) + hi * bfhi(gv[h][j]);
                }
        }
#pragma unroll
        for (int o = 32; o >= 1; o >>= 1) {
#pragma unroll
            for (int i = 0; i < 2; ++i) { s[i] += __shfl_xor(s[i], o); gd[i] += __shfl_xor(gd[i], o); }
        }
        float vs[2];
#pragma unroll
        for (int i = 0; i < 2; ++i) {
            s[i] *= (1.0f / 1024.0f); vs[i] = 0.f;
#pragma unroll
            for (int j = 0; j < 16; ++j) { xf[i][j] -= s[i]; vs[i] += xf[i][j] * xf[i][j]; }
        }
#pragma unroll
        for (int o = 32; o >= 1; o >>= 1) {
#pragma unroll
            for (int i = 0; i < 2; ++i) vs[i] += __shfl_xor(vs[i], o);
        }
#pragma unroll
        for (int i = 0; i < 2; ++i) {
            const float rstd = 1.0f / sqrtf(vs[i] * (1.0f / 1024.0f) + 1e-5f);
#pragma unroll
            for (int h = 0; h < 2; ++h) {
                u32x4 o;
#pragma unroll
                for (int j = 0; j < 4; ++j) o[j] = pkbf(xf[i][h * 8 + 2 * j] * rstd, xf[i][h * 8 + 2 * j + 1] * rstd);
                __builtin_amdgcn_raw_buffer_store_b128(o, rs, lane * 16 + h * 1024, WS_Y + (unsigned)(row + i) * 2048u, 16);
            }
            const float g = 1.0f / (1.0f + expf(-gd[i]));
            if (lane == 0) __hip_atomic_store((gu32*)(ws + WS_G) + row + i, __float_as_uint(g), RLX_AGENT);
        }
    }
    bf16x8 A0[4], A1[4];
    {
        f32x4 acc[4][4];
#pragma unroll
        for (int n = 0; n < 4; ++n)
#pragma unroll
            for (int m = 0; m < 4; ++m) acc[n][m] = (f32x4){0.f, 0.f, 0.f, 0.f};
        bf16x8 B0[4], B1[4];
        const LaneC c = lane_consts<PH_S1>(wm, wn, lane);
        __builtin_amdgcn_s_waitcnt(0xC07F);
        BAR();
        load_frags<PH_S1, 0, 4>(smem, c, A0, B0);
        SCHED0();
#define S1STEP(t) cstep<PH_S1, PH_S1, false, 4>(smem + ((t) & 3) * SLOT, smem + (((t) + 1) & 3) * SLOT, c, c, acc, A0, B0, A1, B1)
_Pragma("unroll 1")
        for (int t = 0; t < 3; ++t) S1STEP(t);
        signal_add((gu32*)(ws + WS_CNTY) + (bid >> 4) * 32, lane);
        {
            const char* p = (const char*)a.w1 + bid * 28672 + wid * 8192 + (wid == 3 ? (lane & 31) : lane) * 128; PREFETCH(pf, p);
        }
_Pragma("unroll 1")
        for (int t = 3; t < 7; ++t) S1STEP(t);
        {
            const char* p = (const char*)a.w2 + bid * 28672 + wid * 8192 + (wid == 3 ? (lane & 31) : lane) * 128; PREFETCH(pf, p);
            if (wid >= 2) { const char* p2 = (const char*)(wid == 3 ? a.cim : a.cre) + bid * 8192 + lane * 128; PREFETCH(pf, p2); }
        }
        unsigned pv = 0;
        if (wid == 0) pv = __hip_atomic_load(cntY, RLX_AGENT);
_Pragma("unroll 1")
        for (int t = 7; t < 11; ++t) S1STEP(t);
        if (wid == 0) wait_count(cntY, pv, 64u);
_Pragma("unroll 1")
        for (int t = 11; t < 15; ++t) S1STEP(t);
        asm volatile("s_waitcnt vmcnt(0)" : "+v"(pf)::"memory");
        cstep<PH_S1, PH_S1, true, 4>(smem + 3 * SLOT, smem, c, c, acc, A0, B0, A1, B1);

        const int ch = lane & 15, q = lane >> 4, seg = wm * 4 + q;
        f32x4 gq[4];
#pragma unroll
        for (int m = 0; m < 4; ++m) gq[m] = *(const f32x4*)(gptr + rb * 128 + seg * 16 + m * 4);
        f32x4 k0[2], k1[2];
#pragma unroll
        for (int cbk = 0; cbk < 2; ++cbk) {
            const int cl = wn * 32 + cbk * 16 + ch;
            k0[cbk] = *(const LAS f32x4*)(smem + LDS_COEF + cl * 32); k1[cbk] = *(const LAS f32x4*)(smem + LDS_COEF + cl * 32 + 16);
        }
#pragma unroll
        for (int i = 1; i < 16; ++i)
#pragma unroll
            for (int cbk = 0; cbk < 2; ++cbk) {
                const float sr = acc[cbk][(i - 1) >> 2][(i - 1) & 3], si = acc[2 + cbk][(i - 1) >> 2][(i - 1) & 3];
                acc[cbk][i >> 2][i & 3] = fmaf(k0[cbk][0], sr, fmaf(-k0[cbk][1], si, acc[cbk][i >> 2][i & 3]));
                acc[2 + cbk][i >> 2][i & 3] = fmaf(k0[cbk][0], si, fmaf(k0[cbk][1], sr, acc[2 + cbk][i >> 2][i & 3]));
            }
#pragma unroll
        for (int cbk = 0; cbk < 2; ++cbk)
            *(LAS f32x2*)(smem + LDS_E + (seg * 64 + wn * 32 + cbk * 16 + ch) * 8) = (f32x2){acc[cbk][3][3], acc[2 + cbk][3][3]};
        __builtin_amdgcn_s_waitcnt(0xC07F);
        BAR();
        asm volatile("" ::: "memory");
        f32x2 e[2][8];
#pragma unroll
        for (int cbk = 0; cbk < 2; ++cbk)
#pragma unroll
            for (int sgi = 0; sgi < 8; ++sgi) e[cbk][sgi] = *(const LAS f32x2*)(smem + LDS_E + (sgi * 64 + wn * 32 + cbk * 16 + ch) * 8);
        if (wm == 0 && rb < 15) {
            float er[2] = {0.f, 0.f}, ei[2] = {0.f, 0.f};
#pragma unroll
            for (int sgi = 0; sgi < 8; ++sgi)
#pragma unroll
                for (int cbk = 0; cbk < 2; ++cbk) {
                    const float t = fmaf(k1[cbk][0], er[cbk], fmaf(-k1[cbk][1], ei[cbk], e[cbk][sgi][0]));
                    ei[cbk] = fmaf(k1[cbk][0], ei[cbk], fmaf(k1[cbk][1], er[cbk], e[cbk][sgi][1])); er[cbk] = t; }
            const float sr_ = q ? er[1] + 0.f : er[0] + 0.f, si_ = q ? ei[1] + 0.f : ei[0] + 0.f;
            if (q < 2) { const u32x4 v = {__float_as_uint(sr_), 1u, __float_as_uint(si_), 1u};
                __builtin_amdgcn_raw_buffer_store_b128(v, rs, (wn * 32 + q * 16 + ch) * 16, WS_GRAN + (unsigned)(cb * 16 + rb) * 1024u, 16); }
        }
#pragma unroll
        for (int cbk = 0; cbk < 2; ++cbk)
#pragma unroll
            for (int i = 0; i < 16; ++i) {
                const float g2 = 2.0f * gq[i >> 2][i & 3], sr = acc[cbk][i >> 2][i & 3], si = acc[2 + cbk][i >> 2][i & 3];
                acc[cbk][i >> 2][i & 3] = g2 * fmaf(k0[cbk][2], sr, -k0[cbk][3] * si);
                acc[2 + cbk][i >> 2][i & 3] = g2 * fmaf(k0[cbk][2], si, k0[cbk][3] * sr);
            }
        float Sr[2] = {0.f, 0.f}, Si[2] = {0.f, 0.f};
        if (rb > 0) {
            u32x4 gr[2][4];
            const unsigned long long t0 = __builtin_amdgcn_s_memrealtime();
            for (;;) {
                bool ok = true;
                asm volatile("" ::: "memory");
#pragma unroll
                for (int i = 0; i < 4; ++i) {
                    const int rp = rb - 1 - q - 4 * i, rc = rp < 0 ? 0 : rp;
#pragma unroll
                    for (int cbk = 0; cbk < 2; ++cbk) {
                        gr[cbk][i] = __builtin_amdgcn_raw_buffer_load_b128(rs, (wn * 32 + cbk * 16 + ch) * 16 + rc * 1024, WS_GRAN + (unsigned)cb * 16384u, 16);
                        ok &= (gr[cbk][i][1] == 1u) & (gr[cbk][i][3] == 1u);
                    }
                }
                if (__all(ok) || deadline(t0)) break;
                __builtin_amdgcn_s_sleep(1);
            }
#pragma unroll
            for (int cbk = 0; cbk < 2; ++cbk) {
                const float Pr = k1[cbk][2], Pi = k1[cbk][3];
                const float P2r = Pr * Pr - Pi * Pi, P2i = 2.0f * Pr * Pi, P4r = P2r * P2r - P2i * P2i, P4i = 2.0f * P2r * P2i;
                float hr = 0.f, hi = 0.f;
#pragma unroll
                for (int i = 3; i >= 0; --i) {
                    const bool valid = rb - 1 - q - 4 * i >= 0;
                    const float vr = valid ? __uint_as_float(gr[cbk][i][0]) : 0.f, vi = valid ? __uint_as_float(gr[cbk][i][2]) : 0.f;
                    const float t = fmaf(P4r, hr, fmaf(-P4i, hi, vr)); hi = fmaf(P4r, hi, fmaf(P4i, hr, vi)); hr = t;
                }
                const float ar = (q & 1) ? Pr : 1.0f, ai = (q & 1) ? Pi : 0.f, br = (q & 2) ? P2r : 1.0f, bi = (q & 2) ? P2i : 0.f;
                const float wr = ar * br - ai * bi, wi = ar * bi + ai * br;
                float tr = wr * hr - wi * hi, ti = wr * hi + wi * hr;
                tr += __shfl_xor(tr, 16); ti += __shfl_xor(ti, 16);
                tr += __shfl_xor(tr, 32); ti += __shfl_xor(ti, 32);
                Sr[cbk] = tr; Si[cbk] = ti;
            }
        }
#pragma unroll
        for (int cbk = 0; cbk < 2; ++cbk) {
            float cr = Sr[cbk], ci = Si[cbk];
#pragma unroll
            for (int sgi = 0; sgi < 7; ++sgi) {
                const float t = fmaf(k1[cbk][0], cr, fmaf(-k1[cbk][1], ci, e[cbk][sgi][0])), u = fmaf(k1[cbk][0], ci, fmaf(k1[cbk][1], cr, e[cbk][sgi][1]));
                cr = sgi < seg ? t : cr; ci = sgi < seg ? u : ci;
            }
            float wr = k0[cbk][2] * cr - k0[cbk][3] * ci, wi = k0[cbk][2] * ci + k0[cbk][3] * cr;
            float hr[16], hi[16];
#pragma unroll
            for (int i = 0; i < 16; ++i) {
                const float t = k0[cbk][0] * wr - k0[cbk][1] * wi; wi = k0[cbk][0] * wi + k0[cbk][1] * wr; wr = t;
                const float g2 = 2.0f * gq[i >> 2][i & 3];
                hr[i] = fmaf(g2, wr, acc[cbk][i >> 2][i & 3]); hi[i] = -fmaf(g2, wi, acc[2 + cbk][i >> 2][i & 3]);
            }
            const int kb = 4 * wn + 2 * cbk + (ch >> 3);
            const unsigned vo = (unsigned)((kb * 8 + seg) * 256 + (((ch & 7) ^ (4 * (kb & 1))) * 16));
            const unsigned so = WS_A2 + (unsigned)(rb * 88 + 2 * cb) * 16384u;
#pragma unroll
            for (int th = 0; th < 2; ++th) {
                u32x4 vr, vi;
#pragma unroll
                for (int j = 0; j < 4; ++j) { vr[j] = pkbf(hr[th * 8 + 2 * j], hr[th * 8 + 2 * j + 1]); vi[j] = pkbf(hi[th * 8 + 2 * j], hi[th * 8 + 2 * j + 1]); }
                __builtin_amdgcn_raw_buffer_store_b128(vr, rs, vo + th * 128, so, 16);
                __builtin_amdgcn_raw_buffer_store_b128(vi, rs, vo + th * 128, so + 16384u, 16);
            }
        }
    }
    {
        f32x4 acc[7][4];
#pragma unroll
        for (int n = 0; n < 7; ++n)
#pragma unroll
            for (int m = 0; m < 4; ++m) acc[n][m] = (f32x4){0.f, 0.f, 0.f, 0.f};
        bf16x8 B0[7], B1[7];
        const LaneC c = lane_consts<PH_M1>(wm, wn, lane);
        BAR();
        load_frags<PH_M1, 0, 7>(smem, c, A0, B0);
        SCHED0();
        int sc = 0;
#define M1STEP() do { const int sn_ = sc == 2 ? 0 : sc + 1; cstep<PH_M1, PH_M1, false, 7>(smem + sc * SLOTM, smem + sn_ * SLOTM, c, c, acc, A0, B0, A1, B1); sc = sn_; } while (0)
_Pragma("unroll 1")
        for (int t = 0; t < 3; ++t) M1STEP();
        signal_add(cntS, lane);
_Pragma("unroll 1")
        for (int t = 3; t < 8; ++t) M1STEP();
        unsigned pv = 0;
        if (wid == 0) pv = __hip_atomic_load(cntS, RLX_AGENT);
_Pragma("unroll 1")
        for (int t = 8; t < 11; ++t) M1STEP();
        if (wid == 0) wait_count(cntS, pv, 32u);
_Pragma("unroll 1")
        for (int t = 11; t < 15; ++t) M1STEP();
        WAITV(0);
        cstep<PH_M1, PH_M1, true, 7>(smem + sc * SLOTM, smem, c, c, acc, A0, B0, A1, B1);
        if (wn == 0) m1_epilogue<0>(acc, smem, rs, gptr, rb, cb, wm, lane);
        else m1_epilogue<1>(acc, smem, rs, gptr, rb, cb, wm, lane);
    }
    {
        f32x4 acc[4][4];
#pragma unroll
        for (int n = 0; n < 4; ++n)
#pragma unroll
            for (int m = 0; m < 4; ++m) acc[n][m] = (f32x4){0.f, 0.f, 0.f, 0.f};
        bf16x8 B0[4], B1[4];
        const LaneC cs = lane_consts<PH_GS>(wm, wn, lane);
        BAR();
        load_frags<PH_GS, 0, 4>(smem, cs, A0, B0);
        SCHED0();
#define GSSTEP(t) cstep<PH_GS, PH_GS, false, 4>(smem + ((t) & 3) * SLOT, smem + (((t) + 1) & 3) * SLOT, cs, cs, acc, A0, B0, A1, B1)
_Pragma("unroll 1")
        for (int t = 0; t < 3; ++t) GSSTEP(t);
        signal_add(cntM, lane);
_Pragma("unroll 1")
        for (int t = 3; t < 6; ++t) GSSTEP(t);
        unsigned pv = 0;
        if (wid == 0) pv = __hip_atomic_load(cntM, RLX_AGENT);
_Pragma("unroll 1")
        for (int t = 6; t < 8; ++t) GSSTEP(t);
        if (wid == 0) wait_count(cntM, pv, 32u);
_Pragma("unroll 1")
        for (int t = 8; t < 12; ++t) GSSTEP(t);
        WAITV(0);
_Pragma("unroll 1")
        for (int t = 12; t < 15; ++t) GSSTEP(t);
        const LaneC cm = lane_consts<PH_GM>(wm, wn, lane);
        cstep<PH_GS, PH_GM, false, 4>(smem + 3 * SLOT, smem, cs, cm, acc, A0, B0, A1, B1);
_Pragma("unroll 1")
        for (int t = 16; t < 43; ++t) cstep<PH_GM, PH_GM, false, 4>(smem + (t & 3) * SLOT, smem + ((t + 1) & 3) * SLOT, cm, cm, acc, A0, B0, A1, B1);
        cstep<PH_GM, PH_GM, true, 4>(smem + 3 * SLOT, smem, cm, cm, acc, A0, B0, A1, B1);

        const int r = lane & 15, q = lane >> 4;
        if (kh) {
#pragma unroll
            for (int n = 0; n < 4; ++n)
#pragma unroll
                for (int mm = 0; mm < 2; ++mm) { const f32x4 t = acc[n][mm]; acc[n][mm] = acc[n][2 + mm]; acc[n][2 + mm] = t; }
        }
        const unsigned xo = WS_XB + (unsigned)(bid * 4 + wid) * 8192u, xi = WS_XB + (unsigned)((bid ^ 1) * 4 + wid) * 8192u;
#pragma unroll
        for (int n = 0; n < 4; ++n)
#pragma unroll
            for (int mm = 0; mm < 2; ++mm) {
                const f32x4 v = acc[n][2 + mm];
                __builtin_amdgcn_raw_buffer_store_b128(__builtin_bit_cast(u32x4, v), rs, lane * 16 + (n * 2 + mm) * 1024, xo, 16);
            }
        f32x4 o[4][2];
        const int t0r = rb * 128 + wm * 64 + kh * 32 + r, d0 = c8 * 128 + wn * 64 + q * 4;
#pragma unroll
        for (int mm = 0; mm < 2; ++mm) {
            const float g = gptr[t0r + mm * 16];
#pragma unroll
            for (int n = 0; n < 4; ++n) {
                const u32x2 xw = *(const u32x2*)(a.x + (size_t)(t0r + mm * 16) * 1024 + d0 + n * 16);
                const u32x2 dw = *(const u32x2*)(a.dsk + d0 + n * 16), bw = *(const u32x2*)(a.b2 + d0 + n * 16);
                const f32x4 own = acc[n][mm];
                o[n][mm][0] = own[0] + g * (bflo(xw.x) * bflo(dw.x)) + (1.0f - g) * bflo(bw.x);
                o[n][mm][1] = own[1] + g * (bfhi(xw.x) * bfhi(dw.x)) + (1.0f - g) * bfhi(bw.x);
                o[n][mm][2] = own[2] + g * (bflo(xw.y) * bflo(dw.y)) + (1.0f - g) * bflo(bw.y);
                o[n][mm][3] = own[3] + g * (bfhi(xw.y) * bfhi(dw.y)) + (1.0f - g) * bfhi(bw.y);
            }
        }
        WAITV(0);
        if (lane == 0) __hip_atomic_store((gu32*)(ws + WS_XFLAG) + (bid * 4 + wid) * 16, 1u, RLX_AGENT);
        {
            gu32* fl = (gu32*)(ws + WS_XFLAG) + ((bid ^ 1) * 4 + wid) * 16;
            wait_count(fl, __hip_atomic_load(fl, RLX_AGENT), 1u);
        }
#pragma unroll
        for (int n = 0; n < 4; ++n)
#pragma unroll
            for (int mm = 0; mm < 2; ++mm) {
                const u32x4 v = __builtin_amdgcn_raw_buffer_load_b128(rs, lane * 16 + (n * 2 + mm) * 1024, xi, 16);
                o[n][mm] += __builtin_bit_cast(f32x4, v);
            }
#pragma unroll
        for (int mm = 0; mm < 2; ++mm)
#pragma unroll
            for (int n = 0; n < 4; ++n) *(f32x4*)(a.out + (size_t)(t0r + mm * 16) * 1024 + d0 + n * 16) = o[n][mm];
    }
}
}

extern "C" void kernel_launch(void* const* d_in, const int* in_sizes, int n_in,
                              void* d_out, int out_size, void* d_ws, size_t ws_size,
                              hipStream_t stream) {
    using namespace gm;
    Args a;
    a.x = (const u16*)d_in[0]; a.lre = (const u16*)d_in[1]; a.lim = (const u16*)d_in[2]; a.bre = (const u16*)d_in[3]; a.bim = (const u16*)d_in[4];
    a.cre = (const u16*)d_in[5]; a.cim = (const u16*)d_in[6]; a.ldt = (const u16*)d_in[7]; a.dsk = (const u16*)d_in[8];
    a.w1 = (const u16*)d_in[10]; a.b1 = (const u16*)d_in[11]; a.w2 = (const u16*)d_in[12]; a.b2 = (const u16*)d_in[13]; a.gw = (const u16*)d_in[14];
    a.out = (float*)d_out; a.ws = (char*)d_ws;
    (void)hipFuncSetAttribute((const void*)fused_kernel, hipFuncAttributeMaxDynamicSharedMemorySize, LDS_BYTES);
    (void)hipMemsetAsync(d_ws, 0, WS_SYNC_BYTES, stream);
    fused_kernel<<<256, 512, LDS_BYTES, stream>>>(a);
}
```

```cpp
#include <hip/hip_runtime.h>
#include <hip/hip_bf16.h>

namespace gm {
#define LAS __attribute__((address_space(3)))
#define GAS __attribute__((address_space(1)))
typedef unsigned short u16;
typedef short bf16x8 __attribute__((ext_vector_type(8)));
typedef short s16x4 __attribute__((ext_vector_type(4)));
typedef float f32x4 __attribute__((ext_vector_type(4)));
typedef float f32x2 __attribute__((ext_vector_type(2)));
typedef unsigned u32x4 __attribute__((ext_vector_type(4)));
typedef unsigned u32x2 __attribute__((ext_vector_type(2)));
typedef LAS char* ldsp;
typedef GAS unsigned gu32;

#define BAR() __builtin_amdgcn_s_barrier()
#define WAITV(n) asm volatile("s_waitcnt vmcnt(%0)" ::"i"(n) : "memory")
#define SCHED0() __builtin_amdgcn_sched_barrier(0)
#define RLX_AGENT __ATOMIC_RELAXED, __HIP_MEMORY_SCOPE_AGENT

constexpr unsigned WS_CNTY = 0;
constexpr unsigned WS_CNTS = 2048;
constexpr unsigned WS_CNTM = 6144;
constexpr unsigned WS_XFLAG = 16384;
constexpr unsigned WS_GRAN = 81920;
constexpr unsigned WS_SYNC_BYTES = 81920 + 262144;
constexpr unsigned WS_G = 1u << 20;
constexpr unsigned WS_Y = 2u << 20;
constexpr unsigned WS_A2 = 8u << 20;
constexpr unsigned WS_XB = 32u << 20;
constexpr unsigned WS_END = 40u << 20;

constexpr int SLOT = 32768, SLOTM = 45056;
constexpr int LDS_E = 135168;
constexpr int LDS_COEF = LDS_E + 4096;
constexpr int LDS_B1 = LDS_COEF + 2048;
constexpr int LDS_BYTES = LDS_B1 + 512;

enum { PH_S1 = 0, PH_M1 = 1, PH_GS = 2, PH_GM = 3 };

struct Args {
    const u16 *x, *lre, *lim, *bre, *bim, *cre, *cim, *ldt, *dsk, *w1, *b1, *w2, *b2, *gw;
    float* out;
    char* ws;
};

__device__ __forceinline__ bf16x8 lds128(ldsp p) { return *(const LAS bf16x8*)p; }
__device__ __forceinline__ s16x4 ldstr(ldsp p) { return __builtin_amdgcn_ds_read_tr16_b64_v4i16((LAS s16x4*)p); }
__device__ __forceinline__ bf16x8 cat(s16x4 a, s16x4 b) { return __builtin_shufflevector(a, b, 0, 1, 2, 3, 4, 5, 6, 7); }
__device__ __forceinline__ void dma16(const void* g, ldsp l) { __builtin_amdgcn_global_load_lds((const unsigned*)g, (LAS unsigned*)l, 16, 0, 0); }
__device__ __forceinline__ void dma16c(const void* g, ldsp l) { __builtin_amdgcn_global_load_lds((const unsigned*)g, (LAS unsigned*)l, 16, 0, 16); }
__device__ __forceinline__ float bflo(unsigned w) { return __uint_as_float(w << 16); }
__device__ __forceinline__ float bfhi(unsigned w) { return __uint_as_float(w & 0xffff0000u); }
__device__ __forceinline__ float bf1(u16 b) { return __uint_as_float((unsigned)b << 16); }
__device__ __forceinline__ unsigned pkbf(float lo, float hi) {
    typedef __bf16 bf2_t __attribute__((ext_vector_type(2)));
    bf2_t v = {(__bf16)lo, (__bf16)hi};
    return __builtin_bit_cast(unsigned, v);
}
__device__ __forceinline__ bool deadline(unsigned long long t0) { return __builtin_amdgcn_s_memrealtime() - t0 > 400000ull; }

struct LaneC { unsigned a0, a1, b0, b1; };
template <int PH>
__device__ __forceinline__ LaneC lane_consts(int wm, int wn, int lane) {
    const int r = lane & 15, q = lane >> 4, rr = r >> 2, p = lane & 3;
    const unsigned rl = r * 128 + ((q ^ (r >> 1)) * 16);
    LaneC c;
    if constexpr (PH == PH_S1) { c.a0 = wm * 8192 + rl; c.a1 = c.a0 ^ 64; c.b0 = 16384 + wn * 8192 + rl; c.b1 = c.b0 ^ 64; }
    if constexpr (PH == PH_M1) {
        c.a0 = wm * 8192 + rl; c.a1 = c.a0 ^ 64;
        const unsigned t0 = 16384 + q * 7 * 512 + rr * 64 + ((q & 1) * 32) + p * 8;
        c.b0 = wn ? (t0 ^ 32) + 3 * 512 : t0;
        c.b1 = wn ? t0 + 4 * 512 : (t0 ^ 32);
    }
    if constexpr (PH == PH_GS) {
        c.a0 = (q * 8 + wm * 4) * 256 + (p >> 1) * 128 + (4 * (q & 1) + rr) * 16 + (p & 1) * 8; c.a1 = c.a0 ^ 64;
        c.b0 = 16384 + wn * 8192 + rl; c.b1 = c.b0 ^ 64;
    }
    if constexpr (PH == PH_GM) {
        c.a0 = wm * 8192 + lane * 16; c.a1 = c.a0;
        c.b0 = 16384 + q * 4 * 512 + wn * 1024 + rr * 64 + ((q & 1) * 32) + p * 8; c.b1 = c.b0 ^ 32;
    }
    return c;
}
template <int PH, int KK, int NB>
__device__ __forceinline__ void load_frags(ldsp slot, const LaneC& c, bf16x8 (&A)[4], bf16x8 (&B)[NB]) {
    if constexpr (PH == PH_S1 || PH == PH_M1) {
        ldsp pa = slot + (KK ? c.a1 : c.a0);
#pragma unroll
        for (int m = 0; m < 4; ++m) A[m] = lds128(pa + m * 2048);
    } else if constexpr (PH == PH_GS) {
        ldsp p0 = slot + c.a0, p1 = slot + c.a1;
#pragma unroll
        for (int m = 0; m < 4; ++m) A[m] = cat(ldstr(p0 + (KK * 32 + m) * 256), ldstr(p1 + (KK * 32 + m) * 256));
    } else {
        ldsp pa = slot + c.a0;
#pragma unroll
        for (int m = 0; m < 4; ++m) A[m] = lds128(pa + (m * 2 + KK) * 1024);
    }
    if constexpr (PH == PH_S1 || PH == PH_GS) {
        ldsp pb = slot + (KK ? c.b1 : c.b0);
#pragma unroll
        for (int n = 0; n < NB; ++n) B[n] = lds128(pb + n * 2048);
    } else {
        constexpr int NS2 = PH == PH_M1 ? 7 : 4;
        ldsp p0 = slot + c.b0, p1 = slot + c.b1;
#pragma unroll
        for (int n = 0; n < NB; ++n) { ldsp p = ((n & 1) ? p1 : p0) + KK * 4 * NS2 * 512 + (n >> 1) * 512; B[n] = cat(ldstr(p), ldstr(p + 256)); }
    }
}
template <int PH, int NB>
__device__ __forceinline__ void mma(f32x4 (&acc)[NB][4], const bf16x8 (&A)[4], const bf16x8 (&B)[NB]) {
#pragma unroll
    for (int n = 0; n < NB; ++n)
#pragma unroll
        for (int m = 0; m < 4; ++m) {
            if constexpr (PH == PH_S1) acc[n][m] = __builtin_amdgcn_mfma_f32_16x16x32_bf16(A[m], B[n], acc[n][m], 0, 0, 0);
            else acc[n][m] = __builtin_amdgcn_mfma_f32_16x16x32_bf16(B[n], A[m], acc[n][m], 0, 0, 0);
        }
}
template <int PH> constexpr int nreads() { return PH == PH_S1 ? 8 : PH == PH_M1 ? 18 : 12; }
template <int NR, int NM>
__device__ __forceinline__ void sched_half() {
#pragma unroll
    for (int i = 0; i < NR; ++i) { __builtin_amdgcn_sched_group_barrier(0x8, 1, 0); __builtin_amdgcn_sched_group_barrier(0x100, 1, 0); }
    __builtin_amdgcn_sched_group_barrier(0x8, NM - NR, 0);
}
template <int PC, int PN, bool LAST, int NB>
__device__ __forceinline__ void cstep(ldsp cur, ldsp nxt, const LaneC& cc, const LaneC& cn, f32x4 (&acc)[NB][4], bf16x8 (&A0)[4], bf16x8 (&B0)[NB], bf16x8 (&A1)[4], bf16x8 (&B1)[NB]) {
    load_frags<PC, 1, NB>(cur, cc, A1, B1);
    mma<PC, NB>(acc, A0, B0);
    sched_half<nreads<PC>(), 4 * NB>();
    SCHED0();
    __builtin_amdgcn_s_waitcnt(0xC07F);
    BAR();
    SCHED0();
    if constexpr (!LAST) load_frags<PN, 0, NB>(nxt, cn, A0, B0);
    mma<PC, NB>(acc, A1, B1);
    if constexpr (!LAST) sched_half<nreads<PN>(), 4 * NB>();
    SCHED0();
}

__device__ __forceinline__ void wait_count(gu32* cnt, unsigned v, unsigned need) {
    if ((unsigned)__builtin_amdgcn_readfirstlane(v) < need) {
        const unsigned long long t0 = __builtin_amdgcn_s_memrealtime();
        do { __builtin_amdgcn_s_sleep(2); v = __hip_atomic_load(cnt, RLX_AGENT); } while ((unsigned)__builtin_amdgcn_readfirstlane(v) < need && !deadline(t0));
    }
    asm volatile("" ::: "memory");
}
__device__ __forceinline__ void signal_add(gu32* cnt, int lane) {
    WAITV(0);
    if (lane == 0) __hip_atomic_fetch_add(cnt, 1u, RLX_AGENT);
}
#define STAMP(i) do { } while (0)
#define PREFETCH(dummy, p) asm volatile("global_load_dword %0, %1, off" : "+v"(dummy) : "v"(p) : "memory")

__device__ __forceinline__ float gelu_tanh(float v) {
    const float u = v * (1.5957691216f + 0.0713548163f * v * v);
    const float e = __builtin_amdgcn_exp2f(u * -1.4426950409f);
    return v * __builtin_amdgcn_rcpf(1.0f + e);
}

template <int WN>
__device__ __forceinline__ void m1_epilogue(f32x4 (&acc)[7][4], ldsp smem, __amdgpu_buffer_rsrc_t rs, int rb, int cb, int wm, int lane) {
    const int r = lane & 15, q = lane >> 4;
    float og[4];
#pragma unroll
    for (int m = 0; m < 4; ++m) og[m] = 1.0f - __uint_as_float(__builtin_amdgcn_raw_buffer_load_b32(rs, (wm * 64 + m * 16 + r) * 4, WS_G + rb * 512, 16));
    u32x2 pk[7][4];
#pragma unroll
    for (int n = 0; n < 7; ++n) {
        const u32x2 bw = *(const LAS u32x2*)(smem + LDS_B1 + (WN * 112 + n * 16 + q * 4) * 2);
        const float b0 = bflo(bw.x), b1 = bfhi(bw.x), b2 = bflo(bw.y), b3 = bfhi(bw.y);
#pragma unroll
        for (int m = 0; m < 4; ++m) {
            const f32x4 a = acc[n][m];
            pk[n][m].x = pkbf(gelu_tanh(a[0] + b0) * og[m], gelu_tanh(a[1] + b1) * og[m]);
            pk[n][m].y = pkbf(gelu_tanh(a[2] + b2) * og[m], gelu_tanh(a[3] + b3) * og[m]);
        }
        constexpr int dummy = 0; (void)dummy;
        const int nbt = 7 * WN + n;
        if ((nbt & 1) == 1 || (WN == 0 && n == 6)) {
            const int pair = 7 * cb + (nbt >> 1);
            const unsigned base = WS_A2 + (unsigned)(rb * 88 + 32 + (pair >> 1)) * 16384u + (unsigned)(pair & 1) * 1024u + (unsigned)(wm * 4) * 2048u;
#pragma unroll
            for (int m = 0; m < 4; ++m) {
                if (WN == 0 && n == 6) __builtin_amdgcn_raw_buffer_store_b64(pk[n][m], rs, lane * 16 + m * 2048, base, 16);
                else if (WN == 1 && n == 0) __builtin_amdgcn_raw_buffer_store_b64(pk[n][m], rs, lane * 16 + 8 + m * 2048, base, 16);
                else { const u32x4 v = {pk[n > 0 ? n - 1 : 0][m].x, pk[n > 0 ? n - 1 : 0][m].y, pk[n][m].x, pk[n][m].y};
                       __builtin_amdgcn_raw_buffer_store_b128(v, rs, lane * 16 + m * 2048, base, 16); }
            }
        }
    }
}

__global__ __launch_bounds__(512, 2) void fused_kernel(Args a) {
    extern __shared__ __attribute__((aligned(16))) char smem_[];
    ldsp smem = (ldsp)smem_;
    const int wid = __builtin_amdgcn_readfirstlane(threadIdx.x >> 6), lane = threadIdx.x & 63;
    const int bid = blockIdx.x, xcd = bid & 7, rk = bid >> 3;
    const int kh = xcd & 1, rb = (xcd >> 1) * 4 + (rk >> 3), c8 = rk & 7, cb = kh * 8 + c8;
    char* const ws = a.ws;

    if (wid >= 4) {
        const int w = wid - 4;
        const unsigned swz = (unsigned)(((lane & 7) ^ ((4 * (w & 1) + (lane >> 4)) & 7)) * 16);
        {
            const unsigned va = (unsigned)((rb * 128 + 16 * (lane >> 5) + ((lane >> 3) & 3) + 32 * (w & 1) + 4 * (w >> 1)) * 2048) + swz;
            const unsigned vb = (unsigned)((cb * 64 + 16 * (w >> 1) + 8 * (w & 1) + (lane >> 3)) * 2048) + swz;
            auto issue = [&](int t) {
                ldsp s = smem + (t & 3) * SLOT + w * 1024;
                const char* pa = (const char*)a.x + t * 128;
#pragma unroll
                for (int n = 0; n < 4; ++n) dma16(pa + (64 * (n >> 1) + 8 * (n & 1)) * 2048 + va, s + n * 4096);
#pragma unroll
                for (int n = 0; n < 4; ++n) dma16((const char*)((n & 1) ? a.bim : a.bre) + t * 128 + (n >> 1) * 32 * 2048 + vb, s + 16384 + n * 4096);
            };
_Pragma("unroll 1")
            for (int t = 0; t < 4; ++t) issue(t);
            WAITV(24); BAR();
_Pragma("unroll 1")
            for (int t = 0; t < 12; ++t) { WAITV(16); BAR(); issue(t + 4); }
            WAITV(16); BAR();
            WAITV(8); BAR();
            WAITV(0); BAR();
            BAR();
        }
        {
            const unsigned va = (unsigned)((rb * 128 + 8 * w + (lane >> 3)) * 2048) + swz;
            unsigned vb[7];
#pragma unroll
            for (int i = 0; i < 7; ++i) {
                const int sl = 2 * i + (lane >> 5), kq = 2 * w + (sl >= 7 ? 1 : 0), nb2 = sl >= 7 ? sl - 7 : sl;
                vb[i] = (unsigned)((8 * kq + ((lane >> 2) & 7)) * 7168 + cb * 448 + nb2 * 64 + (((lane & 3) ^ (2 * (kq & 1))) * 16));
            }
            int slot = 0;
            auto issue = [&](int t) {
                ldsp s = smem + slot * SLOTM;
                slot = slot == 2 ? 0 : slot + 1;
                const char* pa = ws + WS_Y + t * 128;
#pragma unroll
                for (int n = 0; n < 4; ++n) dma16c(pa + n * 32 * 2048 + va, s + w * 1024 + n * 4096);
                const char* pb = (const char*)a.w1 + (size_t)t * 64 * 7168;
#pragma unroll
                for (int i = 0; i < 7; ++i) dma16(pb + vb[i], s + 16384 + (w * 7 + i) * 1024);
            };
_Pragma("unroll 1")
            for (int t = 0; t < 3; ++t) issue(t);
            BAR();
            WAITV(22); BAR();
_Pragma("unroll 1")
            for (int t = 0; t < 13; ++t) { WAITV(11); BAR(); issue(t + 3); }
            WAITV(11); BAR();
            WAITV(0); BAR();
            BAR();
        }
        {
            const char* a2 = ws + WS_A2 + (size_t)rb * 88 * 16384 + w * 4096 + lane * 16;
            const unsigned vr = (unsigned)((c8 * 128 + 8 * w + (lane >> 3)) * 2048) + swz;
            const int rho = (lane >> 2) & 7;
            unsigned vt[2];
#pragma unroll
            for (int qq = 0; qq < 2; ++qq) { const int q = 2 * (w & 1) + qq; const int k = 32 * (w >> 1) + (rho < 4 ? 4 * q + rho : 16 + 4 * q + rho - 4);
                vt[qq] = (unsigned)(k * 2048 + c8 * 256 + (lane >> 5) * 64 + (((lane & 3) ^ (2 * qq)) * 16)); }
            auto issue = [&](int t) {
                ldsp s = smem + (t & 3) * SLOT;
                if (t < 16) {
                    const int ks = kh * 16 + t;
                    const char* pa = a2 + (size_t)ks * 16384;
#pragma unroll
                    for (int n = 0; n < 4; ++n) dma16c(pa + n * 1024, s + w * 4096 + n * 1024);
                    const char* pb = (const char*)((ks & 1) ? a.cim : a.cre) + (ks >> 1) * 128;
#pragma unroll
                    for (int n = 0; n < 4; ++n) dma16(pb + n * 32 * 2048 + vr, s + 16384 + (w + 4 * n) * 1024);
                } else {
                    const int km = kh * 28 + (t - 16);
                    const char* pa = a2 + (size_t)(32 + km) * 16384;
#pragma unroll
                    for (int n = 0; n < 4; ++n) dma16c(pa + n * 1024, s + w * 4096 + n * 1024);
                    const char* pb = (const char*)a.w2 + (size_t)km * 64 * 2048;
#pragma unroll
                    for (int n = 0; n < 4; ++n) dma16(pb + (n & 1) * 128 + vt[n >> 1], s + 16384 + (w * 4 + n) * 1024);
                }
            };
_Pragma("unroll 1")
            for (int t = 0; t < 4; ++t) issue(t);
            WAITV(24); BAR();
_Pragma("unroll 1")
            for (int t = 0; t < 40; ++t) { WAITV(16); BAR(); issue(t + 4); }
            WAITV(16); BAR();
            WAITV(8); BAR();
            WAITV(0); BAR();
            BAR();
        }
        return;
    }

    const int wm = wid >> 1, wn = wid & 1;
    const __amdgpu_buffer_rsrc_t rs = __builtin_amdgcn_make_buffer_rsrc(ws, 0, WS_END, 0x00020000);
    gu32* const cntY = (gu32*)(ws + WS_CNTY) + rb * 32;
    gu32* const cntS = (gu32*)(ws + WS_CNTS) + (rb * 2 + kh) * 32;
    gu32* const cntM = (gu32*)(ws + WS_CNTM) + (rb * 2 + kh) * 32;
    unsigned pf = 0;
    STAMP(0);
    {
        if (wid < 2) { const char* p = (const char*)(wid ? a.bim : a.bre) + bid * 8192 + lane * 128; PREFETCH(pf, p); }
        const int row = bid * 8 + wid * 2;
        u32x4 xv[2][2], gv[2];
#pragma unroll
        for (int i = 0; i < 2; ++i)
#pragma unroll
            for (int h = 0; h < 2; ++h) xv[i][h] = *(const u32x4*)(a.x + (size_t)(row + i) * 1024 + h * 512 + lane * 8);
#pragma unroll
        for (int h = 0; h < 2; ++h) gv[h] = *(const u32x4*)(a.gw + h * 512 + lane * 8);
        if (wid == 3) {
            const int p = cb * 64 + lane;
            const float lr = bf1(a.lre[p]), li = bf1(a.lim[p]);
            const float dt = (float)(__bf16)expf(bf1(a.ldt[p]));
            const float zr = dt * lr, zi = dt * li;
            float sn, cs; sincosf(zi, &sn, &cs);
            const float er = expf(zr), Lr = er * cs, Li = er * sn;
            const float den = 1.0f / (lr * lr + li * li), nr = Lr - 1.0f;
            const float cr = (nr * lr + Li * li) * den, ci = (Li * lr - nr * li) * den;
            float pr = Lr, pi = Li;
#pragma unroll
            for (int i = 0; i < 4; ++i) { const float t = pr * pr - pi * pi; pi = 2.0f * pr * pi; pr = t; }
            const float p16r = pr, p16i = pi;
#pragma unroll
            for (int i = 0; i < 3; ++i) { const float t = pr * pr - pi * pi; pi = 2.0f * pr * pi; pr = t; }
            *(LAS f32x4*)(smem + LDS_COEF + lane * 32) = (f32x4){Lr, Li, cr, ci};
            *(LAS f32x4*)(smem + LDS_COEF + lane * 32 + 16) = (f32x4){p16r, p16i, pr, pi};
        }
        if (wid == 2 && lane < 56) *(LAS u32x2*)(smem + LDS_B1 + lane * 8) = *(const u32x2*)(a.b1 + cb * 224 + lane * 4);
        float xf[2][16], s[2], gd[2];
#pragma unroll
        for (int i = 0; i < 2; ++i) {
            s[i] = 0.f; gd[i] = 0.f;
#pragma unroll
            for (int h = 0; h < 2; ++h)
#pragma unroll
                for (int j = 0; j < 4; ++j) {
                    const float lo = bflo(xv[i][h][j]), hi = bfhi(xv[i][h][j]);
                    xf[i][h * 8 + 2 * j] = lo; xf[i][h * 8 + 2 * j + 1] = hi;
                    s[i] += lo + hi; gd[i] += lo * bflo(gv[h][j]) + hi * bfhi(gv[h][j]);
                }
        }
#pragma unroll
        for (int o = 32; o >= 1; o >>= 1) {
#pragma unroll
            for (int i = 0; i < 2; ++i) { s[i] += __shfl_xor(s[i], o); gd[i] += __shfl_xor(gd[i], o); }
        }
        float vs[2];
#pragma unroll
        for (int i = 0; i < 2; ++i) {
            s[i] *= (1.0f / 1024.0f); vs[i] = 0.f;
#pragma unroll
            for (int j = 0; j < 16; ++j) { xf[i][j] -= s[i]; vs[i] += xf[i][j] * xf[i][j]; }
        }
#pragma unroll
        for (int o = 32; o >= 1; o >>= 1) {
#pragma unroll
            for (int i = 0; i < 2; ++i) vs[i] += __shfl_xor(vs[i], o);
        }
#pragma unroll
        for (int i = 0; i < 2; ++i) {
            const float rstd = 1.0f / sqrtf(vs[i] * (1.0f / 1024.0f) + 1e-5f);
#pragma unroll
            for (int h = 0; h < 2; ++h) {
                u32x4 o;
#pragma unroll
                for (int j = 0; j < 4; ++j) o[j] = pkbf(xf[i][h * 8 + 2 * j] * rstd, xf[i][h * 8 + 2 * j + 1] * rstd);
                __builtin_amdgcn_raw_buffer_store_b128(o, rs, lane * 16 + h * 1024, WS_Y + (unsigned)(row + i) * 2048u, 16);
            }
            const float g = 1.0f / (1.0f + expf(-gd[i]));
            if (lane == 0) __hip_atomic_store((gu32*)(ws + WS_G) + row + i, __float_as_uint(g), RLX_AGENT);
        }
    }
    bf16x8 A0[4], A1[4];
    {
        f32x4 acc[4][4];
#pragma unroll
        for (int n = 0; n < 4; ++n)
#pragma unroll
            for (int m = 0; m < 4; ++m) acc[n][m] = (f32x4){0.f, 0.f, 0.f, 0.f};
        bf16x8 B0[4], B1[4];
        const LaneC c = lane_consts<PH_S1>(wm, wn, lane);
        __builtin_amdgcn_s_waitcnt(0xC07F);
        STAMP(1);
        BAR();
        STAMP(2);
        load_frags<PH_S1, 0, 4>(smem, c, A0, B0);
        SCHED0();
#define S1STEP(t) cstep<PH_S1, PH_S1, false, 4>(smem + ((t) & 3) * SLOT, smem + (((t) + 1) & 3) * SLOT, c, c, acc, A0, B0, A1, B1)
_Pragma("unroll 1")
        for (int t = 0; t < 3; ++t) S1STEP(t);
        signal_add((gu32*)(ws + WS_CNTY) + (bid >> 4) * 32, lane);
        {
            const char* p = (const char*)a.w1 + bid * 28672 + wid * 8192 + (wid == 3 ? (lane & 31) : lane) * 128; PREFETCH(pf, p);
        }
_Pragma("unroll 1")
        for (int t = 3; t < 7; ++t) S1STEP(t);
        {
            const char* p = (const char*)a.w2 + bid * 28672 + wid * 8192 + (wid == 3 ? (lane & 31) : lane) * 128; PREFETCH(pf, p);
            if (wid >= 2) { const char* p2 = (const char*)(wid == 3 ? a.cim : a.cre) + bid * 8192 + lane * 128; PREFETCH(pf, p2); }
        }
_Pragma("unroll 1")
        for (int t = 7; t < 11; ++t) S1STEP(t);
        unsigned pv = 0;
        if (wid == 0) pv = __hip_atomic_load(cntY, RLX_AGENT);
_Pragma("unroll 1")
        for (int t = 11; t < 15; ++t) S1STEP(t);
        if (wid == 0) wait_count(cntY, pv, 64u);
        asm volatile("s_waitcnt vmcnt(0)" : "+v"(pf)::"memory");
        cstep<PH_S1, PH_S1, true, 4>(smem + 3 * SLOT, smem, c, c, acc, A0, B0, A1, B1);

        STAMP(3);
        const int ch = lane & 15, q = lane >> 4, seg = wm * 4 + q;
        f32x4 gq[4];
#pragma unroll
        for (int m = 0; m < 4; ++m) gq[m] = __builtin_bit_cast(f32x4, __builtin_amdgcn_raw_buffer_load_b128(rs, (seg * 16 + m * 4) * 4, WS_G + rb * 512, 16));
        f32x4 k0[2], k1[2];
#pragma unroll
        for (int cbk = 0; cbk < 2; ++cbk) {
            const int cl = wn * 32 + cbk * 16 + ch;
            k0[cbk] = *(const LAS f32x4*)(smem + LDS_COEF + cl * 32); k1[cbk] = *(const LAS f32x4*)(smem + LDS_COEF + cl * 32 + 16);
        }
#pragma unroll
        for (int i = 1; i < 16; ++i)
#pragma unroll
            for (int cbk = 0; cbk < 2; ++cbk) {
                const float sr = acc[cbk][(i - 1) >> 2][(i - 1) & 3], si = acc[2 + cbk][(i - 1) >> 2][(i - 1) & 3];
                acc[cbk][i >> 2][i & 3] = fmaf(k0[cbk][0], sr, fmaf(-k0[cbk][1], si, acc[cbk][i >> 2][i & 3]));
                acc[2 + cbk][i >> 2][i & 3] = fmaf(k0[cbk][0], si, fmaf(k0[cbk][1], sr, acc[2 + cbk][i >> 2][i & 3]));
            }
#pragma unroll
        for (int cbk = 0; cbk < 2; ++cbk)
            *(LAS f32x2*)(smem + LDS_E + (seg * 64 + wn * 32 + cbk * 16 + ch) * 8) = (f32x2){acc[cbk][3][3], acc[2 + cbk][3][3]};
        __builtin_amdgcn_s_waitcnt(0xC07F);
        BAR();
        STAMP(4);
        asm volatile("" ::: "memory");
        f32x2 e[2][8];
#pragma unroll
        for (int cbk = 0; cbk < 2; ++cbk)
#pragma unroll
            for (int sgi = 0; sgi < 8; ++sgi) e[cbk][sgi] = *(const LAS f32x2*)(smem + LDS_E + (sgi * 64 + wn * 32 + cbk * 16 + ch) * 8);
        if (wm == 0 && rb < 15) {
            float er[2] = {0.f, 0.f}, ei[2] = {0.f, 0.f};
#pragma unroll
            for (int sgi = 0; sgi < 8; ++sgi)
#pragma unroll
                for (int cbk = 0; cbk < 2; ++cbk) {
                    const float t = fmaf(k1[cbk][0], er[cbk], fmaf(-k1[cbk][1], ei[cbk], e[cbk][sgi][0]));
                    ei[cbk] = fmaf(k1[cbk][0], ei[cbk], fmaf(k1[cbk][1], er[cbk], e[cbk][sgi][1])); er[cbk] = t; }
            const float sr_ = q ? er[1] + 0.f : er[0] + 0.f, si_ = q ? ei[1] + 0.f : ei[0] + 0.f;
            if (q < 2) { const u32x4 v = {__float_as_uint(sr_), 1u, __float_as_uint(si_), 1u};
                __builtin_amdgcn_raw_buffer_store_b128(v, rs, (wn * 32 + q * 16 + ch) * 16, WS_GRAN + (unsigned)(cb * 16 + rb) * 1024u, 16); }
        }
#pragma unroll
        for (int cbk = 0; cbk < 2; ++cbk)
#pragma unroll
            for (int i = 0; i < 16; ++i) {
                const float g2 = 2.0f * gq[i >> 2][i & 3], sr = acc[cbk][i >> 2][i & 3], si = acc[2 + cbk][i >> 2][i & 3];
                acc[cbk][i >> 2][i & 3] = g2 * fmaf(k0[cbk][2], sr, -k0[cbk][3] * si);
                acc[2 + cbk][i >> 2][i & 3] = g2 * fmaf(k0[cbk][2], si, k0[cbk][3] * sr);
            }
        STAMP(5);
        float Sr[2] = {0.f, 0.f}, Si[2] = {0.f, 0.f};
        if (rb > 0) {
            u32x4 gr[2][4];
            const unsigned long long t0 = __builtin_amdgcn_s_memrealtime();
            for (;;) {
                bool ok = true;
                asm volatile("" ::: "memory");
#pragma unroll
                for (int i = 0; i < 4; ++i) {
                    const int rp = rb - 1 - q - 4 * i, rc = rp < 0 ? 0 : rp;
#pragma unroll
                    for (int cbk = 0; cbk < 2; ++cbk) {
                        gr[cbk][i] = __builtin_amdgcn_raw_buffer_load_b128(rs, (wn * 32 + cbk * 16 + ch) * 16 + rc * 1024, WS_GRAN + (unsigned)cb * 16384u, 16);
                        ok &= (gr[cbk][i][1] == 1u) & (gr[cbk][i][3] == 1u);
                    }
                }
                if (__all(ok) || deadline(t0)) break;
                __builtin_amdgcn_s_sleep(1);
            }
#pragma unroll
            for (int cbk = 0; cbk < 2; ++cbk) {
                const float Pr = k1[cbk][2], Pi = k1[cbk][3];
                const float P2r = Pr * Pr - Pi * Pi, P2i = 2.0f * Pr * Pi, P4r = P2r * P2r - P2i * P2i, P4i = 2.0f * P2r * P2i;
                float hr = 0.f, hi = 0.f;
#pragma unroll
                for (int i = 3; i >= 0; --i) {
                    const bool valid = rb - 1 - q - 4 * i >= 0;
                    const float vr = valid ? __uint_as_float(gr[cbk][i][0]) : 0.f, vi = valid ? __uint_as_float(gr[cbk][i][2]) : 0.f;
                    const float t = fmaf(P4r, hr, fmaf(-P4i, hi, vr)); hi = fmaf(P4r, hi, fmaf(P4i, hr, vi)); hr = t;
                }
                const float ar = (q & 1) ? Pr : 1.0f, ai = (q & 1) ? Pi : 0.f, br = (q & 2) ? P2r : 1.0f, bi = (q & 2) ? P2i : 0.f;
                const float wr = ar * br - ai * bi, wi = ar * bi + ai * br;
                float tr = wr * hr - wi * hi, ti = wr * hi + wi * hr;
                tr += __shfl_xor(tr, 16); ti += __shfl_xor(ti, 16);
                tr += __shfl_xor(tr, 32); ti += __shfl_xor(ti, 32);
                Sr[cbk] = tr; Si[cbk] = ti;
            }
        }
        STAMP(6);
#pragma unroll
        for (int cbk = 0; cbk < 2; ++cbk) {
            float cr = Sr[cbk], ci = Si[cbk];
#pragma unroll
            for (int sgi = 0; sgi < 7; ++sgi) {
                const float t = fmaf(k1[cbk][0], cr, fmaf(-k1[cbk][1], ci, e[cbk][sgi][0])), u = fmaf(k1[cbk][0], ci, fmaf(k1[cbk][1], cr, e[cbk][sgi][1]));
                cr = sgi < seg ? t : cr; ci = sgi < seg ? u : ci;
            }
            float wr = k0[cbk][2] * cr - k0[cbk][3] * ci, wi = k0[cbk][2] * ci + k0[cbk][3] * cr;
            float hr[16], hi[16];
#pragma unroll
            for (int i = 0; i < 16; ++i) {
                const float t = k0[cbk][0] * wr - k0[cbk][1] * wi; wi = k0[cbk][0] * wi + k0[cbk][1] * wr; wr = t;
                const float g2 = 2.0f * gq[i >> 2][i & 3];
                hr[i] = fmaf(g2, wr, acc[cbk][i >> 2][i & 3]); hi[i] = -fmaf(g2, wi, acc[2 + cbk][i >> 2][i & 3]);
            }
            const int kb = 4 * wn + 2 * cbk + (ch >> 3);
            const unsigned vo = (unsigned)((kb * 8 + seg) * 256 + (((ch & 7) ^ (4 * (kb & 1))) * 16));
            const unsigned so = WS_A2 + (unsigned)(rb * 88 + 2 * cb) * 16384u;
#pragma unroll
            for (int th = 0; th < 2; ++th) {
                u32x4 vr, vi;
#pragma unroll
                for (int j = 0; j < 4; ++j) { vr[j] = pkbf(hr[th * 8 + 2 * j], hr[th * 8 + 2 * j + 1]); vi[j] = pkbf(hi[th * 8 + 2 * j], hi[th * 8 + 2 * j + 1]); }
                __builtin_amdgcn_raw_buffer_store_b128(vr, rs, vo + th * 128, so, 16);
                __builtin_amdgcn_raw_buffer_store_b128(vi, rs, vo + th * 128, so + 16384u, 16);
            }
        }
    }
    {
        f32x4 acc[7][4];
#pragma unroll
        for (int n = 0; n < 7; ++n)
#pragma unroll
            for (int m = 0; m < 4; ++m) acc[n][m] = (f32x4){0.f, 0.f, 0.f, 0.f};
        bf16x8 B0[7], B1[7];
        const LaneC c = lane_consts<PH_M1>(wm, wn, lane);
        STAMP(7);
        BAR();
        STAMP(8);
        load_frags<PH_M1, 0, 7>(smem, c, A0, B0);
        SCHED0();
        int sc = 0;
#define M1STEP() do { const int sn_ = sc == 2 ? 0 : sc + 1; cstep<PH_M1, PH_M1, false, 7>(smem + sc * SLOTM, smem + sn_ * SLOTM, c, c, acc, A0, B0, A1, B1); sc = sn_; } while (0)
_Pragma("unroll 1")
        for (int t = 0; t < 4; ++t) M1STEP();
        signal_add(cntS, lane);
_Pragma("unroll 1")
        for (int t = 4; t < 11; ++t) M1STEP();
        unsigned pv = 0;
        if (wid == 0) pv = __hip_atomic_load(cntS, RLX_AGENT);
_Pragma("unroll 1")
        for (int t = 11; t < 15; ++t) M1STEP();
        if (wid == 0) wait_count(cntS, pv, 32u);
        cstep<PH_M1, PH_M1, true, 7>(smem + sc * SLOTM, smem, c, c, acc, A0, B0, A1, B1);
        STAMP(9);
        if (wn == 0) m1_epilogue<0>(acc, smem, rs, rb, cb, wm, lane);
        else m1_epilogue<1>(acc, smem, rs, rb, cb, wm, lane);
    }
    {
        f32x4 acc[4][4];
#pragma unroll
        for (int n = 0; n < 4; ++n)
#pragma unroll
            for (int m = 0; m < 4; ++m) acc[n][m] = (f32x4){0.f, 0.f, 0.f, 0.f};
        bf16x8 B0[4], B1[4];
        const LaneC cs = lane_consts<PH_GS>(wm, wn, lane);
        STAMP(10);
        BAR();
        STAMP(11);
        load_frags<PH_GS, 0, 4>(smem, cs, A0, B0);
        SCHED0();
#define GSSTEP(t) cstep<PH_GS, PH_GS, false, 4>(smem + ((t) & 3) * SLOT, smem + (((t) + 1) & 3) * SLOT, cs, cs, acc, A0, B0, A1, B1)
_Pragma("unroll 1")
        for (int t = 0; t < 6; ++t) GSSTEP(t);
        signal_add(cntM, lane);
_Pragma("unroll 1")
        for (int t = 6; t < 9; ++t) GSSTEP(t);
        unsigned pv = 0;
        if (wid == 0) pv = __hip_atomic_load(cntM, RLX_AGENT);
_Pragma("unroll 1")
        for (int t = 9; t < 12; ++t) GSSTEP(t);
        if (wid == 0) wait_count(cntM, pv, 32u);
_Pragma("unroll 1")
        for (int t = 12; t < 15; ++t) GSSTEP(t);
        STAMP(12);
        const LaneC cm = lane_consts<PH_GM>(wm, wn, lane);
        cstep<PH_GS, PH_GM, false, 4>(smem + 3 * SLOT, smem, cs, cm, acc, A0, B0, A1, B1);
_Pragma("unroll 1")
        for (int t = 16; t < 43; ++t) cstep<PH_GM, PH_GM, false, 4>(smem + (t & 3) * SLOT, smem + ((t + 1) & 3) * SLOT, cm, cm, acc, A0, B0, A1, B1);
        cstep<PH_GM, PH_GM, true, 4>(smem + 3 * SLOT, smem, cm, cm, acc, A0, B0, A1, B1);

        STAMP(13);
        const int r = lane & 15, q = lane >> 4;
        if (kh) {
#pragma unroll
            for (int n = 0; n < 4; ++n)
#pragma unroll
                for (int mm = 0; mm < 2; ++mm) { const f32x4 t = acc[n][mm]; acc[n][mm] = acc[n][2 + mm]; acc[n][2 + mm] = t; }
        }
        const unsigned xo = WS_XB + (unsigned)(bid * 4 + wid) * 8192u, xi = WS_XB + (unsigned)((bid ^ 1) * 4 + wid) * 8192u;
#pragma unroll
        for (int n = 0; n < 4; ++n)
#pragma unroll
            for (int mm = 0; mm < 2; ++mm) {
                const f32x4 v = acc[n][2 + mm];
                __builtin_amdgcn_raw_buffer_store_b128(__builtin_bit_cast(u32x4, v), rs, lane * 16 + (n * 2 + mm) * 1024, xo, 16);
            }
        f32x4 o[4][2];
        const int t0r = rb * 128 + wm * 64 + kh * 32 + r, d0 = c8 * 128 + wn * 64 + q * 4;
#pragma unroll
        for (int mm = 0; mm < 2; ++mm) {
            const float g = __uint_as_float(__builtin_amdgcn_raw_buffer_load_b32(rs, (t0r + mm * 16) * 4, WS_G, 16));
#pragma unroll
            for (int n = 0; n < 4; ++n) {
                const u32x2 xw = *(const u32x2*)(a.x + (size_t)(t0r + mm * 16) * 1024 + d0 + n * 16);
                const u32x2 dw = *(const u32x2*)(a.dsk + d0 + n * 16), bw = *(const u32x2*)(a.b2 + d0 + n * 16);
                const f32x4 own = acc[n][mm];
                o[n][mm][0] = own[0] + g * (bflo(xw.x) * bflo(dw.x)) + (1.0f - g) * bflo(bw.x);
                o[n][mm][1] = own[1] + g * (bfhi(xw.x) * bfhi(dw.x)) + (1.0f - g) * bfhi(bw.x);
                o[n][mm][2] = own[2] + g * (bflo(xw.y) * bflo(dw.y)) + (1.0f - g) * bflo(bw.y);
                o[n][mm][3] = own[3] + g * (bfhi(xw.y) * bfhi(dw.y)) + (1.0f - g) * bfhi(bw.y);
            }
        }
        WAITV(0);
        STAMP(14);
        if (lane == 0) __hip_atomic_store((gu32*)(ws + WS_XFLAG) + (bid * 4 + wid) * 16, 1u, RLX_AGENT);
        {
            gu32* fl = (gu32*)(ws + WS_XFLAG) + ((bid ^ 1) * 4 + wid) * 16;
            wait_count(fl, __hip_atomic_load(fl, RLX_AGENT), 1u);
        }
        STAMP(15);
#pragma unroll
        for (int n = 0; n < 4; ++n)
#pragma unroll
            for (int mm = 0; mm < 2; ++mm) {
                const u32x4 v = __builtin_amdgcn_raw_buffer_load_b128(rs, lane * 16 + (n * 2 + mm) * 1024, xi, 16);
                o[n][mm] += __builtin_bit_cast(f32x4, v);
            }
#pragma unroll
        for (int mm = 0; mm < 2; ++mm)
#pragma unroll
            for (int n = 0; n < 4; ++n) *(f32x4*)(a.out + (size_t)(t0r + mm * 16) * 1024 + d0 + n * 16) = o[n][mm];
    }
}
}

extern "C" void kernel_launch(void* const* d_in, const int* in_sizes, int n_in,
                              void* d_out, int out_size, void* d_ws, size_t ws_size,
                              hipStream_t stream) {
    using namespace gm;
    Args a;
    a.x = (const u16*)d_in[0]; a.lre = (const u16*)d_in[1]; a.lim = (const u16*)d_in[2]; a.bre = (const u16*)d_in[3]; a.bim = (const u16*)d_in[4];
    a.cre = (const u16*)d_in[5]; a.cim = (const u16*)d_in[6]; a.ldt = (const u16*)d_in[7]; a.dsk = (const u16*)d_in[8];
    a.w1 = (const u16*)d_in[10]; a.b1 = (const u16*)d_in[11]; a.w2 = (const u16*)d_in[12]; a.b2 = (const u16*)d_in[13]; a.gw = (const u16*)d_in[14];
    a.out = (float*)d_out; a.ws = (char*)d_ws;
    (void)hipFuncSetAttribute((const void*)fused_kernel, hipFuncAttributeMaxDynamicSharedMemorySize, LDS_BYTES);
    (void)hipMemsetAsync(d_ws, 0, WS_SYNC_BYTES, stream);
    fused_kernel<<<256, 512, LDS_BYTES, stream>>>(a);
}
```

```cpp
#include <hip/hip_runtime.h>
#include <hip/hip_bf16.h>

namespace gm {
#define LAS __attribute__((address_space(3)))
#define GAS __attribute__((address_space(1)))
typedef unsigned short u16;
typedef short bf16x8 __attribute__((ext_vector_type(8)));
typedef short s16x4 __attribute__((ext_vector_type(4)));
typedef float f32x4 __attribute__((ext_vector_type(4)));
typedef float f32x2 __attribute__((ext_vector_type(2)));
typedef unsigned u32x4 __attribute__((ext_vector_type(4)));
typedef unsigned u32x2 __attribute__((ext_vector_type(2)));
typedef LAS char* ldsp;
typedef GAS unsigned gu32;
typedef unsigned long long u64;
typedef GAS u64 gu64;

#define BAR() __builtin_amdgcn_s_barrier()
#define WAITV(n) asm volatile("s_waitcnt vmcnt(%0)" ::"i"(n) : "memory")
#define SCHED0() __builtin_amdgcn_sched_barrier(0)
#define RLX_AGENT __ATOMIC_RELAXED, __HIP_MEMORY_SCOPE_AGENT

constexpr unsigned WS_EPOCH = 0;
constexpr unsigned WS_FY = 4096;
constexpr unsigned WS_FS = 16384;
constexpr unsigned WS_FM = 24576;
constexpr unsigned WS_XFLAG = 32768;
constexpr unsigned WS_GRAN = 131072;
constexpr unsigned WS_G = 1u << 20;
constexpr unsigned WS_Y = 2u << 20;
constexpr unsigned WS_A2 = 8u << 20;
constexpr unsigned WS_XB = 32u << 20;
constexpr unsigned WS_END = 40u << 20;

constexpr int SLOT = 32768, SLOTM = 45056;
constexpr int LDS_E = 135168;
constexpr int LDS_COEF = LDS_E + 4096;
constexpr int LDS_B1 = LDS_COEF + 2048;
constexpr int LDS_DUMMY = LDS_B1 + 512;
constexpr int LDS_BYTES = LDS_DUMMY + 256;

enum { PH_S1 = 0, PH_M1 = 1, PH_GS = 2, PH_GM = 3 };

struct Args {
    const u16 *x, *lre, *lim, *bre, *bim, *cre, *cim, *ldt, *dsk, *w1, *b1, *w2, *b2, *gw;
    float* out;
    char* ws;
};

__device__ __forceinline__ bf16x8 lds128(ldsp p) { return *(const LAS bf16x8*)p; }
__device__ __forceinline__ s16x4 ldstr(ldsp p) { return __builtin_amdgcn_ds_read_tr16_b64_v4i16((LAS s16x4*)p); }
__device__ __forceinline__ bf16x8 cat(s16x4 a, s16x4 b) { return __builtin_shufflevector(a, b, 0, 1, 2, 3, 4, 5, 6, 7); }
__device__ __forceinline__ void dma16(const void* g, ldsp l) { __builtin_amdgcn_global_load_lds((const unsigned*)g, (LAS unsigned*)l, 16, 0, 0); }
__device__ __forceinline__ void dma16c(const void* g, ldsp l) { __builtin_amdgcn_global_load_lds((const unsigned*)g, (LAS unsigned*)l, 16, 0, 16); }
__device__ __forceinline__ void st128(u32x4 v, __amdgpu_buffer_rsrc_t rs, unsigned voff, unsigned base, int) { __builtin_amdgcn_raw_buffer_store_b128(v, rs, voff + base, 0, 16); }
__device__ __forceinline__ void st64(u32x2 v, __amdgpu_buffer_rsrc_t rs, unsigned voff, unsigned base, int) { __builtin_amdgcn_raw_buffer_store_b64(v, rs, voff + base, 0, 16); }
__device__ __forceinline__ float bflo(unsigned w) { return __uint_as_float(w << 16); }
__device__ __forceinline__ float bfhi(unsigned w) { return __uint_as_float(w & 0xffff0000u); }
__device__ __forceinline__ float bf1(u16 b) { return __uint_as_float((unsigned)b << 16); }
__device__ __forceinline__ unsigned pkbf(float lo, float hi) {
    typedef __bf16 bf2_t __attribute__((ext_vector_type(2)));
    bf2_t v = {(__bf16)lo, (__bf16)hi};
    return __builtin_bit_cast(unsigned, v);
}
__device__ __forceinline__ bool deadline(unsigned long long t0) { return __builtin_amdgcn_s_memrealtime() - t0 > 400000ull; }

struct LaneC { unsigned a0, a1, b0, b1; };
template <int PH>
__device__ __forceinline__ LaneC lane_consts(int wm, int wn, int lane) {
    const int r = lane & 15, q = lane >> 4, rr = r >> 2, p = lane & 3;
    const unsigned rl = r * 128 + ((q ^ (r >> 1)) * 16);
    LaneC c;
    if constexpr (PH == PH_S1) { c.a0 = wm * 8192 + rl; c.a1 = c.a0 ^ 64; c.b0 = 16384 + wn * 8192 + rl; c.b1 = c.b0 ^ 64; }
    if constexpr (PH == PH_M1) {
        c.a0 = wm * 8192 + rl; c.a1 = c.a0 ^ 64;
        const unsigned t0 = 16384 + q * 7 * 512 + rr * 64 + ((q & 1) * 32) + p * 8;
        c.b0 = wn ? (t0 ^ 32) + 3 * 512 : t0;
        c.b1 = wn ? t0 + 4 * 512 : (t0 ^ 32);
    }
    if constexpr (PH == PH_GS) {
        c.a0 = (q * 8 + wm * 4) * 256 + (p >> 1) * 128 + (4 * (q & 1) + rr) * 16 + (p & 1) * 8; c.a1 = c.a0 ^ 64;
        c.b0 = 16384 + wn * 8192 + rl; c.b1 = c.b0 ^ 64;
    }
    if constexpr (PH == PH_GM) {
        c.a0 = wm * 8192 + lane * 16; c.a1 = c.a0;
        c.b0 = 16384 + q * 4 * 512 + wn * 1024 + rr * 64 + ((q & 1) * 32) + p * 8; c.b1 = c.b0 ^ 32;
    }
    return c;
}
template <int PH, int KK, int NB>
__device__ __forceinline__ void load_frags(ldsp slot, const LaneC& c, bf16x8 (&A)[4], bf16x8 (&B)[NB]) {
    if constexpr (PH == PH_S1 || PH == PH_M1) {
        ldsp pa = slot + (KK ? c.a1 : c.a0);
#pragma unroll
        for (int m = 0; m < 4; ++m) A[m] = lds128(pa + m * 2048);
    } else if constexpr (PH == PH_GS) {
        ldsp p0 = slot + c.a0, p1 = slot + c.a1;
#pragma unroll
        for (int m = 0; m < 4; ++m) A[m] = cat(ldstr(p0 + (KK * 32 + m) * 256), ldstr(p1 + (KK * 32 + m) * 256));
    } else {
        ldsp pa = slot + c.a0;
#pragma unroll
        for (int m = 0; m < 4; ++m) A[m] = lds128(pa + (m * 2 + KK) * 1024);
    }
    if constexpr (PH == PH_S1 || PH == PH_GS) {
        ldsp pb = slot + (KK ? c.b1 : c.b0);
#pragma unroll
        for (int n = 0; n < NB; ++n) B[n] = lds128(pb + n * 2048);
    } else {
        constexpr int NS2 = PH == PH_M1 ? 7 : 4;
        ldsp p0 = slot + c.b0, p1 = slot + c.b1;
#pragma unroll
        for (int n = 0; n < NB; ++n) { ldsp p = ((n & 1) ? p1 : p0) + KK * 4 * NS2 * 512 + (n >> 1) * 512; B[n] = cat(ldstr(p), ldstr(p + 256)); }
    }
}
template <int PH, int NB>
__device__ __forceinline__ void mma(f32x4 (&acc)[NB][4], const bf16x8 (&A)[4], const bf16x8 (&B)[NB]) {
#pragma unroll
    for (int n = 0; n < NB; ++n)
#pragma unroll
        for (int m = 0; m < 4; ++m) {
            if constexpr (PH == PH_S1) acc[n][m] = __builtin_amdgcn_mfma_f32_16x16x32_bf16(A[m], B[n], acc[n][m], 0, 0, 0);
            else acc[n][m] = __builtin_amdgcn_mfma_f32_16x16x32_bf16(B[n], A[m], acc[n][m], 0, 0, 0);
        }
}
template <int PH> constexpr int nreads() { return PH == PH_S1 ? 8 : PH == PH_M1 ? 18 : 12; }
template <int NR, int NM>
__device__ __forceinline__ void sched_half() {
#pragma unroll
    for (int i = 0; i < NR; ++i) { __builtin_amdgcn_sched_group_barrier(0x8, 1, 0); __builtin_amdgcn_sched_group_barrier(0x100, 1, 0); }
    __builtin_amdgcn_sched_group_barrier(0x8, NM - NR, 0);
}
template <int PC, int PN, bool LAST, int NB>
__device__ __forceinline__ void cstep(ldsp cur, ldsp nxt, const LaneC& cc, const LaneC& cn, f32x4 (&acc)[NB][4], bf16x8 (&A0)[4], bf16x8 (&B0)[NB], bf16x8 (&A1)[4], bf16x8 (&B1)[NB]) {
    load_frags<PC, 1, NB>(cur, cc, A1, B1);
    mma<PC, NB>(acc, A0, B0);
    sched_half<nreads<PC>(), 4 * NB>();
    SCHED0();
    __builtin_amdgcn_s_waitcnt(0xC07F);
    BAR();
    SCHED0();
    if constexpr (!LAST) load_frags<PN, 0, NB>(nxt, cn, A0, B0);
    mma<PC, NB>(acc, A1, B1);
    if constexpr (!LAST) sched_half<nreads<PN>(), 4 * NB>();
    SCHED0();
}

__device__ __forceinline__ u64 next_tag(u64 e) { return e * 0x9E3779B97F4A7C15ull + 0xD1B54A32D192ED03ull; }
__device__ __forceinline__ void wait_flags(gu64* fl, u64 v, u64 tag) {
    if (!__all(v == tag)) {
        const unsigned long long t0 = __builtin_amdgcn_s_memrealtime();
        do { __builtin_amdgcn_s_sleep(2); v = __hip_atomic_load(fl, RLX_AGENT); } while (!__all(v == tag) && !deadline(t0));
    }
    asm volatile("" ::: "memory");
}
__device__ __forceinline__ void signal_flag(gu64* fl, u64 tag, int lane) {
    WAITV(0);
    if (lane == 0) __hip_atomic_store(fl, tag, RLX_AGENT);
}
__device__ __forceinline__ void warm(const void* g, ldsp smem) { __builtin_amdgcn_global_load_lds((const unsigned*)g, (LAS unsigned*)(smem + LDS_DUMMY), 4, 0, 0); }
template <int CTRL, int RMASK> __device__ __forceinline__ float dpp_add(float v) {
    return v + __builtin_bit_cast(float, __builtin_amdgcn_update_dpp(0, __builtin_bit_cast(int, v), CTRL, RMASK, 0xf, true));
}
__device__ __forceinline__ float wave_sum(float v) {
    v = dpp_add<0x111, 0xf>(v); v = dpp_add<0x112, 0xf>(v); v = dpp_add<0x114, 0xf>(v); v = dpp_add<0x118, 0xf>(v);
    v = dpp_add<0x142, 0xa>(v); v = dpp_add<0x143, 0xc>(v);
    return __builtin_bit_cast(float, __builtin_amdgcn_readlane(__builtin_bit_cast(int, v), 63));
}
#define STAMP(i) do { } while (0)

__device__ __forceinline__ unsigned gelu2(f32x2 v, float og) {
    const f32x2 t = (v * v) * -0.1029432397f + -2.3022081986f;
    const f32x2 u = v * t;
    f32x2 d; d.x = __builtin_amdgcn_exp2f(u.x); d.y = __builtin_amdgcn_exp2f(u.y);
    d = d + 1.0f;
    f32x2 r; r.x = __builtin_amdgcn_rcpf(d.x); r.y = __builtin_amdgcn_rcpf(d.y);
    const f32x2 o = (v * og) * r;
    return pkbf(o.x, o.y);
}

template <int WN>
__device__ __forceinline__ void m1_epilogue(f32x4 (&acc)[7][4], const float (&og)[4], ldsp smem, __amdgpu_buffer_rsrc_t rs, int rb, int cb, int wm, int lane) {
    const int q = lane >> 4;
    u32x2 pk[7][4];
#pragma unroll
    for (int n = 0; n < 7; ++n) {
        const u32x2 bw = *(const LAS u32x2*)(smem + LDS_B1 + (WN * 112 + n * 16 + q * 4) * 2);
        const f32x2 b01 = {bflo(bw.x), bfhi(bw.x)}, b23 = {bflo(bw.y), bfhi(bw.y)};
#pragma unroll
        for (int m = 0; m < 4; ++m) {
            const f32x4 a = acc[n][m];
            pk[n][m].x = gelu2((f32x2){a[0], a[1]} + b01, og[m]);
            pk[n][m].y = gelu2((f32x2){a[2], a[3]} + b23, og[m]);
        }
        const int nbt = 7 * WN + n;
        if ((nbt & 1) == 1 || (WN == 0 && n == 6)) {
            const int pair = 7 * cb + (nbt >> 1);
            const unsigned base = WS_A2 + (unsigned)(rb * 88 + 32 + (pair >> 1)) * 16384u + (unsigned)(pair & 1) * 1024u + (unsigned)(wm * 4) * 2048u;
#pragma unroll
            for (int m = 0; m < 4; ++m) {
                if (WN == 0 && n == 6) st64(pk[n][m], rs, lane * 16 + m * 2048, base, 16);
                else if (WN == 1 && n == 0) st64(pk[n][m], rs, lane * 16 + 8 + m * 2048, base, 16);
                else { const u32x4 v = {pk[n > 0 ? n - 1 : 0][m].x, pk[n > 0 ? n - 1 : 0][m].y, pk[n][m].x, pk[n][m].y};
                       st128(v, rs, lane * 16 + m * 2048, base, 16); }
            }
        }
    }
}

__global__ __launch_bounds__(512, 2) void fused_kernel(Args a) {
    extern __shared__ __attribute__((aligned(16))) char smem_[];
    ldsp smem = (ldsp)smem_;
    const int wid = __builtin_amdgcn_readfirstlane(threadIdx.x >> 6), lane = threadIdx.x & 63;
    const int bid = blockIdx.x, xcd = bid & 7, rk = bid >> 3;
    const int kh = xcd & 1, rb = (xcd >> 1) * 4 + (rk >> 3), c8 = rk & 7, cb = kh * 8 + c8;
    char* const ws = a.ws;
    asm volatile("" ::"s"(a.x), "s"(a.lre), "s"(a.lim), "s"(a.bre), "s"(a.bim), "s"(a.cre), "s"(a.cim), "s"(a.ldt), "s"(a.dsk), "s"(a.w1), "s"(a.b1), "s"(a.w2), "s"(a.b2), "s"(a.gw), "s"(a.out), "s"(a.ws));

    if (wid >= 4) {
        const int w = wid - 4, un = (xcd >> 1) * 32 + rk;
        const unsigned swz = (unsigned)(((lane & 7) ^ ((4 * (w & 1) + (lane >> 4)) & 7)) * 16);
        {
            const unsigned va = (unsigned)((rb * 128 + 16 * (lane >> 5) + ((lane >> 3) & 3) + 32 * (w & 1) + 4 * (w >> 1)) * 2048) + swz;
            const unsigned vb = (unsigned)((cb * 64 + 16 * (w >> 1) + 8 * (w & 1) + (lane >> 3)) * 2048) + swz;
            auto issue = [&](int t) {
                ldsp s = smem + (t & 3) * SLOT + w * 1024;
                const char* pa = (const char*)a.x + t * 128;
#pragma unroll
                for (int n = 0; n < 4; ++n) dma16(pa + (64 * (n >> 1) + 8 * (n & 1)) * 2048 + va, s + n * 4096);
#pragma unroll
                for (int n = 0; n < 4; ++n) dma16((const char*)((n & 1) ? a.bim : a.bre) + t * 128 + (n >> 1) * 32 * 2048 + vb, s + 16384 + n * 4096);
            };
            unsigned plr = 0, pli = 0, pld = 0;
            if (w == 3) {
                const u16 *p0 = a.lre + cb * 64 + lane, *p1 = a.lim + cb * 64 + lane, *p2 = a.ldt + cb * 64 + lane;
                asm volatile("global_load_ushort %0, %3, off\n\tglobal_load_ushort %1, %4, off\n\tglobal_load_ushort %2, %5, off"
                             : "+v"(plr), "+v"(pli), "+v"(pld) : "v"(p0), "v"(p1), "v"(p2) : "memory");
            }
            if (w == 2) {
                const char* pb = (const char*)a.b1 + cb * 448;
                __builtin_amdgcn_global_load_lds((const unsigned*)(pb + lane * 4), (LAS unsigned*)(smem + LDS_B1), 4, 0, 0);
                __builtin_amdgcn_global_load_lds((const unsigned*)(pb + (lane < 48 ? 64 + lane : 111) * 4), (LAS unsigned*)(smem + LDS_B1 + 256), 4, 0, 0);
            }
_Pragma("unroll 1")
            for (int t = 0; t < 4; ++t) issue(t);
            {
                const char* pB = (const char*)(((lane >> 4) & 1) ? a.bim : a.bre) + (512 * kh + 16 * rk + (lane & 15)) * 2048 + (4 + (lane >> 5)) * 128;
                const char* pX = (const char*)a.x + (512 * (xcd >> 1) + 256 * (1 - kh) + 8 * rk + (lane & 7)) * 2048;
                warm(w == 2 ? pX + (4 + (lane >> 3)) * 128 : pB + (w == 3 ? 2 : w) * 256, smem);
                warm(w == 3 ? pX + (12 + ((lane >> 3) & 3)) * 128 : pB + (3 + w) * 256, smem);
            }
            if (w == 3) {
                asm volatile("s_waitcnt vmcnt(34)" : "+v"(plr), "+v"(pli), "+v"(pld)::"memory");
                const float lr = bflo(plr), li = bflo(pli);
                const float dt = (float)(__bf16)expf(bflo(pld));
                const float zr = dt * lr, zi = dt * li;
                float sn, cs; sincosf(zi, &sn, &cs);
                const float er = expf(zr), Lr = er * cs, Li = er * sn;
                const float den = 1.0f / (lr * lr + li * li), nr = Lr - 1.0f;
                const float cr = (nr * lr + Li * li) * den, ci = (Li * lr - nr * li) * den;
                float pr = Lr, pi = Li;
#pragma unroll
                for (int i = 0; i < 4; ++i) { const float t = pr * pr - pi * pi; pi = 2.0f * pr * pi; pr = t; }
                const float p16r = pr, p16i = pi;
#pragma unroll
                for (int i = 0; i < 3; ++i) { const float t = pr * pr - pi * pi; pi = 2.0f * pr * pi; pr = t; }
                const f32x4 v0 = {Lr, Li, cr, ci}, v1 = {p16r, p16i, pr, pi};
                const unsigned la = (unsigned)(size_t)(smem + LDS_COEF) + lane * 32;
                asm volatile("ds_write_b128 %0, %1\n\tds_write_b128 %0, %2 offset:16\n\ts_waitcnt lgkmcnt(0)" ::"v"(la), "v"(v0), "v"(v1) : "memory");
            }
            WAITV(26); BAR();
_Pragma("unroll 1")
            for (int t = 0; t < 3; ++t) { WAITV(18); BAR(); issue(t + 4); }
_Pragma("unroll 1")
            for (int t = 3; t < 12; ++t) { WAITV(16); BAR(); issue(t + 4); }
            warm((const char*)a.w1 + (size_t)(8 * un + 2 * w + (lane >= 28 ? 1 : 0)) * 7168 + kh * 3584 + (lane >= 56 ? 27 : lane >= 28 ? lane - 28 : lane) * 128, smem);
            WAITV(17); BAR();
            WAITV(9); BAR();
            WAITV(1); BAR();
            BAR();
        }
        {
            const unsigned va = (unsigned)((rb * 128 + 8 * w + (lane >> 3)) * 2048) + swz;
            unsigned vb[7];
#pragma unroll
            for (int i = 0; i < 7; ++i) {
                const int sl = 2 * i + (lane >> 5), kq = 2 * w + (sl >= 7 ? 1 : 0), nb2 = sl >= 7 ? sl - 7 : sl;
                vb[i] = (unsigned)((8 * kq + ((lane >> 2) & 7)) * 7168 + cb * 448 + nb2 * 64 + (((lane & 3) ^ (2 * (kq & 1))) * 16));
            }
            int slot = 0;
            auto issue = [&](int t) {
                ldsp s = smem + slot * SLOTM;
                slot = slot == 2 ? 0 : slot + 1;
                const char* pa = ws + WS_Y + t * 128;
#pragma unroll
                for (int n = 0; n < 4; ++n) dma16c(pa + n * 32 * 2048 + va, s + w * 1024 + n * 4096);
                const char* pb = (const char*)a.w1 + (size_t)t * 64 * 7168;
#pragma unroll
                for (int i = 0; i < 7; ++i) dma16(pb + vb[i], s + 16384 + (w * 7 + i) * 1024);
            };
            issue(0);
            BAR();
            BAR();
_Pragma("unroll 1")
            for (int t = 1; t < 3; ++t) issue(t);
            WAITV(22); BAR();
_Pragma("unroll 1")
            for (int t = 0; t < 13; ++t) { WAITV(11); BAR(); issue(t + 3); }
            warm((const char*)(((lane >> 3) & 1) ? a.cim : a.cre) + (8 * un + 2 * w + ((lane >> 4) & 1)) * 2048 + kh * 1024 + (lane & 7) * 128, smem);
            warm((const char*)a.w2 + (size_t)(1792 * kh + 14 * un) * 2048 + (w * 56 + (lane < 56 ? lane : 55)) * 128, smem);
            WAITV(13); BAR();
            WAITV(2); BAR();
            BAR();
        }
        {
            const char* a2 = ws + WS_A2 + (size_t)rb * 88 * 16384 + w * 4096 + lane * 16;
            const unsigned vr = (unsigned)((c8 * 128 + 8 * w + (lane >> 3)) * 2048) + swz;
            const int rho = (lane >> 2) & 7;
            unsigned vt[2];
#pragma unroll
            for (int qq = 0; qq < 2; ++qq) { const int q = 2 * (w & 1) + qq; const int k = 32 * (w >> 1) + (rho < 4 ? 4 * q + rho : 16 + 4 * q + rho - 4);
                vt[qq] = (unsigned)(k * 2048 + c8 * 256 + (lane >> 5) * 64 + (((lane & 3) ^ (2 * qq)) * 16)); }
            auto issue = [&](int t) {
                ldsp s = smem + (t & 3) * SLOT;
                if (t < 16) {
                    const int ks = kh * 16 + t;
                    const char* pa = a2 + (size_t)ks * 16384;
#pragma unroll
                    for (int n = 0; n < 4; ++n) dma16c(pa + n * 1024, s + w * 4096 + n * 1024);
                    const char* pb = (const char*)((ks & 1) ? a.cim : a.cre) + (ks >> 1) * 128;
#pragma unroll
                    for (int n = 0; n < 4; ++n) dma16(pb + n * 32 * 2048 + vr, s + 16384 + (w + 4 * n) * 1024);
                } else {
                    const int km = kh * 28 + (t - 16);
                    const char* pa = a2 + (size_t)(32 + km) * 16384;
#pragma unroll
                    for (int n = 0; n < 4; ++n) dma16c(pa + n * 1024, s + w * 4096 + n * 1024);
                    const char* pb = (const char*)a.w2 + (size_t)km * 64 * 2048;
#pragma unroll
                    for (int n = 0; n < 4; ++n) dma16(pb + (n & 1) * 128 + vt[n >> 1], s + 16384 + (w * 4 + n) * 1024);
                }
            };
_Pragma("unroll 1")
            for (int t = 0; t < 4; ++t) issue(t);
            WAITV(24); BAR();
_Pragma("unroll 1")
            for (int t = 0; t < 40; ++t) { WAITV(16); BAR(); issue(t + 4); }
            WAITV(16); BAR();
            WAITV(8); BAR();
            WAITV(0); BAR();
            BAR();
        }
        if (bid == 0 && w == 0) {
            const u64 tag = next_tag(__hip_atomic_load((gu64*)(ws + WS_EPOCH), RLX_AGENT));
            const unsigned long long t0 = __builtin_amdgcn_s_memrealtime();
            for (;;) {
                bool ok = true;
#pragma unroll
                for (int i = 0; i < 16; ++i) ok &= __hip_atomic_load((gu64*)(ws + WS_FY) + i * 64 + lane, RLX_AGENT) == tag;
                if (__all(ok) || deadline(t0)) break;
                __builtin_amdgcn_s_sleep(2);
            }
            if (lane == 0) __hip_atomic_store((gu64*)(ws + WS_EPOCH), tag, RLX_AGENT);
        }
        return;
    }

    const int wm = wid >> 1, wn = wid & 1;
    const __amdgpu_buffer_rsrc_t rs = __builtin_amdgcn_make_buffer_rsrc(ws, 0, WS_END, 0x00020000);
    gu64* const fS = (gu64*)(ws + WS_FS) + (rb * 2 + kh) * 32;
    gu64* const fM = (gu64*)(ws + WS_FM) + (rb * 2 + kh) * 32;
    STAMP(0);
    u64 tag;
    const int lnrow = 512 * (xcd >> 1) + 256 * kh + 8 * rk + 2 * wid;
    {
        u32x4 xv[2][2], gv[2];
#pragma unroll
        for (int i = 0; i < 2; ++i)
#pragma unroll
            for (int h = 0; h < 2; ++h) xv[i][h] = *(const u32x4*)(a.x + (size_t)(lnrow + i) * 1024 + h * 512 + lane * 8);
#pragma unroll
        for (int h = 0; h < 2; ++h) gv[h] = *(const u32x4*)(a.gw + h * 512 + lane * 8);
        const u64 ep = __hip_atomic_load((gu64*)(ws + WS_EPOCH), RLX_AGENT);
        f32x2 xf[2][8];
        float s[2], gd[2];
#pragma unroll
        for (int i = 0; i < 2; ++i) {
            f32x2 s2 = {0.f, 0.f}, g2 = {0.f, 0.f};
#pragma unroll
            for (int h = 0; h < 2; ++h)
#pragma unroll
                for (int j = 0; j < 4; ++j) {
                    const f32x2 v = {bflo(xv[i][h][j]), bfhi(xv[i][h][j])}, gw = {bflo(gv[h][j]), bfhi(gv[h][j])};
                    xf[i][h * 4 + j] = v; s2 += v; g2 += v * gw;
                }
            s[i] = wave_sum(s2.x + s2.y) * (1.0f / 1024.0f); gd[i] = wave_sum(g2.x + g2.y);
        }
#pragma unroll
        for (int i = 0; i < 2; ++i) {
            f32x2 v2 = {0.f, 0.f};
#pragma unroll
            for (int j = 0; j < 8; ++j) { xf[i][j] -= s[i]; v2 += xf[i][j] * xf[i][j]; }
            const float rstd = 1.0f / sqrtf(wave_sum(v2.x + v2.y) * (1.0f / 1024.0f) + 1e-5f);
#pragma unroll
            for (int h = 0; h < 2; ++h) {
                u32x4 o;
#pragma unroll
                for (int j = 0; j < 4; ++j) { const f32x2 v = xf[i][h * 4 + j] * rstd; o[j] = pkbf(v.x, v.y); }
                st128(o, rs, lane * 16 + h * 1024, WS_Y + (unsigned)(lnrow + i) * 2048u, 16);
            }
        }
        const float gl = lane ? gd[1] : gd[0];
        const float g = 1.0f / (1.0f + expf(-gl));
        if (lane < 2) __hip_atomic_store((gu32*)(ws + WS_G) + lnrow + lane, __float_as_uint(g), RLX_AGENT);
        tag = next_tag(((u64)(unsigned)__builtin_amdgcn_readfirstlane((unsigned)(ep >> 32)) << 32) | (unsigned)__builtin_amdgcn_readfirstlane((unsigned)ep));
    }
    const unsigned tag_lo = (unsigned)tag, tag_hi = (unsigned)(tag >> 32);
    bf16x8 A0[4], A1[4];
    {
        f32x4 acc[4][4];
#pragma unroll
        for (int n = 0; n < 4; ++n)
#pragma unroll
            for (int m = 0; m < 4; ++m) acc[n][m] = (f32x4){0.f, 0.f, 0.f, 0.f};
        bf16x8 B0[4], B1[4];
        const LaneC c = lane_consts<PH_S1>(wm, wn, lane);
        __builtin_amdgcn_s_waitcnt(0xC07F);
        STAMP(1);
        BAR();
        STAMP(2);
        load_frags<PH_S1, 0, 4>(smem, c, A0, B0);
        SCHED0();
#define S1STEP(t) cstep<PH_S1, PH_S1, false, 4>(smem + ((t) & 3) * SLOT, smem + (((t) + 1) & 3) * SLOT, c, c, acc, A0, B0, A1, B1)
_Pragma("unroll 1")
        for (int t = 0; t < 4; ++t) S1STEP(t);
        signal_flag((gu64*)(ws + WS_FY) + (lnrow >> 7) * 64 + (rk & 15) * 4 + wid, tag, lane);
_Pragma("unroll 1")
        for (int t = 4; t < 10; ++t) S1STEP(t);
        gu64* const fY = (gu64*)(ws + WS_FY) + rb * 64 + lane;
        const u64 pv = __hip_atomic_load(fY, RLX_AGENT);
_Pragma("unroll 1")
        for (int t = 10; t < 13; ++t) S1STEP(t);
        wait_flags(fY, pv, tag);
        const int ch = lane & 15, q = lane >> 4, seg = wm * 4 + q;
        f32x4 gq[4];
#pragma unroll
        for (int m = 0; m < 4; ++m) gq[m] = __builtin_bit_cast(f32x4, __builtin_amdgcn_raw_buffer_load_b128(rs, (seg * 16 + m * 4) * 4, WS_G + rb * 512, 16));
_Pragma("unroll 1")
        for (int t = 13; t < 15; ++t) S1STEP(t);
        cstep<PH_S1, PH_S1, true, 4>(smem + 3 * SLOT, smem, c, c, acc, A0, B0, A1, B1);

        STAMP(3);
        f32x4 k0[2], k1[2];
#pragma unroll
        for (int cbk = 0; cbk < 2; ++cbk) {
            const int cl = wn * 32 + cbk * 16 + ch;
            k0[cbk] = *(const LAS f32x4*)(smem + LDS_COEF + cl * 32); k1[cbk] = *(const LAS f32x4*)(smem + LDS_COEF + cl * 32 + 16);
        }
#pragma unroll
        for (int i = 1; i < 16; ++i)
#pragma unroll
            for (int cbk = 0; cbk < 2; ++cbk) {
                const float sr = acc[cbk][(i - 1) >> 2][(i - 1) & 3], si = acc[2 + cbk][(i - 1) >> 2][(i - 1) & 3];
                acc[cbk][i >> 2][i & 3] = fmaf(k0[cbk][0], sr, fmaf(-k0[cbk][1], si, acc[cbk][i >> 2][i & 3]));
                acc[2 + cbk][i >> 2][i & 3] = fmaf(k0[cbk][0], si, fmaf(k0[cbk][1], sr, acc[2 + cbk][i >> 2][i & 3]));
            }
#pragma unroll
        for (int cbk = 0; cbk < 2; ++cbk)
            *(LAS f32x2*)(smem + LDS_E + (seg * 64 + wn * 32 + cbk * 16 + ch) * 8) = (f32x2){acc[cbk][3][3], acc[2 + cbk][3][3]};
        __builtin_amdgcn_s_waitcnt(0xC07F);
        BAR();
        STAMP(4);
        asm volatile("" ::: "memory");
        f32x2 e[2][8];
#pragma unroll
        for (int cbk = 0; cbk < 2; ++cbk)
#pragma unroll
            for (int sgi = 0; sgi < 8; ++sgi) e[cbk][sgi] = *(const LAS f32x2*)(smem + LDS_E + (sgi * 64 + wn * 32 + cbk * 16 + ch) * 8);
        if (wm == 0 && rb < 15) {
            float er[2] = {0.f, 0.f}, ei[2] = {0.f, 0.f};
#pragma unroll
            for (int sgi = 0; sgi < 8; ++sgi)
#pragma unroll
                for (int cbk = 0; cbk < 2; ++cbk) {
                    const float t = fmaf(k1[cbk][0], er[cbk], fmaf(-k1[cbk][1], ei[cbk], e[cbk][sgi][0]));
                    ei[cbk] = fmaf(k1[cbk][0], ei[cbk], fmaf(k1[cbk][1], er[cbk], e[cbk][sgi][1])); er[cbk] = t; }
            const float sr_ = q ? er[1] + 0.f : er[0] + 0.f, si_ = q ? ei[1] + 0.f : ei[0] + 0.f;
            if (q < 2) { const u32x4 v = {__float_as_uint(sr_), tag_lo, __float_as_uint(si_), tag_hi};
                st128(v, rs, (wn * 32 + q * 16 + ch) * 16, WS_GRAN + (unsigned)(cb * 16 + rb) * 1024u, 16); }
        }
#pragma unroll
        for (int cbk = 0; cbk < 2; ++cbk)
#pragma unroll
            for (int i = 0; i < 16; ++i) {
                const float g2 = 2.0f * gq[i >> 2][i & 3], sr = acc[cbk][i >> 2][i & 3], si = acc[2 + cbk][i >> 2][i & 3];
                acc[cbk][i >> 2][i & 3] = g2 * fmaf(k0[cbk][2], sr, -k0[cbk][3] * si);
                acc[2 + cbk][i >> 2][i & 3] = g2 * fmaf(k0[cbk][2], si, k0[cbk][3] * sr);
            }
        STAMP(5);
        float Sr[2] = {0.f, 0.f}, Si[2] = {0.f, 0.f};
        if (rb > 0) {
            u32x4 gr[2][4];
            const unsigned long long t0 = __builtin_amdgcn_s_memrealtime();
            for (;;) {
                bool ok = true;
                asm volatile("" ::: "memory");
#pragma unroll
                for (int i = 0; i < 4; ++i) {
                    const int rp = rb - 1 - q - 4 * i, rc = rp < 0 ? 0 : rp;
#pragma unroll
                    for (int cbk = 0; cbk < 2; ++cbk) {
                        gr[cbk][i] = __builtin_amdgcn_raw_buffer_load_b128(rs, (wn * 32 + cbk * 16 + ch) * 16 + rc * 1024, WS_GRAN + (unsigned)cb * 16384u, 16);
                        ok &= (gr[cbk][i][1] == tag_lo) & (gr[cbk][i][3] == tag_hi);
                    }
                }
                if (__all(ok) || deadline(t0)) break;
                __builtin_amdgcn_s_sleep(1);
            }
#pragma unroll
            for (int cbk = 0; cbk < 2; ++cbk) {
                const float Pr = k1[cbk][2], Pi = k1[cbk][3];
                const float P2r = Pr * Pr - Pi * Pi, P2i = 2.0f * Pr * Pi, P4r = P2r * P2r - P2i * P2i, P4i = 2.0f * P2r * P2i;
                float hr = 0.f, hi = 0.f;
#pragma unroll
                for (int i = 3; i >= 0; --i) {
                    const bool valid = rb - 1 - q - 4 * i >= 0;
                    const float vr = valid ? __uint_as_float(gr[cbk][i][0]) : 0.f, vi = valid ? __uint_as_float(gr[cbk][i][2]) : 0.f;
                    const float t = fmaf(P4r, hr, fmaf(-P4i, hi, vr)); hi = fmaf(P4r, hi, fmaf(P4i, hr, vi)); hr = t;
                }
                const float ar = (q & 1) ? Pr : 1.0f, ai = (q & 1) ? Pi : 0.f, br = (q & 2) ? P2r : 1.0f, bi = (q & 2) ? P2i : 0.f;
                const float wr = ar * br - ai * bi, wi = ar * bi + ai * br;
                float tr = wr * hr - wi * hi, ti = wr * hi + wi * hr;
                tr += __shfl_xor(tr, 16); ti += __shfl_xor(ti, 16);
                tr += __shfl_xor(tr, 32); ti += __shfl_xor(ti, 32);
                Sr[cbk] = tr; Si[cbk] = ti;
            }
        }
        BAR();
        STAMP(6);
#pragma unroll
        for (int cbk = 0; cbk < 2; ++cbk) {
            float cr = Sr[cbk], ci = Si[cbk];
#pragma unroll
            for (int sgi = 0; sgi < 7; ++sgi) {
                const float t = fmaf(k1[cbk][0], cr, fmaf(-k1[cbk][1], ci, e[cbk][sgi][0])), u = fmaf(k1[cbk][0], ci, fmaf(k1[cbk][1], cr, e[cbk][sgi][1]));
                cr = sgi < seg ? t : cr; ci = sgi < seg ? u : ci;
            }
            float wr = k0[cbk][2] * cr - k0[cbk][3] * ci, wi = k0[cbk][2] * ci + k0[cbk][3] * cr;
            float hr[16], hi[16];
#pragma unroll
            for (int i = 0; i < 16; ++i) {
                const float t = k0[cbk][0] * wr - k0[cbk][1] * wi; wi = k0[cbk][0] * wi + k0[cbk][1] * wr; wr = t;
                const float g2 = 2.0f * gq[i >> 2][i & 3];
                hr[i] = fmaf(g2, wr, acc[cbk][i >> 2][i & 3]); hi[i] = -fmaf(g2, wi, acc[2 + cbk][i >> 2][i & 3]);
            }
            const int kb = 4 * wn + 2 * cbk + (ch >> 3);
            const unsigned vo = (unsigned)((kb * 8 + seg) * 256 + (((ch & 7) ^ (4 * (kb & 1))) * 16));
            const unsigned so = WS_A2 + (unsigned)(rb * 88 + 2 * cb) * 16384u;
#pragma unroll
            for (int th = 0; th < 2; ++th) {
                u32x4 vr, vi;
#pragma unroll
                for (int j = 0; j < 4; ++j) { vr[j] = pkbf(hr[th * 8 + 2 * j], hr[th * 8 + 2 * j + 1]); vi[j] = pkbf(hi[th * 8 + 2 * j], hi[th * 8 + 2 * j + 1]); }
                st128(vr, rs, vo + th * 128, so, 16);
                st128(vi, rs, vo + th * 128, so + 16384u, 16);
            }
        }
    }
    {
        f32x4 acc[7][4];
#pragma unroll
        for (int n = 0; n < 7; ++n)
#pragma unroll
            for (int m = 0; m < 4; ++m) acc[n][m] = (f32x4){0.f, 0.f, 0.f, 0.f};
        bf16x8 B0[7], B1[7];
        const LaneC c = lane_consts<PH_M1>(wm, wn, lane);
        STAMP(7);
        BAR();
        STAMP(8);
        load_frags<PH_M1, 0, 7>(smem, c, A0, B0);
        SCHED0();
        int sc = 0;
#define M1STEP() do { const int sn_ = sc == 2 ? 0 : sc + 1; cstep<PH_M1, PH_M1, false, 7>(smem + sc * SLOTM, smem + sn_ * SLOTM, c, c, acc, A0, B0, A1, B1); sc = sn_; } while (0)
_Pragma("unroll 1")
        for (int t = 0; t < 4; ++t) M1STEP();
        signal_flag(fS + c8 * 4 + wid, tag, lane);
_Pragma("unroll 1")
        for (int t = 4; t < 11; ++t) M1STEP();
        u64 pv = 0;
        if (wid == 0) pv = __hip_atomic_load(fS + (lane & 31), RLX_AGENT);
        float og[4];
#pragma unroll
        for (int m = 0; m < 4; ++m) og[m] = __uint_as_float(__builtin_amdgcn_raw_buffer_load_b32(rs, (wm * 64 + m * 16 + (lane & 15)) * 4, WS_G + rb * 512, 16));
_Pragma("unroll 1")
        for (int t = 11; t < 15; ++t) M1STEP();
        if (wid == 0) wait_flags(fS + (lane & 31), pv, tag);
        cstep<PH_M1, PH_M1, true, 7>(smem + sc * SLOTM, smem, c, c, acc, A0, B0, A1, B1);
        STAMP(9);
#pragma unroll
        for (int m = 0; m < 4; ++m) og[m] = 1.0f - og[m];
        if (wn == 0) m1_epilogue<0>(acc, og, smem, rs, rb, cb, wm, lane);
        else m1_epilogue<1>(acc, og, smem, rs, rb, cb, wm, lane);
    }
    {
        f32x4 acc[4][4];
#pragma unroll
        for (int n = 0; n < 4; ++n)
#pragma unroll
            for (int m = 0; m < 4; ++m) acc[n][m] = (f32x4){0.f, 0.f, 0.f, 0.f};
        bf16x8 B0[4], B1[4];
        const LaneC cs = lane_consts<PH_GS>(wm, wn, lane);
        STAMP(10);
        BAR();
        STAMP(11);
        load_frags<PH_GS, 0, 4>(smem, cs, A0, B0);
        SCHED0();
#define GSSTEP(t) cstep<PH_GS, PH_GS, false, 4>(smem + ((t) & 3) * SLOT, smem + (((t) + 1) & 3) * SLOT, cs, cs, acc, A0, B0, A1, B1)
_Pragma("unroll 1")
        for (int t = 0; t < 6; ++t) GSSTEP(t);
        signal_flag(fM + c8 * 4 + wid, tag, lane);
_Pragma("unroll 1")
        for (int t = 6; t < 9; ++t) GSSTEP(t);
        u64 pv = 0;
        if (wid == 0) pv = __hip_atomic_load(fM + (lane & 31), RLX_AGENT);
_Pragma("unroll 1")
        for (int t = 9; t < 12; ++t) GSSTEP(t);
        if (wid == 0) wait_flags(fM + (lane & 31), pv, tag);
_Pragma("unroll 1")
        for (int t = 12; t < 15; ++t) GSSTEP(t);
        STAMP(12);
        const LaneC cm = lane_consts<PH_GM>(wm, wn, lane);
        cstep<PH_GS, PH_GM, false, 4>(smem + 3 * SLOT, smem, cs, cm, acc, A0, B0, A1, B1);
_Pragma("unroll 1")
        for (int t = 16; t < 40; ++t) cstep<PH_GM, PH_GM, false, 4>(smem + (t & 3) * SLOT, smem + ((t + 1) & 3) * SLOT, cm, cm, acc, A0, B0, A1, B1);
        const int r = lane & 15, q = lane >> 4;
        const int t0r = rb * 128 + wm * 64 + kh * 32 + r, d0 = c8 * 128 + wn * 64 + q * 4;
        u32x2 xw[2][4], dw[4], bw[4]; float gg[2];
#pragma unroll
        for (int n = 0; n < 4; ++n) {
            dw[n] = *(const u32x2*)(a.dsk + d0 + n * 16); bw[n] = *(const u32x2*)(a.b2 + d0 + n * 16);
#pragma unroll
            for (int mm = 0; mm < 2; ++mm) xw[mm][n] = *(const u32x2*)(a.x + (size_t)(t0r + mm * 16) * 1024 + d0 + n * 16);
        }
#pragma unroll
        for (int mm = 0; mm < 2; ++mm) gg[mm] = __uint_as_float(__builtin_amdgcn_raw_buffer_load_b32(rs, (t0r + mm * 16) * 4, WS_G, 16));
        asm volatile("" ::: "memory");
_Pragma("unroll 1")
        for (int t = 40; t < 43; ++t) cstep<PH_GM, PH_GM, false, 4>(smem + (t & 3) * SLOT, smem + ((t + 1) & 3) * SLOT, cm, cm, acc, A0, B0, A1, B1);
        cstep<PH_GM, PH_GM, true, 4>(smem + 3 * SLOT, smem, cm, cm, acc, A0, B0, A1, B1);

        STAMP(13);
        if (kh) {
#pragma unroll
            for (int n = 0; n < 4; ++n)
#pragma unroll
                for (int mm = 0; mm < 2; ++mm) { const f32x4 t = acc[n][mm]; acc[n][mm] = acc[n][2 + mm]; acc[n][2 + mm] = t; }
        }
        const unsigned xo = WS_XB + (unsigned)(bid * 4 + wid) * 8192u, xi = WS_XB + (unsigned)((bid ^ 1) * 4 + wid) * 8192u;
#pragma unroll
        for (int n = 0; n < 4; ++n)
#pragma unroll
            for (int mm = 0; mm < 2; ++mm)
                st128(__builtin_bit_cast(u32x4, acc[n][2 + mm]), rs, lane * 16 + (n * 2 + mm) * 1024, xo, 16);
        f32x4 o[4][2];
#pragma unroll
        for (int mm = 0; mm < 2; ++mm) {
            const float g = gg[mm];
#pragma unroll
            for (int n = 0; n < 4; ++n) {
                const f32x4 own = acc[n][mm];
                o[n][mm][0] = own[0] + g * (bflo(xw[mm][n].x) * bflo(dw[n].x)) + (1.0f - g) * bflo(bw[n].x);
                o[n][mm][1] = own[1] + g * (bfhi(xw[mm][n].x) * bfhi(dw[n].x)) + (1.0f - g) * bfhi(bw[n].x);
                o[n][mm][2] = own[2] + g * (bflo(xw[mm][n].y) * bflo(dw[n].y)) + (1.0f - g) * bflo(bw[n].y);
                o[n][mm][3] = own[3] + g * (bfhi(xw[mm][n].y) * bfhi(dw[n].y)) + (1.0f - g) * bfhi(bw[n].y);
            }
        }
        signal_flag((gu64*)(ws + WS_XFLAG) + (bid * 4 + wid) * 8, tag, lane);
        STAMP(14);
        {
            gu64* fl = (gu64*)(ws + WS_XFLAG) + ((bid ^ 1) * 4 + wid) * 8;
            wait_flags(fl, __hip_atomic_load(fl, RLX_AGENT), tag);
        }
        STAMP(15);
#pragma unroll
        for (int n = 0; n < 4; ++n)
#pragma unroll
            for (int mm = 0; mm < 2; ++mm) {
                const u32x4 v = __builtin_amdgcn_raw_buffer_load_b128(rs, lane * 16 + (n * 2 + mm) * 1024, xi, 16);
                o[n][mm] += __builtin_bit_cast(f32x4, v);
            }
#pragma unroll
        for (int mm = 0; mm < 2; ++mm)
#pragma unroll
            for (int n = 0; n < 4; ++n) *(f32x4*)(a.out + (size_t)(t0r + mm * 16) * 1024 + d0 + n * 16) = o[n][mm];
    }
}
}

extern "C" void kernel_launch(void* const* d_in, const int* in_sizes, int n_in,
                              void* d_out, int out_size, void* d_ws, size_t ws_size,
                              hipStream_t stream) {
    using namespace gm;
    Args a;
    a.x = (const u16*)d_in[0]; a.lre = (const u16*)d_in[1]; a.lim = (const u16*)d_in[2]; a.bre = (const u16*)d_in[3]; a.bim = (const u16*)d_in[4];
    a.cre = (const u16*)d_in[5]; a.cim = (const u16*)d_in[6]; a.ldt = (const u16*)d_in[7]; a.dsk = (const u16*)d_in[8];
    a.w1 = (const u16*)d_in[10]; a.b1 = (const u16*)d_in[11]; a.w2 = (const u16*)d_in[12]; a.b2 = (const u16*)d_in[13]; a.gw = (const u16*)d_in[14];
    a.out = (float*)d_out; a.ws = (char*)d_ws;
    (void)hipFuncSetAttribute((const void*)fused_kernel, hipFuncAttributeMaxDynamicSharedMemorySize, LDS_BYTES);
    fused_kernel<<<256, 512, LDS_BYTES, stream>>>(a);
}
```

```cpp
#include <hip/hip_runtime.h>
#include <hip/hip_bf16.h>

namespace gm {
#define LAS __attribute__((address_space(3)))
#define GAS __attribute__((address_space(1)))
typedef unsigned short u16;
typedef short bf16x8 __attribute__((ext_vector_type(8)));
typedef short s16x4 __attribute__((ext_vector_type(4)));
typedef float f32x4 __attribute__((ext_vector_type(4)));
typedef float f32x2 __attribute__((ext_vector_type(2)));
typedef unsigned u32x4 __attribute__((ext_vector_type(4)));
typedef unsigned u32x2 __attribute__((ext_vector_type(2)));
typedef LAS char* ldsp;
typedef GAS unsigned gu32;
typedef unsigned long long u64;
typedef GAS u64 gu64;

#define BAR() __builtin_amdgcn_s_barrier()
#define WAITV(n) asm volatile("s_waitcnt vmcnt(%0)" ::"i"(n) : "memory")
#define SCHED0() __builtin_amdgcn_sched_barrier(0)
#define RLX_AGENT __ATOMIC_RELAXED, __HIP_MEMORY_SCOPE_AGENT

constexpr unsigned WS_EPOCH = 0;
constexpr unsigned WS_FY = 4096;
constexpr unsigned WS_FS = 16384;
constexpr unsigned WS_FM = 24576;
constexpr unsigned WS_XFLAG = 32768;
constexpr unsigned WS_GRAN = 131072;
constexpr unsigned WS_G = 1u << 20;
constexpr unsigned WS_Y = 2u << 20;
constexpr unsigned WS_A2 = 8u << 20;
constexpr unsigned WS_XB = 32u << 20;
constexpr unsigned WS_END = 40u << 20;

constexpr int SLOT = 32768, SLOTM = 45056;
constexpr int LDS_E = 135168;
constexpr int LDS_COEF = LDS_E + 4096;
constexpr int LDS_B1 = LDS_COEF + 2048;
constexpr int LDS_DUMMY = LDS_B1 + 512;
constexpr int LDS_BYTES = LDS_DUMMY + 256;

enum { PH_S1 = 0, PH_M1 = 1, PH_GS = 2, PH_GM = 3 };

struct Args {
    const u16 *x, *lre, *lim, *bre, *bim, *cre, *cim, *ldt, *dsk, *w1, *b1, *w2, *b2, *gw;
    float* out;
    char* ws;
};

__device__ __forceinline__ bf16x8 lds128(ldsp p) { return *(const LAS bf16x8*)p; }
__device__ __forceinline__ s16x4 ldstr(ldsp p) { return __builtin_amdgcn_ds_read_tr16_b64_v4i16((LAS s16x4*)p); }
__device__ __forceinline__ bf16x8 cat(s16x4 a, s16x4 b) { return __builtin_shufflevector(a, b, 0, 1, 2, 3, 4, 5, 6, 7); }
__device__ __forceinline__ void dma16(const void* g, ldsp l) { __builtin_amdgcn_global_load_lds((const unsigned*)g, (LAS unsigned*)l, 16, 0, 0); }
__device__ __forceinline__ void dma16c(const void* g, ldsp l) { __builtin_amdgcn_global_load_lds((const unsigned*)g, (LAS unsigned*)l, 16, 0, 16); }
__device__ __forceinline__ void st128(u32x4 v, __amdgpu_buffer_rsrc_t rs, unsigned voff, unsigned base, int) { __builtin_amdgcn_raw_buffer_store_b128(v, rs, voff + base, 0, 16); }
__device__ __forceinline__ void st64(u32x2 v, __amdgpu_buffer_rsrc_t rs, unsigned voff, unsigned base, int) { __builtin_amdgcn_raw_buffer_store_b64(v, rs, voff + base, 0, 16); }
__device__ __forceinline__ float bflo(unsigned w) { return __uint_as_float(w << 16); }
__device__ __forceinline__ float bfhi(unsigned w) { return __uint_as_float(w & 0xffff0000u); }
__device__ __forceinline__ float bf1(u16 b) { return __uint_as_float((unsigned)b << 16); }
__device__ __forceinline__ unsigned pkbf(float lo, float hi) {
    typedef __bf16 bf2_t __attribute__((ext_vector_type(2)));
    bf2_t v = {(__bf16)lo, (__bf16)hi};
    return __builtin_bit_cast(unsigned, v);
}
__device__ __forceinline__ bool deadline(unsigned long long t0) { return __builtin_amdgcn_s_memrealtime() - t0 > 400000ull; }

struct LaneC { unsigned a0, a1, b0, b1, b2; };
template <int PH>
__device__ __forceinline__ LaneC lane_consts(int wm, int wn, int lane) {
    const int r = lane & 15, q = lane >> 4, rr = r >> 2, p = lane & 3;
    const unsigned rl = r * 128 + ((q ^ (r >> 1)) * 16);
    LaneC c; c.b2 = 0;
    if constexpr (PH == PH_S1) { c.a0 = wm * 8192 + rl; c.a1 = c.a0 ^ 64; c.b0 = 16384 + wn * 8192 + rl; c.b1 = c.b0 ^ 64; }
    if constexpr (PH == PH_M1) {
        c.a0 = wm * 8192 + rl; c.a1 = c.a0 ^ 64;
        const unsigned t0 = 16384 + q * 7 * 512 + rr * 64 + ((q & 1) * 32) + p * 8;
        c.b0 = t0 + wn * 2048;
        c.b1 = (t0 ^ 32) + wn * 2048;
        c.b2 = t0 ^ (wn * 32);
    }
    if constexpr (PH == PH_GS) {
        c.a0 = (q * 8 + wm * 4) * 256 + (p >> 1) * 128 + (4 * (q & 1) + rr) * 16 + (p & 1) * 8; c.a1 = c.a0 ^ 64;
        c.b0 = 16384 + wn * 8192 + rl; c.b1 = c.b0 ^ 64;
    }
    if constexpr (PH == PH_GM) {
        c.a0 = wm * 8192 + lane * 16; c.a1 = c.a0;
        c.b0 = 16384 + q * 4 * 512 + wn * 1024 + rr * 64 + ((q & 1) * 32) + p * 8; c.b1 = c.b0 ^ 32;
    }
    return c;
}
template <int PH, int KK, int NB>
__device__ __forceinline__ void load_frags(ldsp slot, const LaneC& c, bf16x8 (&A)[4], bf16x8 (&B)[NB]) {
    if constexpr (PH == PH_S1 || PH == PH_M1) {
        ldsp pa = slot + (KK ? c.a1 : c.a0);
#pragma unroll
        for (int m = 0; m < 4; ++m) A[m] = lds128(pa + m * 2048);
    } else if constexpr (PH == PH_GS) {
        ldsp p0 = slot + c.a0, p1 = slot + c.a1;
#pragma unroll
        for (int m = 0; m < 4; ++m) A[m] = cat(ldstr(p0 + (KK * 32 + m) * 256), ldstr(p1 + (KK * 32 + m) * 256));
    } else {
        ldsp pa = slot + c.a0;
#pragma unroll
        for (int m = 0; m < 4; ++m) A[m] = lds128(pa + (m * 2 + KK) * 1024);
    }
    if constexpr (PH == PH_S1 || PH == PH_GS) {
        ldsp pb = slot + (KK ? c.b1 : c.b0);
#pragma unroll
        for (int n = 0; n < NB; ++n) B[n] = lds128(pb + n * 2048);
    } else {
        constexpr int NS2 = PH == PH_M1 ? 7 : 4;
        ldsp p0 = slot + c.b0, p1 = slot + c.b1;
#pragma unroll
        for (int n = 0; n < NB; ++n) { ldsp p = (n == 6 ? slot + c.b2 : (n & 1) ? p1 : p0) + KK * 4 * NS2 * 512 + (n >> 1) * 512; B[n] = cat(ldstr(p), ldstr(p + 256)); }
    }
}
template <int PH, int NB>
__device__ __forceinline__ void mma(f32x4 (&acc)[NB][4], const bf16x8 (&A)[4], const bf16x8 (&B)[NB]) {
#pragma unroll
    for (int n = 0; n < NB; ++n)
#pragma unroll
        for (int m = 0; m < 4; ++m) {
            if constexpr (PH == PH_S1) acc[n][m] = __builtin_amdgcn_mfma_f32_16x16x32_bf16(A[m], B[n], acc[n][m], 0, 0, 0);
            else acc[n][m] = __builtin_amdgcn_mfma_f32_16x16x32_bf16(B[n], A[m], acc[n][m], 0, 0, 0);
        }
}
template <int PH> constexpr int nreads() { return PH == PH_S1 ? 8 : PH == PH_M1 ? 18 : 12; }
template <int NR, int NM>
__device__ __forceinline__ void sched_half() {
#pragma unroll
    for (int i = 0; i < NR; ++i) { __builtin_amdgcn_sched_group_barrier(0x8, 1, 0); __builtin_amdgcn_sched_group_barrier(0x100, 1, 0); }
    __builtin_amdgcn_sched_group_barrier(0x8, NM - NR, 0);
}
template <int PC, int PN, bool LAST, int NB>
__device__ __forceinline__ void cstep(ldsp cur, ldsp nxt, const LaneC& cc, const LaneC& cn, f32x4 (&acc)[NB][4], bf16x8 (&A0)[4], bf16x8 (&B0)[NB], bf16x8 (&A1)[4], bf16x8 (&B1)[NB]) {
    load_frags<PC, 1, NB>(cur, cc, A1, B1);
    mma<PC, NB>(acc, A0, B0);
    sched_half<nreads<PC>(), 4 * NB>();
    SCHED0();
    __builtin_amdgcn_s_waitcnt(0xC07F);
    BAR();
    SCHED0();
    if constexpr (!LAST) load_frags<PN, 0, NB>(nxt, cn, A0, B0);
    mma<PC, NB>(acc, A1, B1);
    if constexpr (!LAST) sched_half<nreads<PN>(), 4 * NB>();
    SCHED0();
}

__device__ __forceinline__ u64 next_tag(u64 e) { return e * 0x9E3779B97F4A7C15ull + 0xD1B54A32D192ED03ull; }
__device__ __forceinline__ void wait_flags(gu64* fl, u64 v, u64 tag) {
    if (!__all(v == tag)) {
        const unsigned long long t0 = __builtin_amdgcn_s_memrealtime();
        do { __builtin_amdgcn_s_sleep(2); v = __hip_atomic_load(fl, RLX_AGENT); } while (!__all(v == tag) && !deadline(t0));
    }
    asm volatile("" ::: "memory");
}
__device__ __forceinline__ void signal_flag(gu64* fl, u64 tag, int lane) {
    WAITV(0);
    if (lane == 0) __hip_atomic_store(fl, tag, RLX_AGENT);
}
__device__ __forceinline__ void warm(const void* g, ldsp smem) { __builtin_amdgcn_global_load_lds((const unsigned*)g, (LAS unsigned*)(smem + LDS_DUMMY), 4, 0, 0); }
template <int CTRL, int RMASK> __device__ __forceinline__ float dpp_add(float v) {
    return v + __builtin_bit_cast(float, __builtin_amdgcn_update_dpp(0, __builtin_bit_cast(int, v), CTRL, RMASK, 0xf, true));
}
__device__ __forceinline__ float wave_sum(float v) {
    v = dpp_add<0x111, 0xf>(v); v = dpp_add<0x112, 0xf>(v); v = dpp_add<0x114, 0xf>(v); v = dpp_add<0x118, 0xf>(v);
    v = dpp_add<0x142, 0xa>(v); v = dpp_add<0x143, 0xc>(v);
    return __builtin_bit_cast(float, __builtin_amdgcn_readlane(__builtin_bit_cast(int, v), 63));
}
#define STAMP(i) do { } while (0)

__device__ __forceinline__ unsigned gelu2(f32x2 v, float og) {
    const f32x2 t = (v * v) * -0.1029432397f + -2.3022081986f;
    const f32x2 u = v * t;
    f32x2 d; d.x = __builtin_amdgcn_exp2f(u.x); d.y = __builtin_amdgcn_exp2f(u.y);
    d = d + 1.0f;
    f32x2 r; r.x = __builtin_amdgcn_rcpf(d.x); r.y = __builtin_amdgcn_rcpf(d.y);
    const f32x2 o = (v * og) * r;
    return pkbf(o.x, o.y);
}

__device__ __forceinline__ void m1_epilogue(f32x4 (&acc)[7][4], const float (&og)[4], ldsp smem, __amdgpu_buffer_rsrc_t rs, int rb, int cb, int wm, int wn, int lane) {
    const int q = lane >> 4;
    u32x2 pk[7][4];
#pragma unroll
    for (int n = 0; n < 7; ++n) {
        const int nbt = n == 6 ? 6 + wn : n + 8 * wn;
        const u32x2 bw = *(const LAS u32x2*)(smem + LDS_B1 + (nbt * 16 + q * 4) * 2);
        const f32x2 b01 = {bflo(bw.x), bfhi(bw.x)}, b23 = {bflo(bw.y), bfhi(bw.y)};
#pragma unroll
        for (int m = 0; m < 4; ++m) {
            const f32x4 a = acc[n][m];
            pk[n][m].x = gelu2((f32x2){a[0], a[1]} + b01, og[m]);
            pk[n][m].y = gelu2((f32x2){a[2], a[3]} + b23, og[m]);
        }
        if ((n & 1) || n == 6) {
            const int pair = 7 * cb + (nbt >> 1);
            const unsigned base = WS_A2 + (unsigned)(rb * 88 + 32 + (pair >> 1)) * 16384u + (unsigned)(pair & 1) * 1024u + (unsigned)(wm * 4) * 2048u;
#pragma unroll
            for (int m = 0; m < 4; ++m) {
                if (n == 6) st64(pk[n][m], rs, lane * 16 + m * 2048, base + wn * 8, 16);
                else { const u32x4 v = {pk[n - (n & 1)][m].x, pk[n - (n & 1)][m].y, pk[n][m].x, pk[n][m].y}; st128(v, rs, lane * 16 + m * 2048, base, 16); }
            }
        }
    }
}

__global__ __launch_bounds__(512, 2) void fused_kernel(Args a) {
    extern __shared__ __attribute__((aligned(16))) char smem_[];
    ldsp smem = (ldsp)smem_;
    const int wid = __builtin_amdgcn_readfirstlane(threadIdx.x >> 6), lane = threadIdx.x & 63;
    const int bid = blockIdx.x, xcd = bid & 7, rk = bid >> 3;
    const int kh = xcd & 1, rb = (xcd >> 1) * 4 + (rk >> 3), c8 = rk & 7, cb = kh * 8 + c8;
    char* const ws = a.ws;
    const char* pc; unsigned code_len;
    asm volatile("s_getpc_b64 %0\n.Lgm_pc%=:\n\ts_mov_b32 %1, gm_code_end-.Lgm_pc%=" : "=s"(pc), "=s"(code_len));
    asm volatile("" ::"s"(a.x), "s"(a.lre), "s"(a.lim), "s"(a.bre), "s"(a.bim), "s"(a.cre), "s"(a.cim), "s"(a.ldt), "s"(a.dsk), "s"(a.w1), "s"(a.b1), "s"(a.w2), "s"(a.b2), "s"(a.gw), "s"(a.out), "s"(a.ws));

    if (wid >= 4) {
        const int w = wid - 4, un = (xcd >> 1) * 32 + rk;
        const unsigned swz = (unsigned)(((lane & 7) ^ ((4 * (w & 1) + (lane >> 4)) & 7)) * 16);
        {
            const unsigned va = (unsigned)((rb * 128 + 16 * (lane >> 5) + ((lane >> 3) & 3) + 32 * (w & 1) + 4 * (w >> 1)) * 2048) + swz;
            const unsigned vb = (unsigned)((cb * 64 + 16 * (w >> 1) + 8 * (w & 1) + (lane >> 3)) * 2048) + swz;
            auto issue = [&](int t) {
                ldsp s = smem + (t & 3) * SLOT + w * 1024;
                const char* pa = (const char*)a.x + t * 128;
#pragma unroll
                for (int n = 0; n < 4; ++n) dma16(pa + (64 * (n >> 1) + 8 * (n & 1)) * 2048 + va, s + n * 4096);
#pragma unroll
                for (int n = 0; n < 4; ++n) dma16((const char*)((n & 1) ? a.bim : a.bre) + t * 128 + (n >> 1) * 32 * 2048 + vb, s + 16384 + n * 4096);
            };
            if (w < 2) {
                const unsigned o = (unsigned)(((rk & 3) * 2 + w) * 8192 + lane * 128);
                warm(pc + (o < code_len && code_len < (1u << 20) ? o : 0u), smem);
            }
            unsigned plr = 0, pli = 0, pld = 0;
            if (w == 3) {
                const u16 *p0 = a.lre + cb * 64 + lane, *p1 = a.lim + cb * 64 + lane, *p2 = a.ldt + cb * 64 + lane;
                asm volatile("global_load_ushort %0, %3, off\n\tglobal_load_ushort %1, %4, off\n\tglobal_load_ushort %2, %5, off"
                             : "+v"(plr), "+v"(pli), "+v"(pld) : "v"(p0), "v"(p1), "v"(p2) : "memory");
            }
            if (w == 2) {
                const char* pb = (const char*)a.b1 + cb * 448;
                __builtin_amdgcn_global_load_lds((const unsigned*)(pb + lane * 4), (LAS unsigned*)(smem + LDS_B1), 4, 0, 0);
                __builtin_amdgcn_global_load_lds((const unsigned*)(pb + (lane < 48 ? 64 + lane : 111) * 4), (LAS unsigned*)(smem + LDS_B1 + 256), 4, 0, 0);
            }
_Pragma("unroll 1")
            for (int t = 0; t < 4; ++t) issue(t);
            {
                const char* pB = (const char*)(((lane >> 4) & 1) ? a.bim : a.bre) + (512 * kh + 16 * rk + (lane & 15)) * 2048 + (4 + (lane >> 5)) * 128;
                const char* pX = (const char*)a.x + (512 * (xcd >> 1) + 256 * (1 - kh) + 8 * rk + (lane & 7)) * 2048;
                warm(w == 2 ? pX + (4 + (lane >> 3)) * 128 : pB + (w == 3 ? 2 : w) * 256, smem);
                warm(w == 3 ? pX + (12 + ((lane >> 3) & 3)) * 128 : pB + (3 + w) * 256, smem);
            }
            if (w == 3) {
                asm volatile("s_waitcnt vmcnt(34)" : "+v"(plr), "+v"(pli), "+v"(pld)::"memory");
                const float lr = bflo(plr), li = bflo(pli);
                const float dt = (float)(__bf16)expf(bflo(pld));
                const float zr = dt * lr, zi = dt * li;
                float sn, cs; sincosf(zi, &sn, &cs);
                const float er = expf(zr), Lr = er * cs, Li = er * sn;
                const float den = 1.0f / (lr * lr + li * li), nr = Lr - 1.0f;
                const float cr = (nr * lr + Li * li) * den, ci = (Li * lr - nr * li) * den;
                float pr = Lr, pi = Li;
#pragma unroll
                for (int i = 0; i < 4; ++i) { const float t = pr * pr - pi * pi; pi = 2.0f * pr * pi; pr = t; }
                const float p16r = pr, p16i = pi;
#pragma unroll
                for (int i = 0; i < 3; ++i) { const float t = pr * pr - pi * pi; pi = 2.0f * pr * pi; pr = t; }
                const f32x4 v0 = {Lr, Li, cr, ci}, v1 = {p16r, p16i, pr, pi};
                const unsigned la = (unsigned)(size_t)(smem + LDS_COEF) + lane * 32;
                asm volatile("ds_write_b128 %0, %1\n\tds_write_b128 %0, %2 offset:16\n\ts_waitcnt lgkmcnt(0)" ::"v"(la), "v"(v0), "v"(v1) : "memory");
            }
            WAITV(26); BAR();
_Pragma("unroll 1")
            for (int t = 0; t < 3; ++t) { WAITV(18); BAR(); issue(t + 4); }
_Pragma("unroll 1")
            for (int t = 3; t < 12; ++t) { WAITV(16); BAR(); issue(t + 4); }
            warm((const char*)a.w1 + (size_t)(8 * un + 2 * w + (lane >= 28 ? 1 : 0)) * 7168 + kh * 3584 + (lane >= 56 ? 27 : lane >= 28 ? lane - 28 : lane) * 128, smem);
            WAITV(17); BAR();
            WAITV(9); BAR();
            WAITV(1); BAR();
            BAR();
        }
        {
            const unsigned va = (unsigned)((rb * 128 + 8 * w + (lane >> 3)) * 2048) + swz;
            unsigned vb[7];
#pragma unroll
            for (int i = 0; i < 7; ++i) {
                const int sl = 2 * i + (lane >> 5), kq = 2 * w + (sl >= 7 ? 1 : 0), nb2 = sl >= 7 ? sl - 7 : sl;
                vb[i] = (unsigned)((8 * kq + ((lane >> 2) & 7)) * 7168 + cb * 448 + nb2 * 64 + (((lane & 3) ^ (2 * (kq & 1))) * 16));
            }
            int slot = 0;
            auto issue = [&](int t) {
                ldsp s = smem + slot * SLOTM;
                slot = slot == 2 ? 0 : slot + 1;
                const char* pa = ws + WS_Y + t * 128;
#pragma unroll
                for (int n = 0; n < 4; ++n) dma16c(pa + n * 32 * 2048 + va, s + w * 1024 + n * 4096);
                const char* pb = (const char*)a.w1 + (size_t)t * 64 * 7168;
#pragma unroll
                for (int i = 0; i < 7; ++i) dma16(pb + vb[i], s + 16384 + (w * 7 + i) * 1024);
            };
            issue(0);
            BAR();
            BAR();
_Pragma("unroll 1")
            for (int t = 1; t < 3; ++t) issue(t);
            WAITV(22); BAR();
_Pragma("unroll 1")
            for (int t = 0; t < 13; ++t) { WAITV(11); BAR(); issue(t + 3); }
            warm((const char*)(((lane >> 3) & 1) ? a.cim : a.cre) + (8 * un + 2 * w + ((lane >> 4) & 1)) * 2048 + kh * 1024 + (lane & 7) * 128, smem);
            warm((const char*)a.w2 + (size_t)(1792 * kh + 14 * un) * 2048 + (w * 56 + (lane < 56 ? lane : 55)) * 128, smem);
            WAITV(13); BAR();
            WAITV(2); BAR();
            BAR();
        }
        {
            const char* a2 = ws + WS_A2 + (size_t)rb * 88 * 16384 + w * 4096 + lane * 16;
            const unsigned vr = (unsigned)((c8 * 128 + 8 * w + (lane >> 3)) * 2048) + swz;
            const int rho = (lane >> 2) & 7;
            unsigned vt[2];
#pragma unroll
            for (int qq = 0; qq < 2; ++qq) { const int q = 2 * (w & 1) + qq; const int k = 32 * (w >> 1) + (rho < 4 ? 4 * q + rho : 16 + 4 * q + rho - 4);
                vt[qq] = (unsigned)(k * 2048 + c8 * 256 + (lane >> 5) * 64 + (((lane & 3) ^ (2 * qq)) * 16)); }
            auto issue = [&](int t) {
                ldsp s = smem + (t & 3) * SLOT;
                if (t < 16) {
                    const int ks = kh * 16 + t;
                    const char* pa = a2 + (size_t)ks * 16384;
#pragma unroll
                    for (int n = 0; n < 4; ++n) dma16c(pa + n * 1024, s + w * 4096 + n * 1024);
                    const char* pb = (const char*)((ks & 1) ? a.cim : a.cre) + (ks >> 1) * 128;
#pragma unroll
                    for (int n = 0; n < 4; ++n) dma16(pb + n * 32 * 2048 + vr, s + 16384 + (w + 4 * n) * 1024);
                } else {
                    const int km = kh * 28 + (t - 16);
                    const char* pa = a2 + (size_t)(32 + km) * 16384;
#pragma unroll
                    for (int n = 0; n < 4; ++n) dma16c(pa + n * 1024, s + w * 4096 + n * 1024);
                    const char* pb = (const char*)a.w2 + (size_t)km * 64 * 2048;
#pragma unroll
                    for (int n = 0; n < 4; ++n) dma16(pb + (n & 1) * 128 + vt[n >> 1], s + 16384 + (w * 4 + n) * 1024);
                }
            };
_Pragma("unroll 1")
            for (int t = 0; t < 4; ++t) issue(t);
            WAITV(24); BAR();
_Pragma("unroll 1")
            for (int t = 0; t < 40; ++t) { WAITV(16); BAR(); issue(t + 4); }
            WAITV(16); BAR();
            WAITV(8); BAR();
            WAITV(0); BAR();
            BAR();
        }
        if (bid == 0 && w == 0) {
            const u64 tag = next_tag(__hip_atomic_load((gu64*)(ws + WS_EPOCH), RLX_AGENT));
            const unsigned long long t0 = __builtin_amdgcn_s_memrealtime();
            for (;;) {
                bool ok = true;
#pragma unroll
                for (int i = 0; i < 16; ++i) ok &= __hip_atomic_load((gu64*)(ws + WS_FY) + i * 64 + lane, RLX_AGENT) == tag;
                if (__all(ok) || deadline(t0)) break;
                __builtin_amdgcn_s_sleep(2);
            }
            if (lane == 0) __hip_atomic_store((gu64*)(ws + WS_EPOCH), tag, RLX_AGENT);
        }
        return;
    }

    const int wm = wid >> 1, wn = wid & 1;
    const __amdgpu_buffer_rsrc_t rs = __builtin_amdgcn_make_buffer_rsrc(ws, 0, WS_END, 0x00020000);
    gu64* const fS = (gu64*)(ws + WS_FS) + (rb * 2 + kh) * 32;
    gu64* const fM = (gu64*)(ws + WS_FM) + (rb * 2 + kh) * 32;
    STAMP(0);
    u64 tag;
    const int lnrow = 512 * (xcd >> 1) + 256 * kh + 8 * rk + 2 * wid;
    {
        u32x4 xv[2][2], gv[2];
#pragma unroll
        for (int i = 0; i < 2; ++i)
#pragma unroll
            for (int h = 0; h < 2; ++h) xv[i][h] = *(const u32x4*)(a.x + (size_t)(lnrow + i) * 1024 + h * 512 + lane * 8);
#pragma unroll
        for (int h = 0; h < 2; ++h) gv[h] = *(const u32x4*)(a.gw + h * 512 + lane * 8);
        const u64 ep = __hip_atomic_load((gu64*)(ws + WS_EPOCH), RLX_AGENT);
        f32x2 xf[2][8];
        float s[2], gd[2];
#pragma unroll
        for (int i = 0; i < 2; ++i) {
            f32x2 s2 = {0.f, 0.f}, g2 = {0.f, 0.f};
#pragma unroll
            for (int h = 0; h < 2; ++h)
#pragma unroll
                for (int j = 0; j < 4; ++j) {
                    const f32x2 v = {bflo(xv[i][h][j]), bfhi(xv[i][h][j])}, gw = {bflo(gv[h][j]), bfhi(gv[h][j])};
                    xf[i][h * 4 + j] = v; s2 += v; g2 += v * gw;
                }
            s[i] = wave_sum(s2.x + s2.y) * (1.0f / 1024.0f); gd[i] = wave_sum(g2.x + g2.y);
        }
#pragma unroll
        for (int i = 0; i < 2; ++i) {
            f32x2 v2 = {0.f, 0.f};
#pragma unroll
            for (int j = 0; j < 8; ++j) { xf[i][j] -= s[i]; v2 += xf[i][j] * xf[i][j]; }
            const float rstd = 1.0f / sqrtf(wave_sum(v2.x + v2.y) * (1.0f / 1024.0f) + 1e-5f);
#pragma unroll
            for (int h = 0; h < 2; ++h) {
                u32x4 o;
#pragma unroll
                for (int j = 0; j < 4; ++j) { const f32x2 v = xf[i][h * 4 + j] * rstd; o[j] = pkbf(v.x, v.y); }
                st128(o, rs, lane * 16 + h * 1024, WS_Y + (unsigned)(lnrow + i) * 2048u, 16);
            }
        }
        const float gl = lane ? gd[1] : gd[0];
        const float g = 1.0f / (1.0f + expf(-gl));
        if (lane < 2) __hip_atomic_store((gu32*)(ws + WS_G) + lnrow + lane, __float_as_uint(g), RLX_AGENT);
        tag = next_tag(((u64)(unsigned)__builtin_amdgcn_readfirstlane((unsigned)(ep >> 32)) << 32) | (unsigned)__builtin_amdgcn_readfirstlane((unsigned)ep));
    }
    const unsigned tag_lo = (unsigned)tag, tag_hi = (unsigned)(tag >> 32);
    bf16x8 A0[4], A1[4];
    {
        f32x4 acc[4][4];
#pragma unroll
        for (int n = 0; n < 4; ++n)
#pragma unroll
            for (int m = 0; m < 4; ++m) acc[n][m] = (f32x4){0.f, 0.f, 0.f, 0.f};
        bf16x8 B0[4], B1[4];
        const LaneC c = lane_consts<PH_S1>(wm, wn, lane);
        __builtin_amdgcn_s_waitcnt(0xC07F);
        STAMP(1);
        BAR();
        STAMP(2);
        load_frags<PH_S1, 0, 4>(smem, c, A0, B0);
        SCHED0();
#define S1STEP(t) cstep<PH_S1, PH_S1, false, 4>(smem + ((t) & 3) * SLOT, smem + (((t) + 1) & 3) * SLOT, c, c, acc, A0, B0, A1, B1)
_Pragma("unroll 1")
        for (int t = 0; t < 4; ++t) S1STEP(t);
        signal_flag((gu64*)(ws + WS_FY) + (lnrow >> 7) * 64 + (rk & 15) * 4 + wid, tag, lane);
_Pragma("unroll 1")
        for (int t = 4; t < 10; ++t) S1STEP(t);
        gu64* const fY = (gu64*)(ws + WS_FY) + rb * 64 + lane;
        const u64 pv = __hip_atomic_load(fY, RLX_AGENT);
_Pragma("unroll 1")
        for (int t = 10; t < 13; ++t) S1STEP(t);
        wait_flags(fY, pv, tag);
        const int ch = lane & 15, q = lane >> 4, seg = wm * 4 + q;
        f32x4 gq[4];
#pragma unroll
        for (int m = 0; m < 4; ++m) gq[m] = __builtin_bit_cast(f32x4, __builtin_amdgcn_raw_buffer_load_b128(rs, (seg * 16 + m * 4) * 4, WS_G + rb * 512, 16));
_Pragma("unroll 1")
        for (int t = 13; t < 15; ++t) S1STEP(t);
        cstep<PH_S1, PH_S1, true, 4>(smem + 3 * SLOT, smem, c, c, acc, A0, B0, A1, B1);

        STAMP(3);
        f32x4 k0[2], k1[2];
#pragma unroll
        for (int cbk = 0; cbk < 2; ++cbk) {
            const int cl = wn * 32 + cbk * 16 + ch;
            k0[cbk] = *(const LAS f32x4*)(smem + LDS_COEF + cl * 32); k1[cbk] = *(const LAS f32x4*)(smem + LDS_COEF + cl * 32 + 16);
        }
#pragma unroll
        for (int i = 1; i < 16; ++i)
#pragma unroll
            for (int cbk = 0; cbk < 2; ++cbk) {
                const float sr = acc[cbk][(i - 1) >> 2][(i - 1) & 3], si = acc[2 + cbk][(i - 1) >> 2][(i - 1) & 3];
                acc[cbk][i >> 2][i & 3] = fmaf(k0[cbk][0], sr, fmaf(-k0[cbk][1], si, acc[cbk][i >> 2][i & 3]));
                acc[2 + cbk][i >> 2][i & 3] = fmaf(k0[cbk][0], si, fmaf(k0[cbk][1], sr, acc[2 + cbk][i >> 2][i & 3]));
            }
#pragma unroll
        for (int cbk = 0; cbk < 2; ++cbk)
            *(LAS f32x2*)(smem + LDS_E + (seg * 64 + wn * 32 + cbk * 16 + ch) * 8) = (f32x2){acc[cbk][3][3], acc[2 + cbk][3][3]};
        __builtin_amdgcn_s_waitcnt(0xC07F);
        BAR();
        STAMP(4);
        asm volatile("" ::: "memory");
        f32x2 e[2][8];
#pragma unroll
        for (int cbk = 0; cbk < 2; ++cbk)
#pragma unroll
            for (int sgi = 0; sgi < 8; ++sgi) e[cbk][sgi] = *(const LAS f32x2*)(smem + LDS_E + (sgi * 64 + wn * 32 + cbk * 16 + ch) * 8);
        if (wm == 0 && rb < 15) {
            float er[2] = {0.f, 0.f}, ei[2] = {0.f, 0.f};
#pragma unroll
            for (int sgi = 0; sgi < 8; ++sgi)
#pragma unroll
                for (int cbk = 0; cbk < 2; ++cbk) {
                    const float t = fmaf(k1[cbk][0], er[cbk], fmaf(-k1[cbk][1], ei[cbk], e[cbk][sgi][0]));
                    ei[cbk] = fmaf(k1[cbk][0], ei[cbk], fmaf(k1[cbk][1], er[cbk], e[cbk][sgi][1])); er[cbk] = t; }
            const float sr_ = q ? er[1] + 0.f : er[0] + 0.f, si_ = q ? ei[1] + 0.f : ei[0] + 0.f;
            if (q < 2) { const u32x4 v = {__float_as_uint(sr_), tag_lo, __float_as_uint(si_), tag_hi};
                st128(v, rs, (wn * 32 + q * 16 + ch) * 16, WS_GRAN + (unsigned)(cb * 16 + rb) * 1024u, 16); }
        }
#pragma unroll
        for (int cbk = 0; cbk < 2; ++cbk)
#pragma unroll
            for (int i = 0; i < 16; ++i) {
                const float g2 = 2.0f * gq[i >> 2][i & 3], sr = acc[cbk][i >> 2][i & 3], si = acc[2 + cbk][i >> 2][i & 3];
                acc[cbk][i >> 2][i & 3] = g2 * fmaf(k0[cbk][2], sr, -k0[cbk][3] * si);
                acc[2 + cbk][i >> 2][i & 3] = g2 * fmaf(k0[cbk][2], si, k0[cbk][3] * sr);
            }
        STAMP(5);
        float Sr[2] = {0.f, 0.f}, Si[2] = {0.f, 0.f};
        if (rb > 0) {
            u32x4 gr[2][4];
            const unsigned long long t0 = __builtin_amdgcn_s_memrealtime();
            for (;;) {
                bool ok = true;
                asm volatile("" ::: "memory");
#pragma unroll
                for (int i = 0; i < 4; ++i) {
                    const int rp = rb - 1 - q - 4 * i, rc = rp < 0 ? 0 : rp;
#pragma unroll
                    for (int cbk = 0; cbk < 2; ++cbk) {
                        gr[cbk][i] = __builtin_amdgcn_raw_buffer_load_b128(rs, (wn * 32 + cbk * 16 + ch) * 16 + rc * 1024, WS_GRAN + (unsigned)cb * 16384u, 16);
                        ok &= (gr[cbk][i][1] == tag_lo) & (gr[cbk][i][3] == tag_hi);
                    }
                }
                if (__all(ok) || deadline(t0)) break;
                __builtin_amdgcn_s_sleep(1);
            }
#pragma unroll
            for (int cbk = 0; cbk < 2; ++cbk) {
                const float Pr = k1[cbk][2], Pi = k1[cbk][3];
                const float P2r = Pr * Pr - Pi * Pi, P2i = 2.0f * Pr * Pi, P4r = P2r * P2r - P2i * P2i, P4i = 2.0f * P2r * P2i;
                float hr = 0.f, hi = 0.f;
#pragma unroll
                for (int i = 3; i >= 0; --i) {
                    const bool valid = rb - 1 - q - 4 * i >= 0;
                    const float vr = valid ? __uint_as_float(gr[cbk][i][0]) : 0.f, vi = valid ? __uint_as_float(gr[cbk][i][2]) : 0.f;
                    const float t = fmaf(P4r, hr, fmaf(-P4i, hi, vr)); hi = fmaf(P4r, hi, fmaf(P4i, hr, vi)); hr = t;
                }
                const float ar = (q & 1) ? Pr : 1.0f, ai = (q & 1) ? Pi : 0.f, br = (q & 2) ? P2r : 1.0f, bi = (q & 2) ? P2i : 0.f;
                const float wr = ar * br - ai * bi, wi = ar * bi + ai * br;
                float tr = wr * hr - wi * hi, ti = wr * hi + wi * hr;
                tr += __shfl_xor(tr, 16); ti += __shfl_xor(ti, 16);
                tr += __shfl_xor(tr, 32); ti += __shfl_xor(ti, 32);
                Sr[cbk] = tr; Si[cbk] = ti;
            }
        }
        BAR();
        STAMP(6);
#pragma unroll
        for (int cbk = 0; cbk < 2; ++cbk) {
            float cr = Sr[cbk], ci = Si[cbk];
#pragma unroll
            for (int sgi = 0; sgi < 7; ++sgi) {
                const float t = fmaf(k1[cbk][0], cr, fmaf(-k1[cbk][1], ci, e[cbk][sgi][0])), u = fmaf(k1[cbk][0], ci, fmaf(k1[cbk][1], cr, e[cbk][sgi][1]));
                cr = sgi < seg ? t : cr; ci = sgi < seg ? u : ci;
            }
            float wr = k0[cbk][2] * cr - k0[cbk][3] * ci, wi = k0[cbk][2] * ci + k0[cbk][3] * cr;
            float hr[16], hi[16];
#pragma unroll
            for (int i = 0; i < 16; ++i) {
                const float t = k0[cbk][0] * wr - k0[cbk][1] * wi; wi = k0[cbk][0] * wi + k0[cbk][1] * wr; wr = t;
                const float g2 = 2.0f * gq[i >> 2][i & 3];
                hr[i] = fmaf(g2, wr, acc[cbk][i >> 2][i & 3]); hi[i] = -fmaf(g2, wi, acc[2 + cbk][i >> 2][i & 3]);
            }
            const int kb = 4 * wn + 2 * cbk + (ch >> 3);
            const unsigned vo = (unsigned)((kb * 8 + seg) * 256 + (((ch & 7) ^ (4 * (kb & 1))) * 16));
            const unsigned so = WS_A2 + (unsigned)(rb * 88 + 2 * cb) * 16384u;
#pragma unroll
            for (int th = 0; th < 2; ++th) {
                u32x4 vr, vi;
#pragma unroll
                for (int j = 0; j < 4; ++j) { vr[j] = pkbf(hr[th * 8 + 2 * j], hr[th * 8 + 2 * j + 1]); vi[j] = pkbf(hi[th * 8 + 2 * j], hi[th * 8 + 2 * j + 1]); }
                st128(vr, rs, vo + th * 128, so, 16);
                st128(vi, rs, vo + th * 128, so + 16384u, 16);
            }
        }
    }
    {
        f32x4 acc[7][4];
#pragma unroll
        for (int n = 0; n < 7; ++n)
#pragma unroll
            for (int m = 0; m < 4; ++m) acc[n][m] = (f32x4){0.f, 0.f, 0.f, 0.f};
        bf16x8 B0[7], B1[7];
        const LaneC c = lane_consts<PH_M1>(wm, wn, lane);
        STAMP(7);
        BAR();
        STAMP(8);
        load_frags<PH_M1, 0, 7>(smem, c, A0, B0);
        SCHED0();
        int sc = 0;
#define M1STEP() do { const int sn_ = sc == 2 ? 0 : sc + 1; cstep<PH_M1, PH_M1, false, 7>(smem + sc * SLOTM, smem + sn_ * SLOTM, c, c, acc, A0, B0, A1, B1); sc = sn_; } while (0)
_Pragma("unroll 1")
        for (int t = 0; t < 4; ++t) M1STEP();
        signal_flag(fS + c8 * 4 + wid, tag, lane);
_Pragma("unroll 1")
        for (int t = 4; t < 11; ++t) M1STEP();
        u64 pv = 0;
        if (wid == 0) pv = __hip_atomic_load(fS + (lane & 31), RLX_AGENT);
        float og[4];
#pragma unroll
        for (int m = 0; m < 4; ++m) og[m] = __uint_as_float(__builtin_amdgcn_raw_buffer_load_b32(rs, (wm * 64 + m * 16 + (lane & 15)) * 4, WS_G + rb * 512, 16));
_Pragma("unroll 1")
        for (int t = 11; t < 15; ++t) M1STEP();
        if (wid == 0) wait_flags(fS + (lane & 31), pv, tag);
        cstep<PH_M1, PH_M1, true, 7>(smem + sc * SLOTM, smem, c, c, acc, A0, B0, A1, B1);
        STAMP(9);
#pragma unroll
        for (int m = 0; m < 4; ++m) og[m] = 1.0f - og[m];
        m1_epilogue(acc, og, smem, rs, rb, cb, wm, wn, lane);
    }
    {
        f32x4 acc[4][4];
#pragma unroll
        for (int n = 0; n < 4; ++n)
#pragma unroll
            for (int m = 0; m < 4; ++m) acc[n][m] = (f32x4){0.f, 0.f, 0.f, 0.f};
        bf16x8 B0[4], B1[4];
        const LaneC cs = lane_consts<PH_GS>(wm, wn, lane);
        STAMP(10);
        BAR();
        STAMP(11);
        load_frags<PH_GS, 0, 4>(smem, cs, A0, B0);
        SCHED0();
#define GSSTEP(t) cstep<PH_GS, PH_GS, false, 4>(smem + ((t) & 3) * SLOT, smem + (((t) + 1) & 3) * SLOT, cs, cs, acc, A0, B0, A1, B1)
_Pragma("unroll 1")
        for (int t = 0; t < 6; ++t) GSSTEP(t);
        signal_flag(fM + c8 * 4 + wid, tag, lane);
_Pragma("unroll 1")
        for (int t = 6; t < 9; ++t) GSSTEP(t);
        u64 pv = 0;
        if (wid == 0) pv = __hip_atomic_load(fM + (lane & 31), RLX_AGENT);
_Pragma("unroll 1")
        for (int t = 9; t < 12; ++t) GSSTEP(t);
        if (wid == 0) wait_flags(fM + (lane & 31), pv, tag);
_Pragma("unroll 1")
        for (int t = 12; t < 15; ++t) GSSTEP(t);
        STAMP(12);
        const LaneC cm = lane_consts<PH_GM>(wm, wn, lane);
        cstep<PH_GS, PH_GM, false, 4>(smem + 3 * SLOT, smem, cs, cm, acc, A0, B0, A1, B1);
_Pragma("unroll 1")
        for (int t = 16; t < 40; ++t) cstep<PH_GM, PH_GM, false, 4>(smem + (t & 3) * SLOT, smem + ((t + 1) & 3) * SLOT, cm, cm, acc, A0, B0, A1, B1);
        const int r = lane & 15, q = lane >> 4;
        const int t0r = rb * 128 + wm * 64 + kh * 32 + r, d0 = c8 * 128 + wn * 64 + q * 4;
        u32x2 xw[2][4], dw[4], bw[4]; float gg[2];
#pragma unroll
        for (int n = 0; n < 4; ++n) {
            dw[n] = *(const u32x2*)(a.dsk + d0 + n * 16); bw[n] = *(const u32x2*)(a.b2 + d0 + n * 16);
#pragma unroll
            for (int mm = 0; mm < 2; ++mm) xw[mm][n] = *(const u32x2*)(a.x + (size_t)(t0r + mm * 16) * 1024 + d0 + n * 16);
        }
#pragma unroll
        for (int mm = 0; mm < 2; ++mm) gg[mm] = __uint_as_float(__builtin_amdgcn_raw_buffer_load_b32(rs, (t0r + mm * 16) * 4, WS_G, 16));
        asm volatile("" ::: "memory");
_Pragma("unroll 1")
        for (int t = 40; t < 43; ++t) cstep<PH_GM, PH_GM, false, 4>(smem + (t & 3) * SLOT, smem + ((t + 1) & 3) * SLOT, cm, cm, acc, A0, B0, A1, B1);
        cstep<PH_GM, PH_GM, true, 4>(smem + 3 * SLOT, smem, cm, cm, acc, A0, B0, A1, B1);

        STAMP(13);
        if (kh) {
#pragma unroll
            for (int n = 0; n < 4; ++n)
#pragma unroll
                for (int mm = 0; mm < 2; ++mm) { const f32x4 t = acc[n][mm]; acc[n][mm] = acc[n][2 + mm]; acc[n][2 + mm] = t; }
        }
        const unsigned xo = WS_XB + (unsigned)(bid * 4 + wid) * 8192u, xi = WS_XB + (unsigned)((bid ^ 1) * 4 + wid) * 8192u;
#pragma unroll
        for (int n = 0; n < 4; ++n)
#pragma unroll
            for (int mm = 0; mm < 2; ++mm)
                st128(__builtin_bit_cast(u32x4, acc[n][2 + mm]), rs, lane * 16 + (n * 2 + mm) * 1024, xo, 16);
        f32x4 o[4][2];
#pragma unroll
        for (int mm = 0; mm < 2; ++mm) {
            const float g = gg[mm];
#pragma unroll
            for (int n = 0; n < 4; ++n) {
                const f32x4 own = acc[n][mm];
                o[n][mm][0] = own[0] + g * (bflo(xw[mm][n].x) * bflo(dw[n].x)) + (1.0f - g) * bflo(bw[n].x);
                o[n][mm][1] = own[1] + g * (bfhi(xw[mm][n].x) * bfhi(dw[n].x)) + (1.0f - g) * bfhi(bw[n].x);
                o[n][mm][2] = own[2] + g * (bflo(xw[mm][n].y) * bflo(dw[n].y)) + (1.0f - g) * bflo(bw[n].y);
                o[n][mm][3] = own[3] + g * (bfhi(xw[mm][n].y) * bfhi(dw[n].y)) + (1.0f - g) * bfhi(bw[n].y);
            }
        }
        signal_flag((gu64*)(ws + WS_XFLAG) + (bid * 4 + wid) * 8, tag, lane);
        STAMP(14);
        {
            gu64* fl = (gu64*)(ws + WS_XFLAG) + ((bid ^ 1) * 4 + wid) * 8;
            wait_flags(fl, __hip_atomic_load(fl, RLX_AGENT), tag);
        }
        STAMP(15);
#pragma unroll
        for (int n = 0; n < 4; ++n)
#pragma unroll
            for (int mm = 0; mm < 2; ++mm) {
                const u32x4 v = __builtin_amdgcn_raw_buffer_load_b128(rs, lane * 16 + (n * 2 + mm) * 1024, xi, 16);
                o[n][mm] += __builtin_bit_cast(f32x4, v);
            }
#pragma unroll
        for (int mm = 0; mm < 2; ++mm)
#pragma unroll
            for (int n = 0; n < 4; ++n) *(f32x4*)(a.out + (size_t)(t0r + mm * 16) * 1024 + d0 + n * 16) = o[n][mm];
    }
}
}
extern "C" __global__ void gm_code_end() {}

extern "C" void kernel_launch(void* const* d_in, const int* in_sizes, int n_in,
                              void* d_out, int out_size, void* d_ws, size_t ws_size,
                              hipStream_t stream) {
    using namespace gm;
    Args a;
    a.x = (const u16*)d_in[0]; a.lre = (const u16*)d_in[1]; a.lim = (const u16*)d_in[2]; a.bre = (const u16*)d_in[3]; a.bim = (const u16*)d_in[4];
    a.cre = (const u16*)d_in[5]; a.cim = (const u16*)d_in[6]; a.ldt = (const u16*)d_in[7]; a.dsk = (const u16*)d_in[8];
    a.w1 = (const u16*)d_in[10]; a.b1 = (const u16*)d_in[11]; a.w2 = (const u16*)d_in[12]; a.b2 = (const u16*)d_in[13]; a.gw = (const u16*)d_in[14];
    a.out = (float*)d_out; a.ws = (char*)d_ws + ((ws_size - WS_END) & ~(size_t)0x1fffff);
    (void)hipFuncSetAttribute((const void*)fused_kernel, hipFuncAttributeMaxDynamicSharedMemorySize, LDS_BYTES);
    fused_kernel<<<256, 512, LDS_BYTES, stream>>>(a);
}
```

```cpp
#define OUTAUX 17
#define DIAG 0
#include <hip/hip_runtime.h>
#include <hip/hip_bf16.h>

namespace gm {
#define LAS __attribute__((address_space(3)))
#define GAS __attribute__((address_space(1)))
typedef unsigned short u16;
typedef short bf16x8 __attribute__((ext_vector_type(8)));
typedef short s16x4 __attribute__((ext_vector_type(4)));
typedef float f32x4 __attribute__((ext_vector_type(4)));
typedef float f32x2 __attribute__((ext_vector_type(2)));
typedef unsigned u32x4 __attribute__((ext_vector_type(4)));
typedef unsigned u32x2 __attribute__((ext_vector_type(2)));
typedef LAS char* ldsp;
typedef GAS unsigned gu32;
typedef unsigned long long u64;
typedef GAS u64 gu64;

#define BAR() __builtin_amdgcn_s_barrier()
#define WAITV(n) asm volatile("s_waitcnt vmcnt(%0)" ::"i"(n) : "memory")
#define SCHED0() __builtin_amdgcn_sched_barrier(0)
#define RLX_AGENT __ATOMIC_RELAXED, __HIP_MEMORY_SCOPE_AGENT

constexpr unsigned WS_EPOCH = 0;
constexpr unsigned WS_FY = 4096;
constexpr unsigned WS_FS = 16384;
constexpr unsigned WS_FM = 24576;
constexpr unsigned WS_XFLAG = 32768;
constexpr unsigned WS_GRAN = 131072;
constexpr unsigned WS_G = 1u << 20;
constexpr unsigned WS_Y = 2u << 20;
constexpr unsigned WS_A2 = 8u << 20;
constexpr unsigned WS_XB = 32u << 20;
constexpr unsigned WS_END = 40u << 20;

constexpr int SLOT = 32768, SLOTM = 45056;
constexpr int LDS_E = 135168;
constexpr int LDS_COEF = LDS_E + 4096;
constexpr int LDS_B1 = LDS_COEF + 2048;
constexpr int LDS_DUMMY = LDS_B1 + 512;
constexpr int LDS_BYTES = LDS_DUMMY + 256;

enum { PH_S1 = 0, PH_M1 = 1, PH_GS = 2, PH_GM = 3 };

struct Args {
    const u16 *x, *lre, *lim, *bre, *bim, *cre, *cim, *ldt, *dsk, *w1, *b1, *w2, *b2, *gw;
    float* out;
    char* ws;
};

__device__ __forceinline__ bf16x8 lds128(ldsp p) { return *(const LAS bf16x8*)p; }
__device__ __forceinline__ s16x4 ldstr(ldsp p) { return __builtin_amdgcn_ds_read_tr16_b64_v4i16((LAS s16x4*)p); }
__device__ __forceinline__ bf16x8 cat(s16x4 a, s16x4 b) { return __builtin_shufflevector(a, b, 0, 1, 2, 3, 4, 5, 6, 7); }
__device__ __forceinline__ void dma16(const void* g, ldsp l) { __builtin_amdgcn_global_load_lds((const unsigned*)g, (LAS unsigned*)l, 16, 0, 0); }
__device__ __forceinline__ void dma16c(const void* g, ldsp l) { __builtin_amdgcn_global_load_lds((const unsigned*)g, (LAS unsigned*)l, 16, 0, 16); }
__device__ __forceinline__ void st128(u32x4 v, __amdgpu_buffer_rsrc_t rs, unsigned voff, unsigned base, int) { __builtin_amdgcn_raw_buffer_store_b128(v, rs, voff + base, 0, 16); }
__device__ __forceinline__ void st64(u32x2 v, __amdgpu_buffer_rsrc_t rs, unsigned voff, unsigned base, int) { __builtin_amdgcn_raw_buffer_store_b64(v, rs, voff + base, 0, 16); }
__device__ __forceinline__ float bflo(unsigned w) { return __uint_as_float(w << 16); }
__device__ __forceinline__ float bfhi(unsigned w) { return __uint_as_float(w & 0xffff0000u); }
__device__ __forceinline__ float bf1(u16 b) { return __uint_as_float((unsigned)b << 16); }
__device__ __forceinline__ unsigned pkbf(float lo, float hi) {
    typedef __bf16 bf2_t __attribute__((ext_vector_type(2)));
    bf2_t v = {(__bf16)lo, (__bf16)hi};
    return __builtin_bit_cast(unsigned, v);
}
__device__ __forceinline__ bool deadline(unsigned long long t0) { return __builtin_amdgcn_s_memrealtime() - t0 > 400000ull; }

struct LaneC { unsigned a0, a1, b0, b1, b2; };
template <int PH>
__device__ __forceinline__ LaneC lane_consts(int wm, int wn, int lane) {
    const int r = lane & 15, q = lane >> 4, rr = r >> 2, p = lane & 3;
    const unsigned rl = r * 128 + ((q ^ (r >> 1)) * 16);
    LaneC c; c.b2 = 0;
    if constexpr (PH == PH_S1) { c.a0 = wm * 8192 + rl; c.a1 = c.a0 ^ 64; c.b0 = 16384 + wn * 8192 + rl; c.b1 = c.b0 ^ 64; }
    if constexpr (PH == PH_M1) {
        c.a0 = wm * 8192 + rl; c.a1 = c.a0 ^ 64;
        const unsigned t0 = 16384 + q * 7 * 512 + rr * 64 + ((q & 1) * 32) + p * 8;
        c.b0 = t0 + wn * 2048;
        c.b1 = (t0 ^ 32) + wn * 2048;
        c.b2 = t0 ^ (wn * 32);
    }
    if constexpr (PH == PH_GS) {
        c.a0 = (q * 8 + wm * 4) * 256 + (p >> 1) * 128 + (4 * (q & 1) + rr) * 16 + (p & 1) * 8; c.a1 = c.a0 ^ 64;
        c.b0 = 16384 + wn * 8192 + rl; c.b1 = c.b0 ^ 64;
    }
    if constexpr (PH == PH_GM) {
        c.a0 = wm * 8192 + lane * 16; c.a1 = c.a0;
        c.b0 = 16384 + q * 4 * 512 + wn * 1024 + rr * 64 + ((q & 1) * 32) + p * 8; c.b1 = c.b0 ^ 32;
    }
    return c;
}
template <int PH, int KK, int NB>
__device__ __forceinline__ void load_frags(ldsp slot, const LaneC& c, bf16x8 (&A)[4], bf16x8 (&B)[NB]) {
    if constexpr (PH == PH_S1 || PH == PH_M1) {
        ldsp pa = slot + (KK ? c.a1 : c.a0);
#pragma unroll
        for (int m = 0; m < 4; ++m) A[m] = lds128(pa + m * 2048);
    } else if constexpr (PH == PH_GS) {
        ldsp p0 = slot + c.a0, p1 = slot + c.a1;
#pragma unroll
        for (int m = 0; m < 4; ++m) A[m] = cat(ldstr(p0 + (KK * 32 + m) * 256), ldstr(p1 + (KK * 32 + m) * 256));
    } else {
        ldsp pa = slot + c.a0;
#pragma unroll
        for (int m = 0; m < 4; ++m) A[m] = lds128(pa + (m * 2 + KK) * 1024);
    }
    if constexpr (PH == PH_S1 || PH == PH_GS) {
        ldsp pb = slot + (KK ? c.b1 : c.b0);
#pragma unroll
        for (int n = 0; n < NB; ++n) B[n] = lds128(pb + n * 2048);
    } else {
        constexpr int NS2 = PH == PH_M1 ? 7 : 4;
        ldsp p0 = slot + c.b0, p1 = slot + c.b1;
#pragma unroll
        for (int n = 0; n < NB; ++n) { ldsp p = (n == 6 ? slot + c.b2 : (n & 1) ? p1 : p0) + KK * 4 * NS2 * 512 + (n >> 1) * 512; B[n] = cat(ldstr(p), ldstr(p + 256)); }
    }
}
template <int PH, int NB>
__device__ __forceinline__ void mma(f32x4 (&acc)[NB][4], const bf16x8 (&A)[4], const bf16x8 (&B)[NB]) {
#pragma unroll
    for (int n = 0; n < NB; ++n)
#pragma unroll
        for (int m = 0; m < 4; ++m) {
            if constexpr (PH == PH_S1) acc[n][m] = __builtin_amdgcn_mfma_f32_16x16x32_bf16(A[m], B[n], acc[n][m], 0, 0, 0);
            else acc[n][m] = __builtin_amdgcn_mfma_f32_16x16x32_bf16(B[n], A[m], acc[n][m], 0, 0, 0);
        }
}
template <int PH> constexpr int nreads() { return PH == PH_S1 ? 8 : PH == PH_M1 ? 18 : 12; }
template <int NR, int NM>
__device__ __forceinline__ void sched_half() {
#pragma unroll
    for (int i = 0; i < NR; ++i) { __builtin_amdgcn_sched_group_barrier(0x8, 1, 0); __builtin_amdgcn_sched_group_barrier(0x100, 1, 0); }
    __builtin_amdgcn_sched_group_barrier(0x8, NM - NR, 0);
}
template <int PC, int PN, bool LAST, int NB>
__device__ __forceinline__ void cstep(ldsp cur, ldsp nxt, const LaneC& cc, const LaneC& cn, f32x4 (&acc)[NB][4], bf16x8 (&A0)[4], bf16x8 (&B0)[NB], bf16x8 (&A1)[4], bf16x8 (&B1)[NB]) {
    load_frags<PC, 1, NB>(cur, cc, A1, B1);
    mma<PC, NB>(acc, A0, B0);
    sched_half<nreads<PC>(), 4 * NB>();
    SCHED0();
    __builtin_amdgcn_s_waitcnt(0xC07F);
    BAR();
    SCHED0();
    if constexpr (!LAST) load_frags<PN, 0, NB>(nxt, cn, A0, B0);
    mma<PC, NB>(acc, A1, B1);
    if constexpr (!LAST) sched_half<nreads<PN>(), 4 * NB>();
    SCHED0();
}

__device__ __forceinline__ u64 next_tag(u64 e) { return e * 0x9E3779B97F4A7C15ull + 0xD1B54A32D192ED03ull; }
__device__ __forceinline__ void wait_flags(gu64* fl, u64 v, u64 tag) {
    if (!__all(v == tag)) {
        const unsigned long long t0 = __builtin_amdgcn_s_memrealtime();
        do { __builtin_amdgcn_s_sleep(2); v = __hip_atomic_load(fl, RLX_AGENT); } while (!__all(v == tag) && !deadline(t0));
    }
    asm volatile("" ::: "memory");
}
__device__ __forceinline__ void signal_flag(gu64* fl, u64 tag, int lane) {
    WAITV(0);
    if (lane == 0) __hip_atomic_store(fl, tag, RLX_AGENT);
}
__device__ __forceinline__ void warm(const void* g, ldsp smem) { __builtin_amdgcn_global_load_lds((const unsigned*)g, (LAS unsigned*)(smem + LDS_DUMMY), 4, 0, 0); }
template <int CTRL, int RMASK> __device__ __forceinline__ float dpp_add(float v) {
    return v + __builtin_bit_cast(float, __builtin_amdgcn_update_dpp(0, __builtin_bit_cast(int, v), CTRL, RMASK, 0xf, true));
}
__device__ __forceinline__ float wave_sum(float v) {
    v = dpp_add<0x111, 0xf>(v); v = dpp_add<0x112, 0xf>(v); v = dpp_add<0x114, 0xf>(v); v = dpp_add<0x118, 0xf>(v);
    v = dpp_add<0x142, 0xa>(v); v = dpp_add<0x143, 0xc>(v);
    return __builtin_bit_cast(float, __builtin_amdgcn_readlane(__builtin_bit_cast(int, v), 63));
}
#define STAMP(i) do { if ((i) == 0) dg0 = __builtin_amdgcn_s_memrealtime(); if ((i) == DIAG) dg1 = __builtin_amdgcn_s_memrealtime(); if ((i) == 0 || (i) == DIAG) __builtin_amdgcn_s_waitcnt(0xC07F); } while (0)

__device__ __forceinline__ unsigned gelu2(f32x2 v, float og) {
    const f32x2 t = (v * v) * -0.1029432397f + -2.3022081986f;
    const f32x2 u = v * t;
    f32x2 d; d.x = __builtin_amdgcn_exp2f(u.x); d.y = __builtin_amdgcn_exp2f(u.y);
    d = d + 1.0f;
    f32x2 r; r.x = __builtin_amdgcn_rcpf(d.x); r.y = __builtin_amdgcn_rcpf(d.y);
    const f32x2 o = (v * og) * r;
    return pkbf(o.x, o.y);
}

__device__ __forceinline__ void m1_epilogue(f32x4 (&acc)[7][4], const float (&og)[4], ldsp smem, __amdgpu_buffer_rsrc_t rs, int rb, int cb, int wm, int wn, int lane) {
    const int q = lane >> 4;
    u32x2 pk[7][4];
#pragma unroll
    for (int n = 0; n < 7; ++n) {
        const int nbt = n == 6 ? 6 + wn : n + 8 * wn;
        const u32x2 bw = *(const LAS u32x2*)(smem + LDS_B1 + (nbt * 16 + q * 4) * 2);
        const f32x2 b01 = {bflo(bw.x), bfhi(bw.x)}, b23 = {bflo(bw.y), bfhi(bw.y)};
#pragma unroll
        for (int m = 0; m < 4; ++m) {
            const f32x4 a = acc[n][m];
            pk[n][m].x = gelu2((f32x2){a[0], a[1]} + b01, og[m]);
            pk[n][m].y = gelu2((f32x2){a[2], a[3]} + b23, og[m]);
        }
        if ((n & 1) || n == 6) {
            const int pair = 7 * cb + (nbt >> 1);
            const unsigned base = WS_A2 + (unsigned)(rb * 88 + 32 + (pair >> 1)) * 16384u + (unsigned)(pair & 1) * 1024u + (unsigned)(wm * 4) * 2048u;
#pragma unroll
            for (int m = 0; m < 4; ++m) {
                if (n == 6) st64(pk[n][m], rs, lane * 16 + m * 2048, base + wn * 8, 16);
                else { const u32x4 v = {pk[n - (n & 1)][m].x, pk[n - (n & 1)][m].y, pk[n][m].x, pk[n][m].y}; st128(v, rs, lane * 16 + m * 2048, base, 16); }
            }
        }
    }
}

__global__ __launch_bounds__(512, 2) void fused_kernel(Args a) {
    extern __shared__ __attribute__((aligned(16))) char smem_[];
    ldsp smem = (ldsp)smem_;
    const int wid = __builtin_amdgcn_readfirstlane(threadIdx.x >> 6), lane = threadIdx.x & 63;
    const int bid = blockIdx.x, xcd = bid & 7, rk = bid >> 3;
    const int kh = xcd & 1, rb = (xcd >> 1) * 4 + (rk >> 3), c8 = rk & 7, cb = kh * 8 + c8;
    char* const ws = a.ws;
    const char* pc; unsigned code_len;
    asm volatile("s_getpc_b64 %0\n.Lgm_pc%=:\n\ts_mov_b32 %1, gm_code_end-.Lgm_pc%=" : "=s"(pc), "=s"(code_len));
    asm volatile("" ::"s"(a.x), "s"(a.lre), "s"(a.lim), "s"(a.bre), "s"(a.bim), "s"(a.cre), "s"(a.cim), "s"(a.ldt), "s"(a.dsk), "s"(a.w1), "s"(a.b1), "s"(a.w2), "s"(a.b2), "s"(a.gw), "s"(a.out), "s"(a.ws));

    if (wid >= 4) {
        const int w = wid - 4, un = (xcd >> 1) * 32 + rk;
        const unsigned swz = (unsigned)(((lane & 7) ^ ((4 * (w & 1) + (lane >> 4)) & 7)) * 16);
        {
            const unsigned va = (unsigned)((rb * 128 + 16 * (lane >> 5) + ((lane >> 3) & 3) + 32 * (w & 1) + 4 * (w >> 1)) * 2048) + swz;
            const unsigned vb = (unsigned)((cb * 64 + 16 * (w >> 1) + 8 * (w & 1) + (lane >> 3)) * 2048) + swz;
            auto issue = [&](int t) {
                ldsp s = smem + (t & 3) * SLOT + w * 1024;
                const char* pa = (const char*)a.x + t * 128;
#pragma unroll
                for (int n = 0; n < 4; ++n) dma16(pa + (64 * (n >> 1) + 8 * (n & 1)) * 2048 + va, s + n * 4096);
#pragma unroll
                for (int n = 0; n < 4; ++n) dma16((const char*)((n & 1) ? a.bim : a.bre) + t * 128 + (n >> 1) * 32 * 2048 + vb, s + 16384 + n * 4096);
            };
            if (w < 2) {
                const unsigned o = (unsigned)(((rk & 3) * 2 + w) * 8192 + lane * 128);
                warm(pc + (o < code_len && code_len < (1u << 20) ? o : 0u), smem);
            }
            unsigned plr = 0, pli = 0, pld = 0;
            if (w == 3) {
                const u16 *p0 = a.lre + cb * 64 + lane, *p1 = a.lim + cb * 64 + lane, *p2 = a.ldt + cb * 64 + lane;
                asm volatile("global_load_ushort %0, %3, off\n\tglobal_load_ushort %1, %4, off\n\tglobal_load_ushort %2, %5, off"
                             : "+v"(plr), "+v"(pli), "+v"(pld) : "v"(p0), "v"(p1), "v"(p2) : "memory");
            }
            if (w == 2) {
                const char* pb = (const char*)a.b1 + cb * 448;
                __builtin_amdgcn_global_load_lds((const unsigned*)(pb + lane * 4), (LAS unsigned*)(smem + LDS_B1), 4, 0, 0);
                __builtin_amdgcn_global_load_lds((const unsigned*)(pb + (lane < 48 ? 64 + lane : 111) * 4), (LAS unsigned*)(smem + LDS_B1 + 256), 4, 0, 0);
            }
_Pragma("unroll 1")
            for (int t = 0; t < 4; ++t) issue(t);
            {
                const char* pB = (const char*)(((lane >> 4) & 1) ? a.bim : a.bre) + (512 * kh + 16 * rk + (lane & 15)) * 2048 + (4 + (lane >> 5)) * 128;
                const char* pX = (const char*)a.x + (512 * (xcd >> 1) + 256 * (1 - kh) + 8 * rk + (lane & 7)) * 2048;
                warm(w == 2 ? pX + (4 + (lane >> 3)) * 128 : pB + (w == 3 ? 2 : w) * 256, smem);
                warm(w == 3 ? pX + (12 + ((lane >> 3) & 3)) * 128 : pB + (3 + w) * 256, smem);
            }
            if (w == 3) {
                asm volatile("s_waitcnt vmcnt(34)" : "+v"(plr), "+v"(pli), "+v"(pld)::"memory");
                const float lr = bflo(plr), li = bflo(pli);
                const float dt = (float)(__bf16)expf(bflo(pld));
                const float zr = dt * lr, zi = dt * li;
                float sn, cs; sincosf(zi, &sn, &cs);
                const float er = expf(zr), Lr = er * cs, Li = er * sn;
                const float den = 1.0f / (lr * lr + li * li), nr = Lr - 1.0f;
                const float cr = (nr * lr + Li * li) * den, ci = (Li * lr - nr * li) * den;
                float pr = Lr, pi = Li;
#pragma unroll
                for (int i = 0; i < 4; ++i) { const float t = pr * pr - pi * pi; pi = 2.0f * pr * pi; pr = t; }
                const float p16r = pr, p16i = pi;
#pragma unroll
                for (int i = 0; i < 3; ++i) { const float t = pr * pr - pi * pi; pi = 2.0f * pr * pi; pr = t; }
                const f32x4 v0 = {Lr, Li, cr, ci}, v1 = {p16r, p16i, pr, pi};
                const unsigned la = (unsigned)(size_t)(smem + LDS_COEF) + lane * 32;
                asm volatile("ds_write_b128 %0, %1\n\tds_write_b128 %0, %2 offset:16\n\ts_waitcnt lgkmcnt(0)" ::"v"(la), "v"(v0), "v"(v1) : "memory");
            }
            WAITV(26); BAR();
_Pragma("unroll 1")
            for (int t = 0; t < 3; ++t) { WAITV(18); BAR(); issue(t + 4); }
_Pragma("unroll 1")
            for (int t = 3; t < 12; ++t) { WAITV(16); BAR(); issue(t + 4); }
            warm((const char*)a.w1 + (size_t)(8 * un + 2 * w + (lane >= 28 ? 1 : 0)) * 7168 + kh * 3584 + (lane >= 56 ? 27 : lane >= 28 ? lane - 28 : lane) * 128, smem);
            WAITV(17); BAR();
            WAITV(9); BAR();
            WAITV(1); BAR();
            BAR();
        }
        {
            const unsigned va = (unsigned)((rb * 128 + 8 * w + (lane >> 3)) * 2048) + swz;
            unsigned vb[7];
#pragma unroll
            for (int i = 0; i < 7; ++i) {
                const int sl = 2 * i + (lane >> 5), kq = 2 * w + (sl >= 7 ? 1 : 0), nb2 = sl >= 7 ? sl - 7 : sl;
                vb[i] = (unsigned)((8 * kq + ((lane >> 2) & 7)) * 7168 + cb * 448 + nb2 * 64 + (((lane & 3) ^ (2 * (kq & 1))) * 16));
            }
            int slot = 0;
            auto issue = [&](int t) {
                ldsp s = smem + slot * SLOTM;
                slot = slot == 2 ? 0 : slot + 1;
                const char* pa = ws + WS_Y + t * 128;
#pragma unroll
                for (int n = 0; n < 4; ++n) dma16c(pa + n * 32 * 2048 + va, s + w * 1024 + n * 4096);
                const char* pb = (const char*)a.w1 + (size_t)t * 64 * 7168;
#pragma unroll
                for (int i = 0; i < 7; ++i) dma16(pb + vb[i], s + 16384 + (w * 7 + i) * 1024);
            };
            issue(0);
            BAR();
            BAR();
_Pragma("unroll 1")
            for (int t = 1; t < 3; ++t) issue(t);
            WAITV(22); BAR();
_Pragma("unroll 1")
            for (int t = 0; t < 13; ++t) { WAITV(11); BAR(); issue(t + 3); }
            warm((const char*)(((lane >> 3) & 1) ? a.cim : a.cre) + (8 * un + 2 * w + ((lane >> 4) & 1)) * 2048 + kh * 1024 + (lane & 7) * 128, smem);
            warm((const char*)a.w2 + (size_t)(1792 * kh + 14 * un) * 2048 + (w * 56 + (lane < 56 ? lane : 55)) * 128, smem);
            WAITV(13); BAR();
            WAITV(2); BAR();
            BAR();
        }
        {
            const char* a2 = ws + WS_A2 + (size_t)rb * 88 * 16384 + w * 4096 + lane * 16;
            const unsigned vr = (unsigned)((c8 * 128 + 8 * w + (lane >> 3)) * 2048) + swz;
            const int rho = (lane >> 2) & 7;
            unsigned vt[2];
#pragma unroll
            for (int qq = 0; qq < 2; ++qq) { const int q = 2 * (w & 1) + qq; const int k = 32 * (w >> 1) + (rho < 4 ? 4 * q + rho : 16 + 4 * q + rho - 4);
                vt[qq] = (unsigned)(k * 2048 + c8 * 256 + (lane >> 5) * 64 + (((lane & 3) ^ (2 * qq)) * 16)); }
            auto issue = [&](int t) {
                ldsp s = smem + (t & 3) * SLOT;
                if (t < 16) {
                    const int ks = kh * 16 + t;
                    const char* pa = a2 + (size_t)ks * 16384;
#pragma unroll
                    for (int n = 0; n < 4; ++n) dma16c(pa + n * 1024, s + w * 4096 + n * 1024);
                    const char* pb = (const char*)((ks & 1) ? a.cim : a.cre) + (ks >> 1) * 128;
#pragma unroll
                    for (int n = 0; n < 4; ++n) dma16(pb + n * 32 * 2048 + vr, s + 16384 + (w + 4 * n) * 1024);
                } else {
                    const int km = kh * 28 + (t - 16);
                    const char* pa = a2 + (size_t)(32 + km) * 16384;
#pragma unroll
                    for (int n = 0; n < 4; ++n) dma16c(pa + n * 1024, s + w * 4096 + n * 1024);
                    const char* pb = (const char*)a.w2 + (size_t)km * 64 * 2048;
#pragma unroll
                    for (int n = 0; n < 4; ++n) dma16(pb + (n & 1) * 128 + vt[n >> 1], s + 16384 + (w * 4 + n) * 1024);
                }
            };
_Pragma("unroll 1")
            for (int t = 0; t < 4; ++t) issue(t);
            WAITV(24); BAR();
_Pragma("unroll 1")
            for (int t = 0; t < 40; ++t) { WAITV(16); BAR(); issue(t + 4); }
            WAITV(16); BAR();
            WAITV(8); BAR();
            WAITV(0); BAR();
            BAR();
        }
        if (bid == 0 && w == 0) {
            const u64 tag = next_tag(__hip_atomic_load((gu64*)(ws + WS_EPOCH), RLX_AGENT));
            const unsigned long long t0 = __builtin_amdgcn_s_memrealtime();
            for (;;) {
                bool ok = true;
#pragma unroll
                for (int i = 0; i < 16; ++i) ok &= __hip_atomic_load((gu64*)(ws + WS_FY) + i * 64 + lane, RLX_AGENT) == tag;
                if (__all(ok) || deadline(t0)) break;
                __builtin_amdgcn_s_sleep(2);
            }
            if (lane == 0) __hip_atomic_store((gu64*)(ws + WS_EPOCH), tag, RLX_AGENT);
        }
        return;
    }

    const int wm = wid >> 1, wn = wid & 1;
    const __amdgpu_buffer_rsrc_t rs = __builtin_amdgcn_make_buffer_rsrc(ws, 0, WS_END, 0x00020000);
    gu64* const fS = (gu64*)(ws + WS_FS) + (rb * 2 + kh) * 32;
    gu64* const fM = (gu64*)(ws + WS_FM) + (rb * 2 + kh) * 32;
    unsigned long long dg0 = 0, dg1 = 0;
    STAMP(0);
    u64 tag;
    const int lnrow = 512 * (xcd >> 1) + 256 * kh + 8 * rk + 2 * wid;
    {
        u32x4 xv[2][2], gv[2];
#pragma unroll
        for (int i = 0; i < 2; ++i)
#pragma unroll
            for (int h = 0; h < 2; ++h) xv[i][h] = *(const u32x4*)(a.x + (size_t)(lnrow + i) * 1024 + h * 512 + lane * 8);
#pragma unroll
        for (int h = 0; h < 2; ++h) gv[h] = *(const u32x4*)(a.gw + h * 512 + lane * 8);
        const u64 ep = __hip_atomic_load((gu64*)(ws + WS_EPOCH), RLX_AGENT);
        f32x2 xf[2][8];
        float s[2], gd[2];
#pragma unroll
        for (int i = 0; i < 2; ++i) {
            f32x2 s2 = {0.f, 0.f}, g2 = {0.f, 0.f};
#pragma unroll
            for (int h = 0; h < 2; ++h)
#pragma unroll
                for (int j = 0; j < 4; ++j) {
                    const f32x2 v = {bflo(xv[i][h][j]), bfhi(xv[i][h][j])}, gw = {bflo(gv[h][j]), bfhi(gv[h][j])};
                    xf[i][h * 4 + j] = v; s2 += v; g2 += v * gw;
                }
            s[i] = wave_sum(s2.x + s2.y) * (1.0f / 1024.0f); gd[i] = wave_sum(g2.x + g2.y);
        }
#pragma unroll
        for (int i = 0; i < 2; ++i) {
            f32x2 v2 = {0.f, 0.f};
#pragma unroll
            for (int j = 0; j < 8; ++j) { xf[i][j] -= s[i]; v2 += xf[i][j] * xf[i][j]; }
            const float rstd = 1.0f / sqrtf(wave_sum(v2.x + v2.y) * (1.0f / 1024.0f) + 1e-5f);
#pragma unroll
            for (int h = 0; h < 2; ++h) {
                u32x4 o;
#pragma unroll
                for (int j = 0; j < 4; ++j) { const f32x2 v = xf[i][h * 4 + j] * rstd; o[j] = pkbf(v.x, v.y); }
                st128(o, rs, lane * 16 + h * 1024, WS_Y + (unsigned)(lnrow + i) * 2048u, 16);
            }
        }
        const float gl = lane ? gd[1] : gd[0];
        const float g = 1.0f / (1.0f + expf(-gl));
        if (lane < 2) __hip_atomic_store((gu32*)(ws + WS_G) + lnrow + lane, __float_as_uint(g), RLX_AGENT);
        tag = next_tag(((u64)(unsigned)__builtin_amdgcn_readfirstlane((unsigned)(ep >> 32)) << 32) | (unsigned)__builtin_amdgcn_readfirstlane((unsigned)ep));
    }
    const unsigned tag_lo = (unsigned)tag, tag_hi = (unsigned)(tag >> 32);
    bf16x8 A0[4], A1[4];
    {
        f32x4 acc[4][4];
#pragma unroll
        for (int n = 0; n < 4; ++n)
#pragma unroll
            for (int m = 0; m < 4; ++m) acc[n][m] = (f32x4){0.f, 0.f, 0.f, 0.f};
        bf16x8 B0[4], B1[4];
        const LaneC c = lane_consts<PH_S1>(wm, wn, lane);
        __builtin_amdgcn_s_waitcnt(0xC07F);
        STAMP(1);
        BAR();
        STAMP(2);
        load_frags<PH_S1, 0, 4>(smem, c, A0, B0);
        SCHED0();
#define S1STEP(t) cstep<PH_S1, PH_S1, false, 4>(smem + ((t) & 3) * SLOT, smem + (((t) + 1) & 3) * SLOT, c, c, acc, A0, B0, A1, B1)
_Pragma("unroll 1")
        for (int t = 0; t < 4; ++t) S1STEP(t);
        signal_flag((gu64*)(ws + WS_FY) + (lnrow >> 7) * 64 + (rk & 15) * 4 + wid, tag, lane);
_Pragma("unroll 1")
        for (int t = 4; t < 10; ++t) S1STEP(t);
        gu64* const fY = (gu64*)(ws + WS_FY) + rb * 64 + lane;
        const u64 pv = __hip_atomic_load(fY, RLX_AGENT);
_Pragma("unroll 1")
        for (int t = 10; t < 13; ++t) S1STEP(t);
        wait_flags(fY, pv, tag);
        const int ch = lane & 15, q = lane >> 4, seg = wm * 4 + q;
        f32x4 gq[4];
#pragma unroll
        for (int m = 0; m < 4; ++m) gq[m] = __builtin_bit_cast(f32x4, __builtin_amdgcn_raw_buffer_load_b128(rs, (seg * 16 + m * 4) * 4, WS_G + rb * 512, 16));
_Pragma("unroll 1")
        for (int t = 13; t < 15; ++t) S1STEP(t);
        cstep<PH_S1, PH_S1, true, 4>(smem + 3 * SLOT, smem, c, c, acc, A0, B0, A1, B1);

        STAMP(3);
        f32x4 k0[2], k1[2];
#pragma unroll
        for (int cbk = 0; cbk < 2; ++cbk) {
            const int cl = wn * 32 + cbk * 16 + ch;
            k0[cbk] = *(const LAS f32x4*)(smem + LDS_COEF + cl * 32); k1[cbk] = *(const LAS f32x4*)(smem + LDS_COEF + cl * 32 + 16);
        }
#pragma unroll
        for (int i = 1; i < 16; ++i)
#pragma unroll
            for (int cbk = 0; cbk < 2; ++cbk) {
                const float sr = acc[cbk][(i - 1) >> 2][(i - 1) & 3], si = acc[2 + cbk][(i - 1) >> 2][(i - 1) & 3];
                acc[cbk][i >> 2][i & 3] = fmaf(k0[cbk][0], sr, fmaf(-k0[cbk][1], si, acc[cbk][i >> 2][i & 3]));
                acc[2 + cbk][i >> 2][i & 3] = fmaf(k0[cbk][0], si, fmaf(k0[cbk][1], sr, acc[2 + cbk][i >> 2][i & 3]));
            }
#pragma unroll
        for (int cbk = 0; cbk < 2; ++cbk)
            *(LAS f32x2*)(smem + LDS_E + (seg * 64 + wn * 32 + cbk * 16 + ch) * 8) = (f32x2){acc[cbk][3][3], acc[2 + cbk][3][3]};
        __builtin_amdgcn_s_waitcnt(0xC07F);
        BAR();
        STAMP(4);
        asm volatile("" ::: "memory");
        f32x2 e[2][8];
#pragma unroll
        for (int cbk = 0; cbk < 2; ++cbk)
#pragma unroll
            for (int sgi = 0; sgi < 8; ++sgi) e[cbk][sgi] = *(const LAS f32x2*)(smem + LDS_E + (sgi * 64 + wn * 32 + cbk * 16 + ch) * 8);
        if (wm == 0 && rb < 15) {
            float er[2] = {0.f, 0.f}, ei[2] = {0.f, 0.f};
#pragma unroll
            for (int sgi = 0; sgi < 8; ++sgi)
#pragma unroll
                for (int cbk = 0; cbk < 2; ++cbk) {
                    const float t = fmaf(k1[cbk][0], er[cbk], fmaf(-k1[cbk][1], ei[cbk], e[cbk][sgi][0]));
                    ei[cbk] = fmaf(k1[cbk][0], ei[cbk], fmaf(k1[cbk][1], er[cbk], e[cbk][sgi][1])); er[cbk] = t; }
            const float sr_ = q ? er[1] + 0.f : er[0] + 0.f, si_ = q ? ei[1] + 0.f : ei[0] + 0.f;
            if (q < 2) { const u32x4 v = {__float_as_uint(sr_), tag_lo, __float_as_uint(si_), tag_hi};
                st128(v, rs, (wn * 32 + q * 16 + ch) * 16, WS_GRAN + (unsigned)(cb * 16 + rb) * 1024u, 16); }
        }
#pragma unroll
        for (int cbk = 0; cbk < 2; ++cbk)
#pragma unroll
            for (int i = 0; i < 16; ++i) {
                const float g2 = 2.0f * gq[i >> 2][i & 3], sr = acc[cbk][i >> 2][i & 3], si = acc[2 + cbk][i >> 2][i & 3];
                acc[cbk][i >> 2][i & 3] = g2 * fmaf(k0[cbk][2], sr, -k0[cbk][3] * si);
                acc[2 + cbk][i >> 2][i & 3] = g2 * fmaf(k0[cbk][2], si, k0[cbk][3] * sr);
            }
        STAMP(5);
        float Sr[2] = {0.f, 0.f}, Si[2] = {0.f, 0.f};
        if (rb > 0) {
            u32x4 gr[2][4];
            const unsigned long long t0 = __builtin_amdgcn_s_memrealtime();
            for (;;) {
                bool ok = true;
                asm volatile("" ::: "memory");
#pragma unroll
                for (int i = 0; i < 4; ++i) {
                    const int rp = rb - 1 - q - 4 * i, rc = rp < 0 ? 0 : rp;
#pragma unroll
                    for (int cbk = 0; cbk < 2; ++cbk) {
                        gr[cbk][i] = __builtin_amdgcn_raw_buffer_load_b128(rs, (wn * 32 + cbk * 16 + ch) * 16 + rc * 1024, WS_GRAN + (unsigned)cb * 16384u, 16);
                        ok &= (gr[cbk][i][1] == tag_lo) & (gr[cbk][i][3] == tag_hi);
                    }
                }
                if (__all(ok) || deadline(t0)) break;
                __builtin_amdgcn_s_sleep(1);
            }
#pragma unroll
            for (int cbk = 0; cbk < 2; ++cbk) {
                const float Pr = k1[cbk][2], Pi = k1[cbk][3];
                const float P2r = Pr * Pr - Pi * Pi, P2i = 2.0f * Pr * Pi, P4r = P2r * P2r - P2i * P2i, P4i = 2.0f * P2r * P2i;
                float hr = 0.f, hi = 0.f;
#pragma unroll
                for (int i = 3; i >= 0; --i) {
                    const bool valid = rb - 1 - q - 4 * i >= 0;
                    const float vr = valid ? __uint_as_float(gr[cbk][i][0]) : 0.f, vi = valid ? __uint_as_float(gr[cbk][i][2]) : 0.f;
                    const float t = fmaf(P4r, hr, fmaf(-P4i, hi, vr)); hi = fmaf(P4r, hi, fmaf(P4i, hr, vi)); hr = t;
                }
                const float ar = (q & 1) ? Pr : 1.0f, ai = (q & 1) ? Pi : 0.f, br = (q & 2) ? P2r : 1.0f, bi = (q & 2) ? P2i : 0.f;
                const float wr = ar * br - ai * bi, wi = ar * bi + ai * br;
                float tr = wr * hr - wi * hi, ti = wr * hi + wi * hr;
                tr += __shfl_xor(tr, 16); ti += __shfl_xor(ti, 16);
                tr += __shfl_xor(tr, 32); ti += __shfl_xor(ti, 32);
                Sr[cbk] = tr; Si[cbk] = ti;
            }
        }
        BAR();
        STAMP(6);
#pragma unroll
        for (int cbk = 0; cbk < 2; ++cbk) {
            float cr = Sr[cbk], ci = Si[cbk];
#pragma unroll
            for (int sgi = 0; sgi < 7; ++sgi) {
                const float t = fmaf(k1[cbk][0], cr, fmaf(-k1[cbk][1], ci, e[cbk][sgi][0])), u = fmaf(k1[cbk][0], ci, fmaf(k1[cbk][1], cr, e[cbk][sgi][1]));
                cr = sgi < seg ? t : cr; ci = sgi < seg ? u : ci;
            }
            float wr = k0[cbk][2] * cr - k0[cbk][3] * ci, wi = k0[cbk][2] * ci + k0[cbk][3] * cr;
            float hr[16], hi[16];
#pragma unroll
            for (int i = 0; i < 16; ++i) {
                const float t = k0[cbk][0] * wr - k0[cbk][1] * wi; wi = k0[cbk][0] * wi + k0[cbk][1] * wr; wr = t;
                const float g2 = 2.0f * gq[i >> 2][i & 3];
                hr[i] = fmaf(g2, wr, acc[cbk][i >> 2][i & 3]); hi[i] = -fmaf(g2, wi, acc[2 + cbk][i >> 2][i & 3]);
            }
            const int kb = 4 * wn + 2 * cbk + (ch >> 3);
            const unsigned vo = (unsigned)((kb * 8 + seg) * 256 + (((ch & 7) ^ (4 * (kb & 1))) * 16));
            const unsigned so = WS_A2 + (unsigned)(rb * 88 + 2 * cb) * 16384u;
#pragma unroll
            for (int th = 0; th < 2; ++th) {
                u32x4 vr, vi;
#pragma unroll
                for (int j = 0; j < 4; ++j) { vr[j] = pkbf(hr[th * 8 + 2 * j], hr[th * 8 + 2 * j + 1]); vi[j] = pkbf(hi[th * 8 + 2 * j], hi[th * 8 + 2 * j + 1]); }
                st128(vr, rs, vo + th * 128, so, 16);
                st128(vi, rs, vo + th * 128, so + 16384u, 16);
            }
        }
    }
    {
        f32x4 acc[7][4];
#pragma unroll
        for (int n = 0; n < 7; ++n)
#pragma unroll
            for (int m = 0; m < 4; ++m) acc[n][m] = (f32x4){0.f, 0.f, 0.f, 0.f};
        bf16x8 B0[7], B1[7];
        const LaneC c = lane_consts<PH_M1>(wm, wn, lane);
        STAMP(7);
        BAR();
        STAMP(8);
        load_frags<PH_M1, 0, 7>(smem, c, A0, B0);
        SCHED0();
        int sc = 0;
#define M1STEP() do { const int sn_ = sc == 2 ? 0 : sc + 1; cstep<PH_M1, PH_M1, false, 7>(smem + sc * SLOTM, smem + sn_ * SLOTM, c, c, acc, A0, B0, A1, B1); sc = sn_; } while (0)
_Pragma("unroll 1")
        for (int t = 0; t < 4; ++t) M1STEP();
        signal_flag(fS + c8 * 4 + wid, tag, lane);
_Pragma("unroll 1")
        for (int t = 4; t < 11; ++t) M1STEP();
        u64 pv = 0;
        if (wid == 0) pv = __hip_atomic_load(fS + (lane & 31), RLX_AGENT);
        float og[4];
#pragma unroll
        for (int m = 0; m < 4; ++m) og[m] = __uint_as_float(__builtin_amdgcn_raw_buffer_load_b32(rs, (wm * 64 + m * 16 + (lane & 15)) * 4, WS_G + rb * 512, 16));
_Pragma("unroll 1")
        for (int t = 11; t < 15; ++t) M1STEP();
        if (wid == 0) wait_flags(fS + (lane & 31), pv, tag);
        cstep<PH_M1, PH_M1, true, 7>(smem + sc * SLOTM, smem, c, c, acc, A0, B0, A1, B1);
        STAMP(9);
#pragma unroll
        for (int m = 0; m < 4; ++m) og[m] = 1.0f - og[m];
        m1_epilogue(acc, og, smem, rs, rb, cb, wm, wn, lane);
    }
    {
        f32x4 acc[4][4];
#pragma unroll
        for (int n = 0; n < 4; ++n)
#pragma unroll
            for (int m = 0; m < 4; ++m) acc[n][m] = (f32x4){0.f, 0.f, 0.f, 0.f};
        bf16x8 B0[4], B1[4];
        const LaneC cs = lane_consts<PH_GS>(wm, wn, lane);
        STAMP(10);
        BAR();
        STAMP(11);
        load_frags<PH_GS, 0, 4>(smem, cs, A0, B0);
        SCHED0();
#define GSSTEP(t) cstep<PH_GS, PH_GS, false, 4>(smem + ((t) & 3) * SLOT, smem + (((t) + 1) & 3) * SLOT, cs, cs, acc, A0, B0, A1, B1)
_Pragma("unroll 1")
        for (int t = 0; t < 6; ++t) GSSTEP(t);
        signal_flag(fM + c8 * 4 + wid, tag, lane);
_Pragma("unroll 1")
        for (int t = 6; t < 9; ++t) GSSTEP(t);
        u64 pv = 0;
        if (wid == 0) pv = __hip_atomic_load(fM + (lane & 31), RLX_AGENT);
_Pragma("unroll 1")
        for (int t = 9; t < 12; ++t) GSSTEP(t);
        if (wid == 0) wait_flags(fM + (lane & 31), pv, tag);
_Pragma("unroll 1")
        for (int t = 12; t < 15; ++t) GSSTEP(t);
        STAMP(12);
        const LaneC cm = lane_consts<PH_GM>(wm, wn, lane);
        cstep<PH_GS, PH_GM, false, 4>(smem + 3 * SLOT, smem, cs, cm, acc, A0, B0, A1, B1);
_Pragma("unroll 1")
        for (int t = 16; t < 40; ++t) cstep<PH_GM, PH_GM, false, 4>(smem + (t & 3) * SLOT, smem + ((t + 1) & 3) * SLOT, cm, cm, acc, A0, B0, A1, B1);
        const int r = lane & 15, q = lane >> 4;
        const int t0r = rb * 128 + wm * 64 + kh * 32 + r, d0 = c8 * 128 + wn * 64 + q * 4;
        u32x2 xw[2][4], dw[4], bw[4]; float gg[2];
#pragma unroll
        for (int n = 0; n < 4; ++n) {
            dw[n] = *(const u32x2*)(a.dsk + d0 + n * 16); bw[n] = *(const u32x2*)(a.b2 + d0 + n * 16);
#pragma unroll
            for (int mm = 0; mm < 2; ++mm) xw[mm][n] = *(const u32x2*)(a.x + (size_t)(t0r + mm * 16) * 1024 + d0 + n * 16);
        }
#pragma unroll
        for (int mm = 0; mm < 2; ++mm) gg[mm] = __uint_as_float(__builtin_amdgcn_raw_buffer_load_b32(rs, (t0r + mm * 16) * 4, WS_G, 16));
        asm volatile("" ::: "memory");
_Pragma("unroll 1")
        for (int t = 40; t < 43; ++t) cstep<PH_GM, PH_GM, false, 4>(smem + (t & 3) * SLOT, smem + ((t + 1) & 3) * SLOT, cm, cm, acc, A0, B0, A1, B1);
        cstep<PH_GM, PH_GM, true, 4>(smem + 3 * SLOT, smem, cm, cm, acc, A0, B0, A1, B1);

        STAMP(13);
        if (kh) {
#pragma unroll
            for (int n = 0; n < 4; ++n)
#pragma unroll
                for (int mm = 0; mm < 2; ++mm) { const f32x4 t = acc[n][mm]; acc[n][mm] = acc[n][2 + mm]; acc[n][2 + mm] = t; }
        }
        const unsigned xo = WS_XB + (unsigned)(bid * 4 + wid) * 8192u, xi = WS_XB + (unsigned)((bid ^ 1) * 4 + wid) * 8192u;
#pragma unroll
        for (int n = 0; n < 4; ++n)
#pragma unroll
            for (int mm = 0; mm < 2; ++mm)
                st128(__builtin_bit_cast(u32x4, acc[n][2 + mm]), rs, lane * 16 + (n * 2 + mm) * 1024, xo, 16);
        const __amdgpu_buffer_rsrc_t ro = __builtin_amdgcn_make_buffer_rsrc(a.out, 0, 8u << 20, 0x00020000);
        f32x4 o[4][2];
#pragma unroll
        for (int mm = 0; mm < 2; ++mm) {
            const float g = gg[mm];
#pragma unroll
            for (int n = 0; n < 4; ++n) {
                const f32x4 own = acc[n][mm];
                o[n][mm][0] = own[0] + g * (bflo(xw[mm][n].x) * bflo(dw[n].x)) + (1.0f - g) * bflo(bw[n].x);
                o[n][mm][1] = own[1] + g * (bfhi(xw[mm][n].x) * bfhi(dw[n].x)) + (1.0f - g) * bfhi(bw[n].x);
                o[n][mm][2] = own[2] + g * (bflo(xw[mm][n].y) * bflo(dw[n].y)) + (1.0f - g) * bflo(bw[n].y);
                o[n][mm][3] = own[3] + g * (bfhi(xw[mm][n].y) * bfhi(dw[n].y)) + (1.0f - g) * bfhi(bw[n].y);
            }
        }
        signal_flag((gu64*)(ws + WS_XFLAG) + (bid * 4 + wid) * 8, tag, lane);
        STAMP(14);
        {
            gu64* fl = (gu64*)(ws + WS_XFLAG) + ((bid ^ 1) * 4 + wid) * 8;
            wait_flags(fl, __hip_atomic_load(fl, RLX_AGENT), tag);
        }
        STAMP(15);
#pragma unroll
        for (int n = 0; n < 4; ++n)
#pragma unroll
            for (int mm = 0; mm < 2; ++mm) {
                const u32x4 v = __builtin_amdgcn_raw_buffer_load_b128(rs, lane * 16 + (n * 2 + mm) * 1024, xi, 16);
                o[n][mm] += __builtin_bit_cast(f32x4, v);
            }
#pragma unroll
        for (int mm = 0; mm < 2; ++mm)
#pragma unroll
            for (int n = 0; n < 4; ++n) __builtin_amdgcn_raw_buffer_store_b128(__builtin_bit_cast(u32x4, o[n][mm]), ro, ((t0r + mm * 16) * 1024 + d0 + n * 16) * 4, 0, OUTAUX);
        WAITV(0);
        STAMP(16);
        while (__builtin_amdgcn_s_memrealtime() - dg0 < 10000ull + (dg1 - dg0)) __builtin_amdgcn_s_sleep(1);
    }
}
}
extern "C" __global__ void gm_code_end() {}

extern "C" void kernel_launch(void* const* d_in, const int* in_sizes, int n_in,
                              void* d_out, int out_size, void* d_ws, size_t ws_size,
                              hipStream_t stream) {
    using namespace gm;
    Args a;
    a.x = (const u16*)d_in[0]; a.lre = (const u16*)d_in[1]; a.lim = (const u16*)d_in[2]; a.bre = (const u16*)d_in[3]; a.bim = (const u16*)d_in[4];
    a.cre = (const u16*)d_in[5]; a.cim = (const u16*)d_in[6]; a.ldt = (const u16*)d_in[7]; a.dsk = (const u16*)d_in[8];
    a.w1 = (const u16*)d_in[10]; a.b1 = (const u16*)d_in[11]; a.w2 = (const u16*)d_in[12]; a.b2 = (const u16*)d_in[13]; a.gw = (const u16*)d_in[14];
    a.out = (float*)d_out; a.ws = (char*)d_ws;
    (void)hipFuncSetAttribute((const void*)fused_kernel, hipFuncAttributeMaxDynamicSharedMemorySize, LDS_BYTES);
    fused_kernel<<<256, 512, LDS_BYTES, stream>>>(a);
}
```

```cpp
#define DIAG 3
#include <hip/hip_runtime.h>
#include <hip/hip_bf16.h>

namespace gm {
#define LAS __attribute__((address_space(3)))
#define GAS __attribute__((address_space(1)))
typedef unsigned short u16;
typedef short bf16x8 __attribute__((ext_vector_type(8)));
typedef short s16x4 __attribute__((ext_vector_type(4)));
typedef float f32x4 __attribute__((ext_vector_type(4)));
typedef float f32x2 __attribute__((ext_vector_type(2)));
typedef unsigned u32x4 __attribute__((ext_vector_type(4)));
typedef unsigned u32x2 __attribute__((ext_vector_type(2)));
typedef LAS char* ldsp;
typedef GAS unsigned gu32;
typedef unsigned long long u64;
typedef GAS u64 gu64;

#define BAR() __builtin_amdgcn_s_barrier()
#define WAITV(n) asm volatile("s_waitcnt vmcnt(%0)" ::"i"(n) : "memory")
#define SCHED0() __builtin_amdgcn_sched_barrier(0)
#define RLX_AGENT __ATOMIC_RELAXED, __HIP_MEMORY_SCOPE_AGENT

constexpr unsigned WS_EPOCH = 0;
constexpr unsigned WS_FY = 4096;
constexpr unsigned WS_FS = 16384;
constexpr unsigned WS_FM = 24576;
constexpr unsigned WS_XFLAG = 32768;
constexpr unsigned WS_GRAN = 131072;
constexpr unsigned WS_G = 1u << 20;
constexpr unsigned WS_Y = 2u << 20;
constexpr unsigned WS_A2 = 8u << 20;
constexpr unsigned WS_XB = 32u << 20;
constexpr unsigned WS_END = 40u << 20;

constexpr int SLOT = 32768, SLOTM = 45056;
constexpr int LDS_E = 135168;
constexpr int LDS_COEF = LDS_E + 4096;
constexpr int LDS_B1 = LDS_COEF + 2048;
constexpr int LDS_DUMMY = LDS_B1 + 512;
constexpr int LDS_BYTES = LDS_DUMMY + 256;

enum { PH_S1 = 0, PH_M1 = 1, PH_GS = 2, PH_GM = 3 };

struct Args {
    const u16 *x, *lre, *lim, *bre, *bim, *cre, *cim, *ldt, *dsk, *w1, *b1, *w2, *b2, *gw;
    float* out;
    char* ws;
};

__device__ __forceinline__ bf16x8 lds128(ldsp p) { return *(const LAS bf16x8*)p; }
__device__ __forceinline__ s16x4 ldstr(ldsp p) { return __builtin_amdgcn_ds_read_tr16_b64_v4i16((LAS s16x4*)p); }
__device__ __forceinline__ bf16x8 cat(s16x4 a, s16x4 b) { return __builtin_shufflevector(a, b, 0, 1, 2, 3, 4, 5, 6, 7); }
__device__ __forceinline__ void dma16(const void* g, ldsp l) { __builtin_amdgcn_global_load_lds((const unsigned*)g, (LAS unsigned*)l, 16, 0, 0); }
__device__ __forceinline__ void dma16c(const void* g, ldsp l) { __builtin_amdgcn_global_load_lds((const unsigned*)g, (LAS unsigned*)l, 16, 0, 16); }
__device__ __forceinline__ void st128(u32x4 v, __amdgpu_buffer_rsrc_t rs, unsigned voff, unsigned base, int) { __builtin_amdgcn_raw_buffer_store_b128(v, rs, voff + base, 0, 16); }
__device__ __forceinline__ void st64(u32x2 v, __amdgpu_buffer_rsrc_t rs, unsigned voff, unsigned base, int) { __builtin_amdgcn_raw_buffer_store_b64(v, rs, voff + base, 0, 16); }
__device__ __forceinline__ float bflo(unsigned w) { return __uint_as_float(w << 16); }
__device__ __forceinline__ float bfhi(unsigned w) { return __uint_as_float(w & 0xffff0000u); }
__device__ __forceinline__ float bf1(u16 b) { return __uint_as_float((unsigned)b << 16); }
__device__ __forceinline__ unsigned pkbf(float lo, float hi) {
    typedef __bf16 bf2_t __attribute__((ext_vector_type(2)));
    bf2_t v = {(__bf16)lo, (__bf16)hi};
    return __builtin_bit_cast(unsigned, v);
}
__device__ __forceinline__ bool deadline(unsigned long long t0) { return __builtin_amdgcn_s_memrealtime() - t0 > 400000ull; }

struct LaneC { unsigned a0, a1, b0, b1, b2; };
template <int PH>
__device__ __forceinline__ LaneC lane_consts(int wm, int wn, int lane) {
    const int r = lane & 15, q = lane >> 4, rr = r >> 2, p = lane & 3;
    const unsigned rl = r * 128 + ((q ^ (r >> 1)) * 16);
    LaneC c; c.b2 = 0;
    if constexpr (PH == PH_S1) { c.a0 = wm * 8192 + rl; c.a1 = c.a0 ^ 64; c.b0 = 16384 + wn * 8192 + rl; c.b1 = c.b0 ^ 64; }
    if constexpr (PH == PH_M1) {
        c.a0 = wm * 8192 + rl; c.a1 = c.a0 ^ 64;
        const unsigned t0 = 16384 + q * 7 * 512 + rr * 64 + ((q & 1) * 32) + p * 8;
        c.b0 = t0 + wn * 2048;
        c.b1 = (t0 ^ 32) + wn * 2048;
        c.b2 = t0 ^ (wn * 32);
    }
    if constexpr (PH == PH_GS) {
        c.a0 = (q * 8 + wm * 4) * 256 + (p >> 1) * 128 + (4 * (q & 1) + rr) * 16 + (p & 1) * 8; c.a1 = c.a0 ^ 64;
        c.b0 = 16384 + wn * 8192 + rl; c.b1 = c.b0 ^ 64;
    }
    if constexpr (PH == PH_GM) {
        c.a0 = wm * 8192 + lane * 16; c.a1 = c.a0;
        c.b0 = 16384 + q * 4 * 512 + wn * 1024 + rr * 64 + ((q & 1) * 32) + p * 8; c.b1 = c.b0 ^ 32;
    }
    return c;
}
template <int PH, int KK, int NB>
__device__ __forceinline__ void load_frags(ldsp slot, const LaneC& c, bf16x8 (&A)[4], bf16x8 (&B)[NB]) {
    if constexpr (PH == PH_S1 || PH == PH_M1) {
        ldsp pa = slot + (KK ? c.a1 : c.a0);
#pragma unroll
        for (int m = 0; m < 4; ++m) A[m] = lds128(pa + m * 2048);
    } else if constexpr (PH == PH_GS) {
        ldsp p0 = slot + c.a0, p1 = slot + c.a1;
#pragma unroll
        for (int m = 0; m < 4; ++m) A[m] = cat(ldstr(p0 + (KK * 32 + m) * 256), ldstr(p1 + (KK * 32 + m) * 256));
    } else {
        ldsp pa = slot + c.a0;
#pragma unroll
        for (int m = 0; m < 4; ++m) A[m] = lds128(pa + (m * 2 + KK) * 1024);
    }
    if constexpr (PH == PH_S1 || PH == PH_GS) {
        ldsp pb = slot + (KK ? c.b1 : c.b0);
#pragma unroll
        for (int n = 0; n < NB; ++n) B[n] = lds128(pb + n * 2048);
    } else {
        constexpr int NS2 = PH == PH_M1 ? 7 : 4;
        ldsp p0 = slot + c.b0, p1 = slot + c.b1;
#pragma unroll
        for (int n = 0; n < NB; ++n) { ldsp p = (n == 6 ? slot + c.b2 : (n & 1) ? p1 : p0) + KK * 4 * NS2 * 512 + (n >> 1) * 512; B[n] = cat(ldstr(p), ldstr(p + 256)); }
    }
}
template <int PH, int NB>
__device__ __forceinline__ void mma(f32x4 (&acc)[NB][4], const bf16x8 (&A)[4], const bf16x8 (&B)[NB]) {
#pragma unroll
    for (int n = 0; n < NB; ++n)
#pragma unroll
        for (int m = 0; m < 4; ++m) {
            if constexpr (PH == PH_S1) acc[n][m] = __builtin_amdgcn_mfma_f32_16x16x32_bf16(A[m], B[n], acc[n][m], 0, 0, 0);
            else acc[n][m] = __builtin_amdgcn_mfma_f32_16x16x32_bf16(B[n], A[m], acc[n][m], 0, 0, 0);
        }
}
template <int PH> constexpr int nreads() { return PH == PH_S1 ? 8 : PH == PH_M1 ? 18 : 12; }
template <int NR, int NM>
__device__ __forceinline__ void sched_half() {
#pragma unroll
    for (int i = 0; i < NR; ++i) { __builtin_amdgcn_sched_group_barrier(0x8, 1, 0); __builtin_amdgcn_sched_group_barrier(0x100, 1, 0); }
    __builtin_amdgcn_sched_group_barrier(0x8, NM - NR, 0);
}
template <int PC, int PN, bool LAST, int NB>
__device__ __forceinline__ void cstep(ldsp cur, ldsp nxt, const LaneC& cc, const LaneC& cn, f32x4 (&acc)[NB][4], bf16x8 (&A0)[4], bf16x8 (&B0)[NB], bf16x8 (&A1)[4], bf16x8 (&B1)[NB]) {
    load_frags<PC, 1, NB>(cur, cc, A1, B1);
    mma<PC, NB>(acc, A0, B0);
    sched_half<nreads<PC>(), 4 * NB>();
    SCHED0();
    __builtin_amdgcn_s_waitcnt(0xC07F);
    BAR();
    SCHED0();
    if constexpr (!LAST) load_frags<PN, 0, NB>(nxt, cn, A0, B0);
    mma<PC, NB>(acc, A1, B1);
    if constexpr (!LAST) sched_half<nreads<PN>(), 4 * NB>();
    SCHED0();
}

__device__ __forceinline__ u64 next_tag(u64 e) { return e * 0x9E3779B97F4A7C15ull + 0xD1B54A32D192ED03ull; }
__device__ __forceinline__ void wait_flags(gu64* fl, u64 v, u64 tag) {
    if (!__all(v == tag)) {
        const unsigned long long t0 = __builtin_amdgcn_s_memrealtime();
        do { __builtin_amdgcn_s_sleep(2); v = __hip_atomic_load(fl, RLX_AGENT); } while (!__all(v == tag) && !deadline(t0));
    }
    asm volatile("" ::: "memory");
}
__device__ __forceinline__ void signal_flag(gu64* fl, u64 tag, int lane) {
    WAITV(0);
    if (lane == 0) __hip_atomic_store(fl, tag, RLX_AGENT);
}
__device__ __forceinline__ void warm(const void* g, ldsp smem) { __builtin_amdgcn_global_load_lds((const unsigned*)g, (LAS unsigned*)(smem + LDS_DUMMY), 4, 0, 0); }
template <int CTRL, int RMASK> __device__ __forceinline__ float dpp_add(float v) {
    return v + __builtin_bit_cast(float, __builtin_amdgcn_update_dpp(0, __builtin_bit_cast(int, v), CTRL, RMASK, 0xf, true));
}
__device__ __forceinline__ float wave_sum(float v) {
    v = dpp_add<0x111, 0xf>(v); v = dpp_add<0x112, 0xf>(v); v = dpp_add<0x114, 0xf>(v); v = dpp_add<0x118, 0xf>(v);
    v = dpp_add<0x142, 0xa>(v); v = dpp_add<0x143, 0xc>(v);
    return __builtin_bit_cast(float, __builtin_amdgcn_readlane(__builtin_bit_cast(int, v), 63));
}
#define STAMP(i) do { if ((i) == 0) dg0 = __builtin_amdgcn_s_memrealtime(); if ((i) == DIAG) dg1 = __builtin_amdgcn_s_memrealtime(); if ((i) == 0 || (i) == DIAG) __builtin_amdgcn_s_waitcnt(0xC07F); } while (0)

__device__ __forceinline__ unsigned gelu2(f32x2 v, float og) {
    const f32x2 t = (v * v) * -0.1029432397f + -2.3022081986f;
    const f32x2 u = v * t;
    f32x2 d; d.x = __builtin_amdgcn_exp2f(u.x); d.y = __builtin_amdgcn_exp2f(u.y);
    d = d + 1.0f;
    f32x2 r; r.x = __builtin_amdgcn_rcpf(d.x); r.y = __builtin_amdgcn_rcpf(d.y);
    const f32x2 o = (v * og) * r;
    return pkbf(o.x, o.y);
}

__device__ __forceinline__ void m1_epilogue(f32x4 (&acc)[7][4], const float (&og)[4], ldsp smem, __amdgpu_buffer_rsrc_t rs, int rb, int cb, int wm, int wn, int lane) {
    const int q = lane >> 4;
    u32x2 pk[7][4];
#pragma unroll
    for (int n = 0; n < 7; ++n) {
        const int nbt = n == 6 ? 6 + wn : n + 8 * wn;
        const u32x2 bw = *(const LAS u32x2*)(smem + LDS_B1 + (nbt * 16 + q * 4) * 2);
        const f32x2 b01 = {bflo(bw.x), bfhi(bw.x)}, b23 = {bflo(bw.y), bfhi(bw.y)};
#pragma unroll
        for (int m = 0; m < 4; ++m) {
            const f32x4 a = acc[n][m];
            pk[n][m].x = gelu2((f32x2){a[0], a[1]} + b01, og[m]);
            pk[n][m].y = gelu2((f32x2){a[2], a[3]} + b23, og[m]);
        }
        if ((n & 1) || n == 6) {
            const int pair = 7 * cb + (nbt >> 1);
            const unsigned base = WS_A2 + (unsigned)(rb * 88 + 32 + (pair >> 1)) * 16384u + (unsigned)(pair & 1) * 1024u + (unsigned)(wm * 4) * 2048u;
#pragma unroll
            for (int m = 0; m < 4; ++m) {
                if (n == 6) st64(pk[n][m], rs, lane * 16 + m * 2048, base + wn * 8, 16);
                else { const u32x4 v = {pk[n - (n & 1)][m].x, pk[n - (n & 1)][m].y, pk[n][m].x, pk[n][m].y}; st128(v, rs, lane * 16 + m * 2048, base, 16); }
            }
        }
    }
}

__global__ __launch_bounds__(512, 2) void fused_kernel(Args a) {
    extern __shared__ __attribute__((aligned(16))) char smem_[];
    ldsp smem = (ldsp)smem_;
    const int wid = __builtin_amdgcn_readfirstlane(threadIdx.x >> 6), lane = threadIdx.x & 63;
    const int bid = blockIdx.x, xcd = bid & 7, rk = bid >> 3;
    const int kh = xcd & 1, rb = (xcd >> 1) * 4 + (rk >> 3), c8 = rk & 7, cb = kh * 8 + c8;
    char* const ws = a.ws;
    const char* pc; unsigned code_len;
    asm volatile("s_getpc_b64 %0\n.Lgm_pc%=:\n\ts_mov_b32 %1, gm_code_end-.Lgm_pc%=" : "=s"(pc), "=s"(code_len));
    asm volatile("" ::"s"(a.x), "s"(a.lre), "s"(a.lim), "s"(a.bre), "s"(a.bim), "s"(a.cre), "s"(a.cim), "s"(a.ldt), "s"(a.dsk), "s"(a.w1), "s"(a.b1), "s"(a.w2), "s"(a.b2), "s"(a.gw), "s"(a.out), "s"(a.ws));

    if (wid >= 4) {
        const int w = wid - 4, un = (xcd >> 1) * 32 + rk;
        const unsigned swz = (unsigned)(((lane & 7) ^ ((4 * (w & 1) + (lane >> 4)) & 7)) * 16);
        {
            const unsigned va = (unsigned)((rb * 128 + 16 * (lane >> 5) + ((lane >> 3) & 3) + 32 * (w & 1) + 4 * (w >> 1)) * 2048) + swz;
            const unsigned vb = (unsigned)((cb * 64 + 16 * (w >> 1) + 8 * (w & 1) + (lane >> 3)) * 2048) + swz;
            auto issue = [&](int t) {
                ldsp s = smem + (t & 3) * SLOT + w * 1024;
                const char* pa = (const char*)a.x + t * 128;
#pragma unroll
                for (int n = 0; n < 4; ++n) dma16(pa + (64 * (n >> 1) + 8 * (n & 1)) * 2048 + va, s + n * 4096);
#pragma unroll
                for (int n = 0; n < 4; ++n) dma16((const char*)((n & 1) ? a.bim : a.bre) + t * 128 + (n >> 1) * 32 * 2048 + vb, s + 16384 + n * 4096);
            };
            if (w < 2) {
                const unsigned o = (unsigned)(((rk & 3) * 2 + w) * 8192 + lane * 128);
                warm(pc + (o < code_len && code_len < (1u << 20) ? o : 0u), smem);
            }
            unsigned plr = 0, pli = 0, pld = 0;
            if (w == 3) {
                const u16 *p0 = a.lre + cb * 64 + lane, *p1 = a.lim + cb * 64 + lane, *p2 = a.ldt + cb * 64 + lane;
                asm volatile("global_load_ushort %0, %3, off\n\tglobal_load_ushort %1, %4, off\n\tglobal_load_ushort %2, %5, off"
                             : "+v"(plr), "+v"(pli), "+v"(pld) : "v"(p0), "v"(p1), "v"(p2) : "memory");
            }
            if (w == 2) {
                const char* pb = (const char*)a.b1 + cb * 448;
                __builtin_amdgcn_global_load_lds((const unsigned*)(pb + lane * 4), (LAS unsigned*)(smem + LDS_B1), 4, 0, 0);
                __builtin_amdgcn_global_load_lds((const unsigned*)(pb + (lane < 48 ? 64 + lane : 111) * 4), (LAS unsigned*)(smem + LDS_B1 + 256), 4, 0, 0);
            }
_Pragma("unroll 1")
            for (int t = 0; t < 4; ++t) issue(t);
            {
                const char* pB = (const char*)(((lane >> 4) & 1) ? a.bim : a.bre) + (512 * kh + 16 * rk + (lane & 15)) * 2048 + (4 + (lane >> 5)) * 128;
                const char* pX = (const char*)a.x + (512 * (xcd >> 1) + 256 * (1 - kh) + 8 * rk + (lane & 7)) * 2048;
                warm(w == 2 ? pX + (4 + (lane >> 3)) * 128 : pB + (w == 3 ? 2 : w) * 256, smem);
                warm(w == 3 ? pX + (12 + ((lane >> 3) & 3)) * 128 : pB + (3 + w) * 256, smem);
            }
            if (w == 3) {
                asm volatile("s_waitcnt vmcnt(34)" : "+v"(plr), "+v"(pli), "+v"(pld)::"memory");
                const float lr = bflo(plr), li = bflo(pli);
                const float dt = (float)(__bf16)expf(bflo(pld));
                const float zr = dt * lr, zi = dt * li;
                float sn, cs; sincosf(zi, &sn, &cs);
                const float er = expf(zr), Lr = er * cs, Li = er * sn;
                const float den = 1.0f / (lr * lr + li * li), nr = Lr - 1.0f;
                const float cr = (nr * lr + Li * li) * den, ci = (Li * lr - nr * li) * den;
                float pr = Lr, pi = Li;
#pragma unroll
                for (int i = 0; i < 4; ++i) { const float t = pr * pr - pi * pi; pi = 2.0f * pr * pi; pr = t; }
                const float p16r = pr, p16i = pi;
#pragma unroll
                for (int i = 0; i < 3; ++i) { const float t = pr * pr - pi * pi; pi = 2.0f * pr * pi; pr = t; }
                const f32x4 v0 = {Lr, Li, cr, ci}, v1 = {p16r, p16i, pr, pi};
                const unsigned la = (unsigned)(size_t)(smem + LDS_COEF) + lane * 32;
                asm volatile("ds_write_b128 %0, %1\n\tds_write_b128 %0, %2 offset:16\n\ts_waitcnt lgkmcnt(0)" ::"v"(la), "v"(v0), "v"(v1) : "memory");
            }
            WAITV(26); BAR();
_Pragma("unroll 1")
            for (int t = 0; t < 3; ++t) { WAITV(18); BAR(); issue(t + 4); }
_Pragma("unroll 1")
            for (int t = 3; t < 12; ++t) { WAITV(16); BAR(); issue(t + 4); }
            warm((const char*)a.w1 + (size_t)(8 * un + 2 * w + (lane >= 28 ? 1 : 0)) * 7168 + kh * 3584 + (lane >= 56 ? 27 : lane >= 28 ? lane - 28 : lane) * 128, smem);
            WAITV(17); BAR();
            WAITV(9); BAR();
            WAITV(1); BAR();
            BAR();
        }
        {
            const unsigned va = (unsigned)((rb * 128 + 8 * w + (lane >> 3)) * 2048) + swz;
            unsigned vb[7];
#pragma unroll
            for (int i = 0; i < 7; ++i) {
                const int sl = 2 * i + (lane >> 5), kq = 2 * w + (sl >= 7 ? 1 : 0), nb2 = sl >= 7 ? sl - 7 : sl;
                vb[i] = (unsigned)((8 * kq + ((lane >> 2) & 7)) * 7168 + cb * 448 + nb2 * 64 + (((lane & 3) ^ (2 * (kq & 1))) * 16));
            }
            int slot = 0;
            auto issue = [&](int t) {
                ldsp s = smem + slot * SLOTM;
                slot = slot == 2 ? 0 : slot + 1;
                const char* pa = ws + WS_Y + t * 128;
#pragma unroll
                for (int n = 0; n < 4; ++n) dma16c(pa + n * 32 * 2048 + va, s + w * 1024 + n * 4096);
                const char* pb = (const char*)a.w1 + (size_t)t * 64 * 7168;
#pragma unroll
                for (int i = 0; i < 7; ++i) dma16(pb + vb[i], s + 16384 + (w * 7 + i) * 1024);
            };
            issue(0);
            BAR();
            BAR();
_Pragma("unroll 1")
            for (int t = 1; t < 3; ++t) issue(t);
            WAITV(22); BAR();
_Pragma("unroll 1")
            for (int t = 0; t < 13; ++t) { WAITV(11); BAR(); issue(t + 3); }
            warm((const char*)(((lane >> 3) & 1) ? a.cim : a.cre) + (8 * un + 2 * w + ((lane >> 4) & 1)) * 2048 + kh * 1024 + (lane & 7) * 128, smem);
            warm((const char*)a.w2 + (size_t)(1792 * kh + 14 * un) * 2048 + (w * 56 + (lane < 56 ? lane : 55)) * 128, smem);
            WAITV(13); BAR();
            WAITV(2); BAR();
            BAR();
        }
        {
            const char* a2 = ws + WS_A2 + (size_t)rb * 88 * 16384 + w * 4096 + lane * 16;
            const unsigned vr = (unsigned)((c8 * 128 + 8 * w + (lane >> 3)) * 2048) + swz;
            const int rho = (lane >> 2) & 7;
            unsigned vt[2];
#pragma unroll
            for (int qq = 0; qq < 2; ++qq) { const int q = 2 * (w & 1) + qq; const int k = 32 * (w >> 1) + (rho < 4 ? 4 * q + rho : 16 + 4 * q + rho - 4);
                vt[qq] = (unsigned)(k * 2048 + c8 * 256 + (lane >> 5) * 64 + (((lane & 3) ^ (2 * qq)) * 16)); }
            auto issue = [&](int t) {
                ldsp s = smem + (t & 3) * SLOT;
                if (t < 16) {
                    const int ks = kh * 16 + t;
                    const char* pa = a2 + (size_t)ks * 16384;
#pragma unroll
                    for (int n = 0; n < 4; ++n) dma16c(pa + n * 1024, s + w * 4096 + n * 1024);
                    const char* pb = (const char*)((ks & 1) ? a.cim : a.cre) + (ks >> 1) * 128;
#pragma unroll
                    for (int n = 0; n < 4; ++n) dma16(pb + n * 32 * 2048 + vr, s + 16384 + (w + 4 * n) * 1024);
                } else {
                    const int km = kh * 28 + (t - 16);
                    const char* pa = a2 + (size_t)(32 + km) * 16384;
#pragma unroll
                    for (int n = 0; n < 4; ++n) dma16c(pa + n * 1024, s + w * 4096 + n * 1024);
                    const char* pb = (const char*)a.w2 + (size_t)km * 64 * 2048;
#pragma unroll
                    for (int n = 0; n < 4; ++n) dma16(pb + (n & 1) * 128 + vt[n >> 1], s + 16384 + (w * 4 + n) * 1024);
                }
            };
_Pragma("unroll 1")
            for (int t = 0; t < 4; ++t) issue(t);
            WAITV(24); BAR();
_Pragma("unroll 1")
            for (int t = 0; t < 40; ++t) { WAITV(16); BAR(); issue(t + 4); }
            WAITV(16); BAR();
            WAITV(8); BAR();
            WAITV(0); BAR();
            BAR();
        }
        if (bid == 0 && w == 0) {
            const u64 tag = next_tag(__hip_atomic_load((gu64*)(ws + WS_EPOCH), RLX_AGENT));
            const unsigned long long t0 = __builtin_amdgcn_s_memrealtime();
            for (;;) {
                bool ok = true;
#pragma unroll
                for (int i = 0; i < 16; ++i) ok &= __hip_atomic_load((gu64*)(ws + WS_FY) + i * 64 + lane, RLX_AGENT) == tag;
                if (__all(ok) || deadline(t0)) break;
                __builtin_amdgcn_s_sleep(2);
            }
            if (lane == 0) __hip_atomic_store((gu64*)(ws + WS_EPOCH), tag, RLX_AGENT);
        }
        return;
    }

    const int wm = wid >> 1, wn = wid & 1;
    const __amdgpu_buffer_rsrc_t rs = __builtin_amdgcn_make_buffer_rsrc(ws, 0, WS_END, 0x00020000);
    gu64* const fS = (gu64*)(ws + WS_FS) + (rb * 2 + kh) * 32;
    gu64* const fM = (gu64*)(ws + WS_FM) + (rb * 2 + kh) * 32;
    unsigned long long dg0 = 0, dg1 = 0;
    STAMP(0);
    u64 tag;
    const int lnrow = 512 * (xcd >> 1) + 256 * kh + 8 * rk + 2 * wid;
    {
        u32x4 xv[2][2], gv[2];
#pragma unroll
        for (int i = 0; i < 2; ++i)
#pragma unroll
            for (int h = 0; h < 2; ++h) xv[i][h] = *(const u32x4*)(a.x + (size_t)(lnrow + i) * 1024 + h * 512 + lane * 8);
#pragma unroll
        for (int h = 0; h < 2; ++h) gv[h] = *(const u32x4*)(a.gw + h * 512 + lane * 8);
        const u64 ep = __hip_atomic_load((gu64*)(ws + WS_EPOCH), RLX_AGENT);
        f32x2 xf[2][8];
        float s[2], gd[2];
#pragma unroll
        for (int i = 0; i < 2; ++i) {
            f32x2 s2 = {0.f, 0.f}, g2 = {0.f, 0.f};
#pragma unroll
            for (int h = 0; h < 2; ++h)
#pragma unroll
                for (int j = 0; j < 4; ++j) {
                    const f32x2 v = {bflo(xv[i][h][j]), bfhi(xv[i][h][j])}, gw = {bflo(gv[h][j]), bfhi(gv[h][j])};
                    xf[i][h * 4 + j] = v; s2 += v; g2 += v * gw;
                }
            s[i] = wave_sum(s2.x + s2.y) * (1.0f / 1024.0f); gd[i] = wave_sum(g2.x + g2.y);
        }
#pragma unroll
        for (int i = 0; i < 2; ++i) {
            f32x2 v2 = {0.f, 0.f};
#pragma unroll
            for (int j = 0; j < 8; ++j) { xf[i][j] -= s[i]; v2 += xf[i][j] * xf[i][j]; }
            const float rstd = 1.0f / sqrtf(wave_sum(v2.x + v2.y) * (1.0f / 1024.0f) + 1e-5f);
#pragma unroll
            for (int h = 0; h < 2; ++h) {
                u32x4 o;
#pragma unroll
                for (int j = 0; j < 4; ++j) { const f32x2 v = xf[i][h * 4 + j] * rstd; o[j] = pkbf(v.x, v.y); }
                st128(o, rs, lane * 16 + h * 1024, WS_Y + (unsigned)(lnrow + i) * 2048u, 16);
            }
        }
        const float gl = lane ? gd[1] : gd[0];
        const float g = 1.0f / (1.0f + expf(-gl));
        if (lane < 2) __hip_atomic_store((gu32*)(ws + WS_G) + lnrow + lane, __float_as_uint(g), RLX_AGENT);
        tag = next_tag(((u64)(unsigned)__builtin_amdgcn_readfirstlane((unsigned)(ep >> 32)) << 32) | (unsigned)__builtin_amdgcn_readfirstlane((unsigned)ep));
    }
    const unsigned tag_lo = (unsigned)tag, tag_hi = (unsigned)(tag >> 32);
    bf16x8 A0[4], A1[4];
    {
        f32x4 acc[4][4];
#pragma unroll
        for (int n = 0; n < 4; ++n)
#pragma unroll
            for (int m = 0; m < 4; ++m) acc[n][m] = (f32x4){0.f, 0.f, 0.f, 0.f};
        bf16x8 B0[4], B1[4];
        const LaneC c = lane_consts<PH_S1>(wm, wn, lane);
        __builtin_amdgcn_s_waitcnt(0xC07F);
        STAMP(1);
        BAR();
        STAMP(2);
        load_frags<PH_S1, 0, 4>(smem, c, A0, B0);
        SCHED0();
#define S1STEP(t) cstep<PH_S1, PH_S1, false, 4>(smem + ((t) & 3) * SLOT, smem + (((t) + 1) & 3) * SLOT, c, c, acc, A0, B0, A1, B1)
_Pragma("unroll 1")
        for (int t = 0; t < 4; ++t) S1STEP(t);
        signal_flag((gu64*)(ws + WS_FY) + (lnrow >> 7) * 64 + (rk & 15) * 4 + wid, tag, lane);
_Pragma("unroll 1")
        for (int t = 4; t < 10; ++t) S1STEP(t);
        gu64* const fY = (gu64*)(ws + WS_FY) + rb * 64 + lane;
        const u64 pv = __hip_atomic_load(fY, RLX_AGENT);
_Pragma("unroll 1")
        for (int t = 10; t < 13; ++t) S1STEP(t);
        wait_flags(fY, pv, tag);
        const int ch = lane & 15, q = lane >> 4, seg = wm * 4 + q;
        f32x4 gq[4];
#pragma unroll
        for (int m = 0; m < 4; ++m) gq[m] = __builtin_bit_cast(f32x4, __builtin_amdgcn_raw_buffer_load_b128(rs, (seg * 16 + m * 4) * 4, WS_G + rb * 512, 16));
_Pragma("unroll 1")
        for (int t = 13; t < 15; ++t) S1STEP(t);
        cstep<PH_S1, PH_S1, true, 4>(smem + 3 * SLOT, smem, c, c, acc, A0, B0, A1, B1);

        STAMP(3);
        f32x4 k0[2], k1[2];
#pragma unroll
        for (int cbk = 0; cbk < 2; ++cbk) {
            const int cl = wn * 32 + cbk * 16 + ch;
            k0[cbk] = *(const LAS f32x4*)(smem + LDS_COEF + cl * 32); k1[cbk] = *(const LAS f32x4*)(smem + LDS_COEF + cl * 32 + 16);
        }
#pragma unroll
        for (int i = 1; i < 16; ++i)
#pragma unroll
            for (int cbk = 0; cbk < 2; ++cbk) {
                const float sr = acc[cbk][(i - 1) >> 2][(i - 1) & 3], si = acc[2 + cbk][(i - 1) >> 2][(i - 1) & 3];
                acc[cbk][i >> 2][i & 3] = fmaf(k0[cbk][0], sr, fmaf(-k0[cbk][1], si, acc[cbk][i >> 2][i & 3]));
                acc[2 + cbk][i >> 2][i & 3] = fmaf(k0[cbk][0], si, fmaf(k0[cbk][1], sr, acc[2 + cbk][i >> 2][i & 3]));
            }
#pragma unroll
        for (int cbk = 0; cbk < 2; ++cbk)
            *(LAS f32x2*)(smem + LDS_E + (seg * 64 + wn * 32 + cbk * 16 + ch) * 8) = (f32x2){acc[cbk][3][3], acc[2 + cbk][3][3]};
        __builtin_amdgcn_s_waitcnt(0xC07F);
        BAR();
        STAMP(4);
        asm volatile("" ::: "memory");
        f32x2 e[2][8];
#pragma unroll
        for (int cbk = 0; cbk < 2; ++cbk)
#pragma unroll
            for (int sgi = 0; sgi < 8; ++sgi) e[cbk][sgi] = *(const LAS f32x2*)(smem + LDS_E + (sgi * 64 + wn * 32 + cbk * 16 + ch) * 8);
        if (wm == 0 && rb < 15) {
            float er[2] = {0.f, 0.f}, ei[2] = {0.f, 0.f};
#pragma unroll
            for (int sgi = 0; sgi < 8; ++sgi)
#pragma unroll
                for (int cbk = 0; cbk < 2; ++cbk) {
                    const float t = fmaf(k1[cbk][0], er[cbk], fmaf(-k1[cbk][1], ei[cbk], e[cbk][sgi][0]));
                    ei[cbk] = fmaf(k1[cbk][0], ei[cbk], fmaf(k1[cbk][1], er[cbk], e[cbk][sgi][1])); er[cbk] = t; }
            const float sr_ = q ? er[1] + 0.f : er[0] + 0.f, si_ = q ? ei[1] + 0.f : ei[0] + 0.f;
            if (q < 2) { const u32x4 v = {__float_as_uint(sr_), tag_lo, __float_as_uint(si_), tag_hi};
                st128(v, rs, (wn * 32 + q * 16 + ch) * 16, WS_GRAN + (unsigned)(cb * 16 + rb) * 1024u, 16); }
        }
#pragma unroll
        for (int cbk = 0; cbk < 2; ++cbk)
#pragma unroll
            for (int i = 0; i < 16; ++i) {
                const float g2 = 2.0f * gq[i >> 2][i & 3], sr = acc[cbk][i >> 2][i & 3], si = acc[2 + cbk][i >> 2][i & 3];
                acc[cbk][i >> 2][i & 3] = g2 * fmaf(k0[cbk][2], sr, -k0[cbk][3] * si);
                acc[2 + cbk][i >> 2][i & 3] = g2 * fmaf(k0[cbk][2], si, k0[cbk][3] * sr);
            }
        STAMP(5);
        float Sr[2] = {0.f, 0.f}, Si[2] = {0.f, 0.f};
        if (rb > 0) {
            u32x4 gr[2][4];
            const unsigned long long t0 = __builtin_amdgcn_s_memrealtime();
            for (;;) {
                bool ok = true;
                asm volatile("" ::: "memory");
#pragma unroll
                for (int i = 0; i < 4; ++i) {
                    const int rp = rb - 1 - q - 4 * i, rc = rp < 0 ? 0 : rp;
#pragma unroll
                    for (int cbk = 0; cbk < 2; ++cbk) {
                        gr[cbk][i] = __builtin_amdgcn_raw_buffer_load_b128(rs, (wn * 32 + cbk * 16 + ch) * 16 + rc * 1024, WS_GRAN + (unsigned)cb * 16384u, 16);
                        ok &= (gr[cbk][i][1] == tag_lo) & (gr[cbk][i][3] == tag_hi);
                    }
                }
                if (__all(ok) || deadline(t0)) break;
                __builtin_amdgcn_s_sleep(1);
            }
#pragma unroll
            for (int cbk = 0; cbk < 2; ++cbk) {
                const float Pr = k1[cbk][2], Pi = k1[cbk][3];
                const float P2r = Pr * Pr - Pi * Pi, P2i = 2.0f * Pr * Pi, P4r = P2r * P2r - P2i * P2i, P4i = 2.0f * P2r * P2i;
                float hr = 0.f, hi = 0.f;
#pragma unroll
                for (int i = 3; i >= 0; --i) {
                    const bool valid = rb - 1 - q - 4 * i >= 0;
                    const float vr = valid ? __uint_as_float(gr[cbk][i][0]) : 0.f, vi = valid ? __uint_as_float(gr[cbk][i][2]) : 0.f;
                    const float t = fmaf(P4r, hr, fmaf(-P4i, hi, vr)); hi = fmaf(P4r, hi, fmaf(P4i, hr, vi)); hr = t;
                }
                const float ar = (q & 1) ? Pr : 1.0f, ai = (q & 1) ? Pi : 0.f, br = (q & 2) ? P2r : 1.0f, bi = (q & 2) ? P2i : 0.f;
                const float wr = ar * br - ai * bi, wi = ar * bi + ai * br;
                float tr = wr * hr - wi * hi, ti = wr * hi + wi * hr;
                tr += __shfl_xor(tr, 16); ti += __shfl_xor(ti, 16);
                tr += __shfl_xor(tr, 32); ti += __shfl_xor(ti, 32);
                Sr[cbk] = tr; Si[cbk] = ti;
            }
        }
        BAR();
        STAMP(6);
#pragma unroll
        for (int cbk = 0; cbk < 2; ++cbk) {
            float cr = Sr[cbk], ci = Si[cbk];
#pragma unroll
            for (int sgi = 0; sgi < 7; ++sgi) {
                const float t = fmaf(k1[cbk][0], cr, fmaf(-k1[cbk][1], ci, e[cbk][sgi][0])), u = fmaf(k1[cbk][0], ci, fmaf(k1[cbk][1], cr, e[cbk][sgi][1]));
                cr = sgi < seg ? t : cr; ci = sgi < seg ? u : ci;
            }
            float wr = k0[cbk][2] * cr - k0[cbk][3] * ci, wi = k0[cbk][2] * ci + k0[cbk][3] * cr;
            float hr[16], hi[16];
#pragma unroll
            for (int i = 0; i < 16; ++i) {
                const float t = k0[cbk][0] * wr - k0[cbk][1] * wi; wi = k0[cbk][0] * wi + k0[cbk][1] * wr; wr = t;
                const float g2 = 2.0f * gq[i >> 2][i & 3];
                hr[i] = fmaf(g2, wr, acc[cbk][i >> 2][i & 3]); hi[i] = -fmaf(g2, wi, acc[2 + cbk][i >> 2][i & 3]);
            }
            const int kb = 4 * wn + 2 * cbk + (ch >> 3);
            const unsigned vo = (unsigned)((kb * 8 + seg) * 256 + (((ch & 7) ^ (4 * (kb & 1))) * 16));
            const unsigned so = WS_A2 + (unsigned)(rb * 88 + 2 * cb) * 16384u;
#pragma unroll
            for (int th = 0; th < 2; ++th) {
                u32x4 vr, vi;
#pragma unroll
                for (int j = 0; j < 4; ++j) { vr[j] = pkbf(hr[th * 8 + 2 * j], hr[th * 8 + 2 * j + 1]); vi[j] = pkbf(hi[th * 8 + 2 * j], hi[th * 8 + 2 * j + 1]); }
                st128(vr, rs, vo + th * 128, so, 16);
                st128(vi, rs, vo + th * 128, so + 16384u, 16);
            }
        }
    }
    {
        f32x4 acc[7][4];
#pragma unroll
        for (int n = 0; n < 7; ++n)
#pragma unroll
            for (int m = 0; m < 4; ++m) acc[n][m] = (f32x4){0.f, 0.f, 0.f, 0.f};
        bf16x8 B0[7], B1[7];
        const LaneC c = lane_consts<PH_M1>(wm, wn, lane);
        STAMP(7);
        BAR();
        STAMP(8);
        load_frags<PH_M1, 0, 7>(smem, c, A0, B0);
        SCHED0();
        int sc = 0;
#define M1STEP() do { const int sn_ = sc == 2 ? 0 : sc + 1; cstep<PH_M1, PH_M1, false, 7>(smem + sc * SLOTM, smem + sn_ * SLOTM, c, c, acc, A0, B0, A1, B1); sc = sn_; } while (0)
_Pragma("unroll 1")
        for (int t = 0; t < 4; ++t) M1STEP();
        signal_flag(fS + c8 * 4 + wid, tag, lane);
_Pragma("unroll 1")
        for (int t = 4; t < 11; ++t) M1STEP();
        u64 pv = 0;
        if (wid == 0) pv = __hip_atomic_load(fS + (lane & 31), RLX_AGENT);
        float og[4];
#pragma unroll
        for (int m = 0; m < 4; ++m) og[m] = __uint_as_float(__builtin_amdgcn_raw_buffer_load_b32(rs, (wm * 64 + m * 16 + (lane & 15)) * 4, WS_G + rb * 512, 16));
_Pragma("unroll 1")
        for (int t = 11; t < 15; ++t) M1STEP();
        if (wid == 0) wait_flags(fS + (lane & 31), pv, tag);
        cstep<PH_M1, PH_M1, true, 7>(smem + sc * SLOTM, smem, c, c, acc, A0, B0, A1, B1);
        STAMP(9);
#pragma unroll
        for (int m = 0; m < 4; ++m) og[m] = 1.0f - og[m];
        m1_epilogue(acc, og, smem, rs, rb, cb, wm, wn, lane);
    }
    {
        f32x4 acc[4][4];
#pragma unroll
        for (int n = 0; n < 4; ++n)
#pragma unroll
            for (int m = 0; m < 4; ++m) acc[n][m] = (f32x4){0.f, 0.f, 0.f, 0.f};
        bf16x8 B0[4], B1[4];
        const LaneC cs = lane_consts<PH_GS>(wm, wn, lane);
        STAMP(10);
        BAR();
        STAMP(11);
        load_frags<PH_GS, 0, 4>(smem, cs, A0, B0);
        SCHED0();
#define GSSTEP(t) cstep<PH_GS, PH_GS, false, 4>(smem + ((t) & 3) * SLOT, smem + (((t) + 1) & 3) * SLOT, cs, cs, acc, A0, B0, A1, B1)
_Pragma("unroll 1")
        for (int t = 0; t < 6; ++t) GSSTEP(t);
        signal_flag(fM + c8 * 4 + wid, tag, lane);
_Pragma("unroll 1")
        for (int t = 6; t < 9; ++t) GSSTEP(t);
        u64 pv = 0;
        if (wid == 0) pv = __hip_atomic_load(fM + (lane & 31), RLX_AGENT);
_Pragma("unroll 1")
        for (int t = 9; t < 12; ++t) GSSTEP(t);
        if (wid == 0) wait_flags(fM + (lane & 31), pv, tag);
_Pragma("unroll 1")
        for (int t = 12; t < 15; ++t) GSSTEP(t);
        STAMP(12);
        const LaneC cm = lane_consts<PH_GM>(wm, wn, lane);
        cstep<PH_GS, PH_GM, false, 4>(smem + 3 * SLOT, smem, cs, cm, acc, A0, B0, A1, B1);
_Pragma("unroll 1")
        for (int t = 16; t < 40; ++t) cstep<PH_GM, PH_GM, false, 4>(smem + (t & 3) * SLOT, smem + ((t + 1) & 3) * SLOT, cm, cm, acc, A0, B0, A1, B1);
        const int r = lane & 15, q = lane >> 4;
        const int t0r = rb * 128 + wm * 64 + kh * 32 + r, d0 = c8 * 128 + wn * 64 + q * 4;
        u32x2 xw[2][4], dw[4], bw[4]; float gg[2];
#pragma unroll
        for (int n = 0; n < 4; ++n) {
            dw[n] = *(const u32x2*)(a.dsk + d0 + n * 16); bw[n] = *(const u32x2*)(a.b2 + d0 + n * 16);
#pragma unroll
            for (int mm = 0; mm < 2; ++mm) xw[mm][n] = *(const u32x2*)(a.x + (size_t)(t0r + mm * 16) * 1024 + d0 + n * 16);
        }
#pragma unroll
        for (int mm = 0; mm < 2; ++mm) gg[mm] = __uint_as_float(__builtin_amdgcn_raw_buffer_load_b32(rs, (t0r + mm * 16) * 4, WS_G, 16));
        asm volatile("" ::: "memory");
_Pragma("unroll 1")
        for (int t = 40; t < 43; ++t) cstep<PH_GM, PH_GM, false, 4>(smem + (t & 3) * SLOT, smem + ((t + 1) & 3) * SLOT, cm, cm, acc, A0, B0, A1, B1);
        cstep<PH_GM, PH_GM, true, 4>(smem + 3 * SLOT, smem, cm, cm, acc, A0, B0, A1, B1);

        STAMP(13);
        if (kh) {
#pragma unroll
            for (int n = 0; n < 4; ++n)
#pragma unroll
                for (int mm = 0; mm < 2; ++mm) { const f32x4 t = acc[n][mm]; acc[n][mm] = acc[n][2 + mm]; acc[n][2 + mm] = t; }
        }
        const unsigned xo = WS_XB + (unsigned)(bid * 4 + wid) * 8192u, xi = WS_XB + (unsigned)((bid ^ 1) * 4 + wid) * 8192u;
#pragma unroll
        for (int n = 0; n < 4; ++n)
#pragma unroll
            for (int mm = 0; mm < 2; ++mm)
                st128(__builtin_bit_cast(u32x4, acc[n][2 + mm]), rs, lane * 16 + (n * 2 + mm) * 1024, xo, 16);
        f32x4 o[4][2];
#pragma unroll
        for (int mm = 0; mm < 2; ++mm) {
            const float g = gg[mm];
#pragma unroll
            for (int n = 0; n < 4; ++n) {
                const f32x4 own = acc[n][mm];
                o[n][mm][0] = own[0] + g * (bflo(xw[mm][n].x) * bflo(dw[n].x)) + (1.0f - g) * bflo(bw[n].x);
                o[n][mm][1] = own[1] + g * (bfhi(xw[mm][n].x) * bfhi(dw[n].x)) + (1.0f - g) * bfhi(bw[n].x);
                o[n][mm][2] = own[2] + g * (bflo(xw[mm][n].y) * bflo(dw[n].y)) + (1.0f - g) * bflo(bw[n].y);
                o[n][mm][3] = own[3] + g * (bfhi(xw[mm][n].y) * bfhi(dw[n].y)) + (1.0f - g) * bfhi(bw[n].y);
            }
        }
        signal_flag((gu64*)(ws + WS_XFLAG) + (bid * 4 + wid) * 8, tag, lane);
        STAMP(14);
        {
            gu64* fl = (gu64*)(ws + WS_XFLAG) + ((bid ^ 1) * 4 + wid) * 8;
            wait_flags(fl, __hip_atomic_load(fl, RLX_AGENT), tag);
        }
        STAMP(15);
#pragma unroll
        for (int n = 0; n < 4; ++n)
#pragma unroll
            for (int mm = 0; mm < 2; ++mm) {
                const u32x4 v = __builtin_amdgcn_raw_buffer_load_b128(rs, lane * 16 + (n * 2 + mm) * 1024, xi, 16);
                o[n][mm] += __builtin_bit_cast(f32x4, v);
            }
#pragma unroll
        for (int mm = 0; mm < 2; ++mm)
#pragma unroll
            for (int n = 0; n < 4; ++n) *(f32x4*)(a.out + (size_t)(t0r + mm * 16) * 1024 + d0 + n * 16) = o[n][mm];
        WAITV(0);
        STAMP(16);
        while (__builtin_amdgcn_s_memrealtime() - dg0 < 10000ull + (dg1 - dg0)) __builtin_amdgcn_s_sleep(1);
    }
}
}
extern "C" __global__ void gm_code_end() {}

extern "C" void kernel_launch(void* const* d_in, const int* in_sizes, int n_in,
                              void* d_out, int out_size, void* d_ws, size_t ws_size,
                              hipStream_t stream) {
    using namespace gm;
    Args a;
    a.x = (const u16*)d_in[0]; a.lre = (const u16*)d_in[1]; a.lim = (const u16*)d_in[2]; a.bre = (const u16*)d_in[3]; a.bim = (const u16*)d_in[4];
    a.cre = (const u16*)d_in[5]; a.cim = (const u16*)d_in[6]; a.ldt = (const u16*)d_in[7]; a.dsk = (const u16*)d_in[8];
    a.w1 = (const u16*)d_in[10]; a.b1 = (const u16*)d_in[11]; a.w2 = (const u16*)d_in[12]; a.b2 = (const u16*)d_in[13]; a.gw = (const u16*)d_in[14];
    a.out = (float*)d_out; a.ws = (char*)d_ws;
    (void)hipFuncSetAttribute((const void*)fused_kernel, hipFuncAttributeMaxDynamicSharedMemorySize, LDS_BYTES);
    fused_kernel<<<256, 512, LDS_BYTES, stream>>>(a);
}
```

```cpp
#include <hip/hip_runtime.h>
#include <hip/hip_bf16.h>

namespace gm {
#define LAS __attribute__((address_space(3)))
#define GAS __attribute__((address_space(1)))
typedef unsigned short u16;
typedef short bf16x8 __attribute__((ext_vector_type(8)));
typedef short s16x4 __attribute__((ext_vector_type(4)));
typedef float f32x4 __attribute__((ext_vector_type(4)));
typedef float f32x2 __attribute__((ext_vector_type(2)));
typedef unsigned u32x4 __attribute__((ext_vector_type(4)));
typedef unsigned u32x2 __attribute__((ext_vector_type(2)));
typedef LAS char* ldsp;
typedef GAS unsigned gu32;
typedef unsigned long long u64;
typedef GAS u64 gu64;

#define BAR() __builtin_amdgcn_s_barrier()
#define WAITV(n) asm volatile("s_waitcnt vmcnt(%0)" ::"i"(n) : "memory")
#define SCHED0() __builtin_amdgcn_sched_barrier(0)
#define LDS_FENCE() do { SCHED0(); __builtin_amdgcn_s_waitcnt(0xC07F); asm volatile("" ::: "memory"); SCHED0(); } while (0)
#define RLX_AGENT __ATOMIC_RELAXED, __HIP_MEMORY_SCOPE_AGENT

constexpr unsigned WS_EPOCH = 0;
constexpr unsigned WS_FY = 4096;
constexpr unsigned WS_FC = 12288;
constexpr unsigned WS_FS = 16384;
constexpr unsigned WS_FM = 24576;
constexpr unsigned WS_XFLAG = 32768;
constexpr unsigned WS_GRAN = 131072;
constexpr unsigned WS_STAT = 1u << 20;
constexpr unsigned WS_CSP = 2u << 20;
constexpr unsigned WS_A2 = 8u << 20;
constexpr unsigned WS_XB = 32u << 20;
constexpr unsigned WS_END = 40u << 20;

constexpr int SLOT = 32768, SLOTM = 45056;
constexpr int LDS_PSEG = 131072;
constexpr int LDS_E = 135168;
constexpr int LDS_COEF = LDS_E + 4096;
constexpr int LDS_BC = LDS_COEF + 1024;
constexpr int LDS_STAT = LDS_BC + 2048;
constexpr int LDS_G2 = LDS_STAT + 2048;
constexpr int LDS_DUMMY = LDS_G2;
constexpr int LDS_TAG = LDS_G2 + 512;
constexpr int LDS_GATH = LDS_TAG + 64;
constexpr int LDS_BYTES = LDS_GATH + 16384;
static_assert(LDS_BYTES <= 160 * 1024, "LDS");

enum { PH_S1 = 0, PH_M1 = 1, PH_GS = 2, PH_GM = 3 };

struct Args {
    const u16 *x, *lre, *lim, *bre, *bim, *cre, *cim, *ldt, *dsk, *w1, *b1, *w2, *b2, *gw;
    float* out;
    char* ws;
};

__device__ __forceinline__ bf16x8 lds128(ldsp p) { return *(const LAS bf16x8*)p; }
__device__ __forceinline__ s16x4 ldstr(ldsp p) { return __builtin_amdgcn_ds_read_tr16_b64_v4i16((LAS s16x4*)p); }
__device__ __forceinline__ bf16x8 cat(s16x4 a, s16x4 b) { return __builtin_shufflevector(a, b, 0, 1, 2, 3, 4, 5, 6, 7); }
template <bool C> __device__ __forceinline__ void dma16(const void* sb, unsigned vo, unsigned la) {
    if constexpr (C) asm volatile("s_mov_b32 m0, %0\n\ts_nop 0\n\tglobal_load_lds_dwordx4 %1, %2 sc1" ::"s"(la), "v"(vo), "s"(sb) : "memory");
    else asm volatile("s_mov_b32 m0, %0\n\ts_nop 0\n\tglobal_load_lds_dwordx4 %1, %2" ::"s"(la), "v"(vo), "s"(sb) : "memory");
}
__device__ __forceinline__ void dma64c(const void* sb, unsigned vo, unsigned la) {
    asm volatile("s_mov_b32 m0, %0\n\ts_nop 0\n\tglobal_load_lds_dwordx4 %1, %2 sc1\n\tglobal_load_lds_dwordx4 %1, %2 offset:1024 sc1\n\t"
                 "global_load_lds_dwordx4 %1, %2 offset:2048 sc1\n\tglobal_load_lds_dwordx4 %1, %2 offset:3072 sc1" ::"s"(la), "v"(vo), "s"(sb) : "memory");
}
template <bool C> __device__ __forceinline__ void dma4(const void* sb, unsigned vo, unsigned la) {
    if constexpr (C) asm volatile("s_mov_b32 m0, %0\n\ts_nop 0\n\tglobal_load_lds_dword %1, %2 sc1" ::"s"(la), "v"(vo), "s"(sb) : "memory");
    else asm volatile("s_mov_b32 m0, %0\n\ts_nop 0\n\tglobal_load_lds_dword %1, %2" ::"s"(la), "v"(vo), "s"(sb) : "memory");
}
__device__ __forceinline__ void swarm8(unsigned sd, const void* p) {
    asm volatile("s_load_dword %0, %1, 0x0\n\ts_load_dword %0, %1, 0x40\n\ts_load_dword %0, %1, 0x80\n\ts_load_dword %0, %1, 0xc0\n\t"
                 "s_load_dword %0, %1, 0x100\n\ts_load_dword %0, %1, 0x140\n\ts_load_dword %0, %1, 0x180\n\ts_load_dword %0, %1, 0x1c0" ::"s"(sd), "s"(p));
}
__device__ __forceinline__ void swarm4(unsigned sd, const void* p) {
    asm volatile("s_load_dword %0, %1, 0x0\n\ts_load_dword %0, %1, 0x40\n\ts_load_dword %0, %1, 0x80\n\ts_load_dword %0, %1, 0xc0" ::"s"(sd), "s"(p));
}
__device__ __forceinline__ void swarm2x2(unsigned sd, const void* p) {
    asm volatile("s_load_dword %0, %1, 0x0\n\ts_load_dword %0, %1, 0x40\n\ts_load_dword %0, %1, 0x800\n\ts_load_dword %0, %1, 0x840" ::"s"(sd), "s"(p));
}
__device__ __forceinline__ void st128(u32x4 v, __amdgpu_buffer_rsrc_t rs, unsigned voff, unsigned base, int) { __builtin_amdgcn_raw_buffer_store_b128(v, rs, voff + base, 0, 16); }
__device__ __forceinline__ void st64(u32x2 v, __amdgpu_buffer_rsrc_t rs, unsigned voff, unsigned base, int) { __builtin_amdgcn_raw_buffer_store_b64(v, rs, voff + base, 0, 16); }
__device__ __forceinline__ float bflo(unsigned w) { return __uint_as_float(w << 16); }
__device__ __forceinline__ float bfhi(unsigned w) { return __uint_as_float(w & 0xffff0000u); }
__device__ __forceinline__ float bf1(u16 b) { return __uint_as_float((unsigned)b << 16); }
__device__ __forceinline__ unsigned pkbf(float lo, float hi) {
    typedef __bf16 bf2_t __attribute__((ext_vector_type(2)));
    bf2_t v = {(__bf16)lo, (__bf16)hi};
    return __builtin_bit_cast(unsigned, v);
}
__device__ __forceinline__ void cfma(float lr, float li, float& zr, float& zi, float ur, float ui) {
    float tr, ti;
    asm("v_fma_f32 %0, -%3, %5, %6\n\tv_fma_f32 %1, %3, %4, %7\n\tv_fma_f32 %0, %2, %4, %0\n\tv_fma_f32 %1, %2, %5, %1"
        : "=&v"(tr), "=&v"(ti) : "v"(lr), "v"(li), "v"(zr), "v"(zi), "v"(ur), "v"(ui));
    zr = tr; zi = ti;
}
__device__ __forceinline__ bool deadline(unsigned long long t0) { return __builtin_amdgcn_s_memrealtime() - t0 > 400000ull; }

struct LaneC { unsigned a0, a1, b0, b1, b2; };
template <int PH>
__device__ __forceinline__ LaneC lane_consts(int wm, int wn, int lane) {
    const int r = lane & 15, q = lane >> 4, rr = r >> 2, p = lane & 3;
    const unsigned rl = r * 128 + ((q ^ (r >> 1)) * 16);
    LaneC c; c.b2 = 0;
    if constexpr (PH == PH_S1) { c.a0 = wm * 8192 + rl; c.a1 = c.a0 ^ 64; c.b0 = 16384 + wn * 8192 + rl; c.b1 = c.b0 ^ 64; }
    if constexpr (PH == PH_M1) {
        c.a0 = wm * 8192 + rl; c.a1 = c.a0 ^ 64;
        const unsigned t0 = 16384 + q * 7 * 512 + rr * 64 + ((q & 1) * 32) + p * 8;
        c.b0 = t0 + wn * 2048;
        c.b1 = (t0 ^ 32) + wn * 2048;
        c.b2 = t0 ^ (wn * 32);
    }
    if constexpr (PH == PH_GS) {
        c.a0 = (q * 8 + wm * 4) * 256 + (p >> 1) * 128 + (4 * (q & 1) + rr) * 16 + (p & 1) * 8; c.a1 = c.a0 ^ 64;
        c.b0 = 16384 + wn * 8192 + rl; c.b1 = c.b0 ^ 64;
    }
    if constexpr (PH == PH_GM) {
        c.a0 = wm * 8192 + lane * 16; c.a1 = c.a0;
        c.b0 = 16384 + q * 4 * 512 + wn * 1024 + rr * 64 + ((q & 1) * 32) + p * 8; c.b1 = c.b0 ^ 32;
    }
    return c;
}
template <int PH, int KK, int NB>
__device__ __forceinline__ void load_frags(ldsp slot, const LaneC& c, bf16x8 (&A)[4], bf16x8 (&B)[NB]) {
    if constexpr (PH == PH_S1 || PH == PH_M1) {
        ldsp pa = slot + (KK ? c.a1 : c.a0);
#pragma unroll
        for (int m = 0; m < 4; ++m) A[m] = lds128(pa + m * 2048);
    } else if constexpr (PH == PH_GS) {
        ldsp p0 = slot + c.a0, p1 = slot + c.a1;
#pragma unroll
        for (int m = 0; m < 4; ++m) A[m] = cat(ldstr(p0 + (KK * 32 + m) * 256), ldstr(p1 + (KK * 32 + m) * 256));
    } else {
        ldsp pa = slot + c.a0;
#pragma unroll
        for (int m = 0; m < 4; ++m) A[m] = lds128(pa + (m * 2 + KK) * 1024);
    }
    if constexpr (PH == PH_S1 || PH == PH_GS) {
        ldsp pb = slot + (KK ? c.b1 : c.b0);
#pragma unroll
        for (int n = 0; n < NB; ++n) B[n] = lds128(pb + n * 2048);
    } else {
        constexpr int NS2 = PH == PH_M1 ? 7 : 4;
        ldsp p0 = slot + c.b0, p1 = slot + c.b1;
#pragma unroll
        for (int n = 0; n < NB; ++n) { ldsp p = (n == 6 ? slot + c.b2 : (n & 1) ? p1 : p0) + KK * 4 * NS2 * 512 + (n >> 1) * 512; B[n] = cat(ldstr(p), ldstr(p + 256)); }
    }
}
template <int PH, int NB>
__device__ __forceinline__ void mma(f32x4 (&acc)[NB][4], const bf16x8 (&A)[4], const bf16x8 (&B)[NB]) {
#pragma unroll
    for (int n = 0; n < NB; ++n)
#pragma unroll
        for (int m = 0; m < 4; ++m) {
            if constexpr (PH == PH_S1) acc[n][m] = __builtin_amdgcn_mfma_f32_16x16x32_bf16(A[m], B[n], acc[n][m], 0, 0, 0);
            else acc[n][m] = __builtin_amdgcn_mfma_f32_16x16x32_bf16(B[n], A[m], acc[n][m], 0, 0, 0);
        }
}
template <int PH> constexpr int nreads() { return PH == PH_S1 ? 8 : PH == PH_M1 ? 18 : 12; }
template <int NR, int NM>
__device__ __forceinline__ void sched_half() {
#pragma unroll
    for (int i = 0; i < NR; ++i) { __builtin_amdgcn_sched_group_barrier(0x8, 1, 0); __builtin_amdgcn_sched_group_barrier(0x100, 1, 0); }
    __builtin_amdgcn_sched_group_barrier(0x8, NM - NR, 0);
}
template <int PC, int PN, bool LAST, int NB>
__device__ __forceinline__ void cstep(ldsp cur, ldsp nxt, const LaneC& cc, const LaneC& cn, f32x4 (&acc)[NB][4], bf16x8 (&A0)[4], bf16x8 (&B0)[NB], bf16x8 (&A1)[4], bf16x8 (&B1)[NB]) {
    load_frags<PC, 1, NB>(cur, cc, A1, B1);
    mma<PC, NB>(acc, A0, B0);
    sched_half<nreads<PC>(), 4 * NB>();
    SCHED0();
    __builtin_amdgcn_s_waitcnt(0xC07F);
    BAR();
    SCHED0();
    if constexpr (!LAST) load_frags<PN, 0, NB>(nxt, cn, A0, B0);
    mma<PC, NB>(acc, A1, B1);
    if constexpr (!LAST) sched_half<nreads<PN>(), 4 * NB>();
    SCHED0();
}

__device__ __forceinline__ u64 next_tag(u64 e) { return e * 0x9E3779B97F4A7C15ull + 0xD1B54A32D192ED03ull; }
__device__ __forceinline__ void wait_flags(gu64* fl, u64 v, u64 tag) {
    if (!__all(v == tag)) {
        const unsigned long long t0 = __builtin_amdgcn_s_memrealtime();
        do { __builtin_amdgcn_s_sleep(2); v = __hip_atomic_load(fl, RLX_AGENT); } while (!__all(v == tag) && !deadline(t0));
    }
    asm volatile("" ::: "memory");
}
__device__ __forceinline__ void signal_flag(gu64* fl, u64 tag, int lane) {
    WAITV(0);
    if (lane == 0) __hip_atomic_store(fl, tag, RLX_AGENT);
}
template <int CTRL, int RMASK> __device__ __forceinline__ float dpp_add(float v) {
    return v + __builtin_bit_cast(float, __builtin_amdgcn_update_dpp(0, __builtin_bit_cast(int, v), CTRL, RMASK, 0xf, true));
}
__device__ __forceinline__ float wave_sum(float v) {
    v = dpp_add<0x111, 0xf>(v); v = dpp_add<0x112, 0xf>(v); v = dpp_add<0x114, 0xf>(v); v = dpp_add<0x118, 0xf>(v);
    v = dpp_add<0x142, 0xa>(v); v = dpp_add<0x143, 0xc>(v);
    return __builtin_bit_cast(float, __builtin_amdgcn_readlane(__builtin_bit_cast(int, v), 63));
}
#define STAMP(i) do { } while (0)

__device__ __forceinline__ unsigned gelu2(f32x2 v) {
    const f32x2 t = (v * v) * -0.1029432397f + -2.3022081986f;
    const f32x2 u = v * t;
    f32x2 d; d.x = __builtin_amdgcn_exp2f(u.x); d.y = __builtin_amdgcn_exp2f(u.y);
    d = d + 1.0f;
    f32x2 r; r.x = __builtin_amdgcn_rcpf(d.x); r.y = __builtin_amdgcn_rcpf(d.y);
    const f32x2 o = v * r;
    return pkbf(o.x, o.y);
}

__device__ __forceinline__ void m1_epilogue(f32x4 (&acc)[7][4], ldsp smem, __amdgpu_buffer_rsrc_t rs, int rb, int cb, int wm, int wn, int lane) {
    f32x2 st[4];
#pragma unroll
    for (int m = 0; m < 4; ++m) st[m] = *(const LAS f32x2*)(smem + LDS_STAT + (wm * 64 + m * 16 + (lane & 15)) * 16);
    u32x2 pk[7][4];
#pragma unroll
    for (int n = 0; n < 7; ++n) {
        const int nbt = n == 6 ? 6 + wn : n + 8 * wn;
        const f32x4 b = *(const LAS f32x4*)(smem + LDS_BC + (nbt * 16 + (lane >> 4) * 4) * 4), cs = *(const LAS f32x4*)(smem + LDS_BC + 1024 + (nbt * 16 + (lane >> 4) * 4) * 4);
#pragma unroll
        for (int m = 0; m < 4; ++m) {
            const f32x4 v = acc[n][m] * st[m].x + (cs * st[m].y + b);
            pk[n][m].x = gelu2((f32x2){v[0], v[1]});
            pk[n][m].y = gelu2((f32x2){v[2], v[3]});
        }
        if ((n & 1) || n == 6) {
            const int pair = 7 * cb + (nbt >> 1);
            const unsigned base = WS_A2 + (unsigned)(rb * 88 + 32 + (pair >> 1)) * 16384u + (unsigned)(pair & 1) * 1024u + (unsigned)(wm * 4) * 2048u;
#pragma unroll
            for (int m = 0; m < 4; ++m) {
                if (n == 6) st64(pk[n][m], rs, lane * 16 + m * 2048, base + wn * 8, 16);
                else { const u32x4 v = {pk[n - (n & 1)][m].x, pk[n - (n & 1)][m].y, pk[n][m].x, pk[n][m].y}; st128(v, rs, lane * 16 + m * 2048, base, 16); }
            }
        }
    }
}

__device__ __forceinline__ void check_flags(ldsp area, const char* ws, unsigned off, u64 tag, int lane) {
    const u32x4 v = *(const LAS u32x4*)(area + lane * 16);
    const unsigned lo = (unsigned)tag, hi = (unsigned)(tag >> 32);
    if (!__all(v[0] == lo && v[1] == hi && v[2] == lo && v[3] == hi)) {
        const unsigned long long t0 = __builtin_amdgcn_s_memrealtime();
        bool ok;
        do { __builtin_amdgcn_s_sleep(2);
             ok = __hip_atomic_load((gu64*)(ws + off), RLX_AGENT) == tag && __hip_atomic_load((gu64*)(ws + off + 8), RLX_AGENT) == tag;
        } while (!__all(ok) && !deadline(t0));
    }
    asm volatile("" ::: "memory");
}

__global__ __launch_bounds__(512, 2) void fused_kernel(Args a) {
    extern __shared__ __attribute__((aligned(16))) char smem_[];
    ldsp smem = (ldsp)smem_;
    const int wid = __builtin_amdgcn_readfirstlane(threadIdx.x >> 6), lane = threadIdx.x & 63;
    const int bid = blockIdx.x, xcd = bid & 7, rk = bid >> 3;
    const int kh = xcd & 1, rb = (xcd >> 1) * 4 + (rk >> 3), c8 = rk & 7, cb = kh * 8 + c8;
    char* const ws = a.ws;
    const char* pc; unsigned code_len;
    asm volatile("s_getpc_b64 %0\n.Lgm_pc%=:\n\ts_mov_b32 %1, gm_code_end-.Lgm_pc%=" : "=s"(pc), "=s"(code_len));
    if (wid == 3) {
        const unsigned o = (unsigned)(rk + 32 * (lane & 15)) * 128u;
        dma4<false>(pc, o < code_len && code_len < (1u << 20) ? o : 0u, (unsigned)(size_t)smem + LDS_DUMMY);
    }
    asm volatile("" ::"s"(a.x), "s"(a.lre), "s"(a.lim), "s"(a.bre), "s"(a.bim), "s"(a.cre), "s"(a.cim), "s"(a.ldt), "s"(a.dsk), "s"(a.w1), "s"(a.b1), "s"(a.w2), "s"(a.b2), "s"(a.gw), "s"(a.out), "s"(a.ws));

    u32x4 xv[2][2], gv[2]; u64 ep = 0;
    if (wid < 4) {
        const int lnrow = 512 * (xcd >> 1) + 256 * kh + 8 * rk + 2 * wid;
#pragma unroll
        for (int i = 0; i < 2; ++i)
#pragma unroll
            for (int h = 0; h < 2; ++h) xv[i][h] = *(const u32x4*)(a.x + (size_t)(lnrow + i) * 1024 + h * 512 + lane * 8);
#pragma unroll
        for (int h = 0; h < 2; ++h) gv[h] = *(const u32x4*)(a.gw + h * 512 + lane * 8);
        ep = __hip_atomic_load((gu64*)(ws + WS_EPOCH), RLX_AGENT);
    }
    int wid2 = wid;
    asm volatile("" : "+s"(wid2)::"memory");
    if (__builtin_expect(wid2 >= 4, 1)) {
        __builtin_amdgcn_s_waitcnt(0x0F70);
        const int w = wid - 4, un = (xcd >> 1) * 32 + rk;
        const unsigned lb = (unsigned)(size_t)smem;
        const unsigned swz = (unsigned)(((lane & 7) ^ ((4 * (w & 1) + (lane >> 4)) & 7)) * 16);
        const unsigned vl = lane * 16;
        unsigned sd;
        asm volatile("s_mov_b32 %0, 0" : "=s"(sd));
        ldsp const parea = smem + LDS_E + w * 1024;
        const char* const pw2 = (const char*)a.w2 + (size_t)(1792 * kh + un) * 2048 + w * 512;
        constexpr int W2C = 128 * 2048;
        const __amdgpu_buffer_rsrc_t rs = __builtin_amdgcn_make_buffer_rsrc(ws, 0, WS_END, 0x00020000);
        u64 tag;
        unsigned plr = 0, pli = 0, pld = 0; float b1v = 0.f;
        f32x2 l16, wt[4];
        {
            const unsigned va = (unsigned)((rb * 128 + 8 * w + (lane >> 3)) * 2048) + swz;
            unsigned vb[7];
#pragma unroll
            for (int i = 0; i < 7; ++i) {
                const int sl = 2 * i + (lane >> 5), kq = 2 * w + (sl >= 7 ? 1 : 0), nb2 = sl >= 7 ? sl - 7 : sl;
                vb[i] = (unsigned)((8 * kq + ((lane >> 2) & 7)) * 7168 + cb * 448 + nb2 * 64 + (((lane & 3) ^ (2 * (kq & 1))) * 16));
            }
            int slot = 0;
            auto issue = [&](int t) {
                const unsigned s = lb + slot * SLOTM;
                slot = slot == 2 ? 0 : slot + 1;
                const int ks = (t + rb) & 15;
                const char* pb = (const char*)a.w1 + (size_t)ks * 64 * 7168;
#pragma unroll
                for (int i = 0; i < 7; ++i) dma16<false>(pb, vb[i], s + 16384 + (w * 7 + i) * 1024);
#pragma unroll
                for (int n = 0; n < 4; ++n) dma16<false>((const char*)a.x + ks * 128 + n * 32 * 2048, va, s + w * 1024 + n * 4096);
            };
            issue(0);
            { const char* sp = w == 0 ? (const char*)a.b1 + cb * 448 : w == 1 ? (const char*)a.lre + cb * 128 : w == 2 ? (const char*)a.lim + cb * 128 : (const char*)a.ldt + cb * 128;
              dma16<false>(sp, (unsigned)(w == 0 ? (lane < 28 ? lane : 27) : (lane & 7)) * 16u, lb + LDS_GATH + 12288 + w * 1024); }
            issue(1);
            WAITV(12); BAR();
            issue(2);
            { const u32x2 tv = *(const LAS u32x2*)(smem + LDS_TAG);
              tag = ((u64)(unsigned)__builtin_amdgcn_readfirstlane(tv.y) << 32) | (unsigned)__builtin_amdgcn_readfirstlane(tv.x); }
            const char* const pbw = (const char*)((w & 1) ? a.bim : a.bre) + (size_t)(512 * kh + 4 * un + 2 * (w >> 1)) * 2048;
            const unsigned fo = (lane & 8) ? WS_FC + cb * 128 + (lane & 7) * 16 : WS_FY + rb * 128 + (lane & 7) * 16;
            const int lnrow = 512 * (xcd >> 1) + 256 * kh + 8 * rk;
_Pragma("unroll 1")
            for (int t = 0; t < 13; ++t) {
                WAITV(11); BAR();
                issue(t + 3);
                if (t == 1) {
                    if (w == 0) {
                        const u32x4 v = *(const LAS u32x4*)(smem + LDS_BC + lane * 16);
                        st128(v, rs, lane < 8 ? WS_STAT + (unsigned)(lnrow + lane) * 16u : WS_CSP + (unsigned)(cb * 16 + rb) * 896u + (lane - 8) * 16, 0, 16);
                    }
                    b1v = bf1(*(const LAS u16*)(smem + LDS_GATH + 12288 + (w * 56 + (lane < 56 ? lane : 0)) * 2));
                    plr = *(const LAS u16*)(smem + LDS_GATH + 13312 + lane * 2); pli = *(const LAS u16*)(smem + LDS_GATH + 14336 + lane * 2); pld = *(const LAS u16*)(smem + LDS_GATH + 15360 + lane * 2);
                    __builtin_amdgcn_s_waitcnt(0xC07F);
                }
                if (t == 3 && w == 0) __hip_atomic_store((gu64*)(ws + ((lane & 1) ? WS_FC + (cb * 16 + rb) * 8 : WS_FY + ((lnrow >> 7) * 16 + (rk & 15)) * 8)), tag, RLX_AGENT);
                if (t == 8) dma16<true>(ws, fo, lb + LDS_E + w * 1024);
                if (t == 10) check_flags(parea, ws, fo, tag, lane);
                if (t == 10 || t == 11) {
                    const int p0 = (t - 10) * 8 + w;
                    dma16<true>(ws + WS_CSP + cb * 14336 + p0 * 1024, vl, lb + LDS_GATH + p0 * 1024);
                    if (p0 + 4 < 14) dma16<true>(ws + WS_CSP + cb * 14336 + (p0 + 4) * 1024, vl, lb + LDS_GATH + (p0 + 4) * 1024);
                    else dma16<true>(ws + WS_STAT + rb * 2048 + (w - 2) * 1024, vl, lb + LDS_STAT + (w - 2) * 1024);
                }
                if (t != 0 && t != 9 && t != 12) swarm8(sd, t < 9 ? pbw + (t - 1) * 512 : pw2 + (t - 10) * W2C);
            }
            WAITV(11); BAR();
            {
                const int col = w * 56 + (lane < 56 ? lane : 0);
                float s = 0.f;
#pragma unroll
                for (int i = 0; i < 16; ++i) s += *(const LAS float*)(smem + LDS_GATH + i * 896 + col * 4);
                const float gg = *(const LAS float*)(smem + LDS_STAT + (w * 32 + (lane & 31)) * 16 + 8);
                LDS_FENCE();
                *(LAS float*)(smem + LDS_BC + 1024 + col * 4) = s;
                *(LAS float*)(smem + LDS_BC + col * 4) = b1v;
                *(LAS float*)(smem + LDS_G2 + (w * 32 + (lane & 31)) * 4) = 2.0f * gg;
            }
            __builtin_amdgcn_s_waitcnt(0xC07F);
            swarm8(sd, pw2 + 2 * W2C);
            WAITV(0); BAR();
            swarm8(sd, pw2 + 3 * W2C);
            BAR();
        }
        const char* const a2 = ws + WS_A2 + (size_t)rb * 88 * 16384 + w * 4096;
        const unsigned vr = (unsigned)((c8 * 128 + 8 * w + (lane >> 3)) * 2048) + swz;
        unsigned vt[2];
        { const int rho = (lane >> 2) & 7;
#pragma unroll
          for (int qq = 0; qq < 2; ++qq) { const int q = 2 * (w & 1) + qq; const int k = 32 * (w >> 1) + (rho < 4 ? 4 * q + rho : 16 + 4 * q + rho - 4);
              vt[qq] = (unsigned)(k * 2048 + c8 * 256 + (lane >> 5) * 64 + (((lane & 3) ^ (2 * qq)) * 16)); } }
        auto issue_gm = [&](int t) {
            const unsigned s = lb + (t & 3) * SLOT;
            const int km = kh * 28 + t;
            dma64c(a2 + (size_t)(32 + km) * 16384, vl, s + w * 4096);
            const char* pb = (const char*)a.w2 + (size_t)km * 64 * 2048;
#pragma unroll
            for (int n = 0; n < 4; ++n) dma16<false>(pb + (n & 1) * 128, vt[n >> 1], s + 16384 + (w * 4 + n) * 1024);
        };
        auto issue_gs = [&](int t) {
            const unsigned s = lb + (t & 3) * SLOT;
            const int ks = kh * 16 + (t - 28);
            dma64c(a2 + (size_t)ks * 16384, vl, s + w * 4096);
            const char* pb = (const char*)((ks & 1) ? a.cim : a.cre) + (ks >> 1) * 128;
#pragma unroll
            for (int n = 0; n < 4; ++n) dma16<false>(pb + n * 32 * 2048, vr, s + 16384 + (w + 4 * n) * 1024);
        };
        {
            const unsigned va = (unsigned)((rb * 128 + 16 * (lane >> 5) + ((lane >> 3) & 3) + 32 * (w & 1) + 4 * (w >> 1)) * 2048) + swz;
            const unsigned vb = (unsigned)((cb * 64 + 16 * (w >> 1) + 8 * (w & 1) + (lane >> 3)) * 2048) + swz;
            auto issue = [&](int t) {
                const unsigned s = lb + (t & 3) * SLOT + w * 1024;
                const int ko = ((t + rb + cb) & 15) * 128;
#pragma unroll
                for (int n = 0; n < 4; ++n) dma16<false>((const char*)a.x + ko + (64 * (n >> 1) + 8 * (n & 1)) * 2048, va, s + n * 4096);
#pragma unroll
                for (int n = 0; n < 4; ++n) dma16<false>((const char*)((n & 1) ? a.bim : a.bre) + ko + (n >> 1) * 32 * 2048, vb, s + 16384 + n * 4096);
            };
            issue(0);
            issue(1);
            {
                const float lr = bflo(plr), li = bflo(pli);
                const float dt = (float)(__bf16)expf(bflo(pld));
                const float zr = dt * lr, zi = dt * li;
                float sn, cs; sincosf(zi, &sn, &cs);
                const float er = expf(zr), Lr = er * cs, Li = er * sn;
                float pr = Lr, pi = Li;
#pragma unroll
                for (int i = 0; i < 4; ++i) cfma(pr, pi, pr, pi, 0.f, 0.f);
                l16 = (f32x2){pr, pi};
                if (w == 0) {
                    const float den = 1.0f / (lr * lr + li * li), nr = Lr - 1.0f;
                    *(LAS f32x4*)(smem + LDS_COEF + lane * 16) = (f32x4){Lr, Li, (nr * lr + Li * li) * den, (Li * lr - nr * li) * den};
                }
                if (w == 2) {
                    float qr = 1.0f, qi = 0.f;
#pragma unroll
                    for (int i = 0; i < 8; ++i) { *(LAS f32x2*)(smem + LDS_PSEG + (i * 64 + lane) * 8) = (f32x2){qr, qi}; cfma(pr, pi, qr, qi, 0.f, 0.f); }
                }
#pragma unroll
                for (int i = 0; i < 3; ++i) cfma(pr, pi, pr, pi, 0.f, 0.f);
                float p2r = pr, p2i = pi; cfma(pr, pi, p2r, p2i, 0.f, 0.f);
                float p4r = p2r, p4i = p2i; cfma(p2r, p2i, p4r, p4i, 0.f, 0.f);
                const int e0 = (rb - 1 - w) & 3, top = (rb - 1 - w) >> 2;
                float qr = (e0 & 1) ? pr : 1.0f, qi = (e0 & 1) ? pi : 0.f;
                if (e0 & 2) cfma(p2r, p2i, qr, qi, 0.f, 0.f);
#pragma unroll
                for (int j = 3; j >= 0; --j) {
                    wt[j] = j <= top ? (f32x2){qr, qi} : (f32x2){0.f, 0.f};
                    if (j <= top) cfma(p4r, p4i, qr, qi, 0.f, 0.f);
                }
                asm volatile("" : "+v"(wt[0]), "+v"(wt[1]), "+v"(wt[2]), "+v"(wt[3]), "+v"(l16));
                __builtin_amdgcn_s_waitcnt(0xC07F);
            }
            issue(2);
            issue(3);
            WAITV(24); BAR();
            const unsigned fo = WS_FM + (rb * 2 + kh) * 256 + (lane & 15) * 16;
_Pragma("unroll 1")
            for (int t = 0; t < 12; ++t) { WAITV(16); BAR(); issue(t + 4); swarm4(sd, pw2 + (4 + (t >> 1)) * W2C + (t & 1) * 256); }
            dma16<true>(ws, fo, lb + LDS_E + w * 1024);
            WAITV(17); BAR();
            WAITV(0); BAR();
            check_flags(parea, ws, fo, tag, lane);
            issue_gm(0);
            BAR();
            issue_gm(1);
            BAR();
            issue_gm(2); issue_gm(3);
        }
        {
            const unsigned fo = WS_FS + (rb * 2 + kh) * 256 + (lane & 15) * 16;
            const char* const pcr = (const char*)a.cre + (8 * un + 2 * w) * 2048 + kh * 1024;
            const char* const pci = (const char*)a.cim + (8 * un + 2 * w) * 2048 + kh * 1024;
            const unsigned tag_lo = (unsigned)tag, tag_hi = (unsigned)(tag >> 32);
            WAITV(24); BAR();
            if (w == 1) {
                f32x2 e[8];
#pragma unroll
                for (int i = 0; i < 8; ++i) e[i] = *(const LAS f32x2*)(smem + LDS_E + (i * 64 + lane) * 8);
                LDS_FENCE();
                float er = 0.f, ei = 0.f;
#pragma unroll
                for (int i = 0; i < 8; ++i) {
                    *(LAS f32x2*)(smem + LDS_E + (i * 64 + lane) * 8) = (f32x2){er, ei};
                    cfma(l16.x, l16.y, er, ei, e[i].x, e[i].y);
                }
                st128((u32x4){__float_as_uint(er), tag_lo, __float_as_uint(ei), tag_hi}, rs, vl, WS_GRAN + (unsigned)(cb * 16 + rb) * 1024u, 16);
                __builtin_amdgcn_s_waitcnt(0xC07F);
            }
_Pragma("unroll 1")
            for (int t = 0; t < 9; ++t) { WAITV(16); BAR(); issue_gm(t + 4); if (t < 8) swarm4(sd, pw2 + (10 + (t >> 1)) * W2C + (t & 1) * 256); }
#pragma unroll
            for (int j = 0; j < 4; ++j) dma16<true>(ws + WS_GRAN + cb * 16384 + (w + 4 * j) * 1024, vl, lb + LDS_GATH + (w * 4 + j) * 1024);
            WAITV(16); BAR(); issue_gm(13);
            WAITV(16); BAR(); issue_gm(14);
            WAITV(16); BAR(); issue_gm(15);
            {
                u32x4 g[4];
                bool ok = true;
#pragma unroll
                for (int j = 0; j < 4; ++j) {
                    g[j] = *(const LAS u32x4*)(smem + LDS_GATH + (w * 4 + j) * 1024 + lane * 16);
                    ok &= (w + 4 * j >= rb) | ((g[j][1] == tag_lo) & (g[j][3] == tag_hi));
                }
                if (!__all(ok)) {
                    const unsigned long long t0 = __builtin_amdgcn_s_memrealtime();
                    do {
                        __builtin_amdgcn_s_sleep(1);
                        ok = true;
                        asm volatile("" ::: "memory");
#pragma unroll
                        for (int j = 0; j < 4; ++j) {
                            g[j] = __builtin_amdgcn_raw_buffer_load_b128(rs, vl + (w + 4 * j) * 1024, WS_GRAN + (unsigned)cb * 16384u, 16);
                            ok &= (w + 4 * j >= rb) | ((g[j][1] == tag_lo) & (g[j][3] == tag_hi));
                        }
                    } while (!__all(ok) && !deadline(t0));
                }
                float sr = 0.f, si = 0.f;
#pragma unroll
                for (int j = 0; j < 4; ++j) {
                    float vr_ = w + 4 * j < rb ? __uint_as_float(g[j][0]) : 0.f, vi_ = w + 4 * j < rb ? __uint_as_float(g[j][2]) : 0.f;
                    cfma(wt[j].x, wt[j].y, vr_, vi_, sr, si); sr = vr_; si = vi_;
                }
                *(LAS f32x2*)(smem + LDS_BC + lane * 32 + w * 8) = (f32x2){sr, si};
                __builtin_amdgcn_s_waitcnt(0xC07F);
            }
_Pragma("unroll 1")
            for (int t = 12; t < 21; ++t) { WAITV(16); BAR(); issue_gm(t + 4); if (t < 20) swarm2x2(sd, ((t & 1) ? pci : pcr) + ((t - 12) >> 1) * 128); }
            dma16<true>(ws, fo, lb + LDS_E + w * 1024);
            WAITV(16); BAR(); issue_gm(25);
            WAITV(16); BAR(); issue_gm(26);
            WAITV(16); BAR(); issue_gm(27);
            check_flags(parea, ws, fo, tag, lane);
_Pragma("unroll 1")
            for (int t = 24; t < 40; ++t) { WAITV(16); BAR(); issue_gs(t + 4); if (t < 28) { swarm2x2(sd, pcr + (t - 20) * 128); swarm2x2(sd, pci + (t - 20) * 128); } }
            WAITV(16); BAR();
            {
                const int rl = 16 * w + (lane >> 4);
#pragma unroll
                for (int n = 0; n < 4; ++n) {
                    const int rr = rl + 4 * n;
                    dma16<false>((const char*)a.x + (size_t)(rb * 128 + kh * 32) * 2048 + c8 * 256, (unsigned)((64 * (rr >> 5) + (rr & 31)) * 2048 + (((lane & 15) ^ (rr & 15)) * 16)), lb + (w * 4 + n) * 1024);
                }
                dma4<false>((const char*)((w & 1) ? a.b2 : a.dsk) + c8 * 256, (unsigned)lane * 4u, lb + 16384 + w * 256);
            }
            WAITV(13); BAR();
            WAITV(5); BAR();
            WAITV(0); BAR();
            asm volatile("s_waitcnt lgkmcnt(0)" ::"s"(sd));
        }
        if (bid == 0 && w == 0) {
            const unsigned long long t0 = __builtin_amdgcn_s_memrealtime();
            for (;;) {
                bool ok = true;
#pragma unroll
                for (int i = 0; i < 4; ++i) ok &= __hip_atomic_load((gu64*)(ws + WS_FY) + i * 64 + lane, RLX_AGENT) == tag;
                if (__all(ok) || deadline(t0)) break;
                __builtin_amdgcn_s_sleep(2);
            }
            if (lane == 0) __hip_atomic_store((gu64*)(ws + WS_EPOCH), tag, RLX_AGENT);
        }
        return;
    }

    const int wm = wid >> 1, wn = wid & 1;
    const __amdgpu_buffer_rsrc_t rs = __builtin_amdgcn_make_buffer_rsrc(ws, 0, WS_END, 0x00020000);
    STAMP(0);
    {
        if (wid == 0) {
            const u64 t = next_tag(ep);
            if (lane == 0) *(LAS u32x2*)(smem + LDS_TAG) = (u32x2){(unsigned)t, (unsigned)(t >> 32)};
        }
        f32x2 xf[2][8];
        float s[2], gd[2], vs[2];
#pragma unroll
        for (int i = 0; i < 2; ++i) {
            f32x2 s2 = {0.f, 0.f}, g2 = {0.f, 0.f};
#pragma unroll
            for (int h = 0; h < 2; ++h)
#pragma unroll
                for (int j = 0; j < 4; ++j) {
                    const f32x2 v = {bflo(xv[i][h][j]), bfhi(xv[i][h][j])}, gw = {bflo(gv[h][j]), bfhi(gv[h][j])};
                    xf[i][h * 4 + j] = v; s2 += v; g2 += v * gw;
                }
            s[i] = wave_sum(s2.x + s2.y) * (1.0f / 1024.0f); gd[i] = wave_sum(g2.x + g2.y);
        }
#pragma unroll
        for (int i = 0; i < 2; ++i) {
            f32x2 v2 = {0.f, 0.f};
#pragma unroll
            for (int j = 0; j < 8; ++j) { const f32x2 d = xf[i][j] - s[i]; v2 += d * d; }
            vs[i] = wave_sum(v2.x + v2.y);
        }
        const float gl = lane ? gd[1] : gd[0], ml = lane ? s[1] : s[0], vl = lane ? vs[1] : vs[0];
        const float rstd = 1.0f / sqrtf(vl * (1.0f / 1024.0f) + 1e-5f), g = 1.0f / (1.0f + expf(-gl));
        if (lane < 2) *(LAS f32x4*)(smem + LDS_BC + (wid * 2 + lane) * 16) = (f32x4){rstd, -rstd * ml, g, 0.f};
    }
    bf16x8 A0[4], A1[4];
    {
        f32x4 acc[7][4];
        bf16x8 B0[7], B1[7];
        const LaneC c = lane_consts<PH_M1>(wm, wn, lane);
        __builtin_amdgcn_s_waitcnt(0xC07F);
        STAMP(1);
        BAR();
        STAMP(2);
        load_frags<PH_M1, 0, 7>(smem, c, A0, B0);
#pragma unroll
        for (int n = 0; n < 7; ++n)
#pragma unroll
            for (int m = 0; m < 4; ++m) acc[n][m] = (f32x4){0.f, 0.f, 0.f, 0.f};
        SCHED0();
        {
            const bf16x8 ones = {0x3F80, 0x3F80, 0x3F80, 0x3F80, 0x3F80, 0x3F80, 0x3F80, 0x3F80};
            const f32x4 z4 = {0.f, 0.f, 0.f, 0.f};
            f32x4 cs[4];
            load_frags<PH_M1, 1, 7>(smem, c, A1, B1);
            mma<PH_M1, 7>(acc, A0, B0);
            sched_half<18, 28>();
            SCHED0();
            if (wm == 0) {
#pragma unroll
                for (int n = 0; n < 4; ++n) cs[n] = __builtin_amdgcn_mfma_f32_16x16x32_bf16(B0[n], ones, z4, 0, 0, 0);
            } else {
#pragma unroll
                for (int n = 0; n < 4; ++n) cs[n] = __builtin_amdgcn_mfma_f32_16x16x32_bf16(B0[3 + n], ones, z4, 0, 0, 0);
            }
            SCHED0();
            __builtin_amdgcn_s_waitcnt(0xC07F);
            BAR();
            SCHED0();
            if (wm == 0) {
#pragma unroll
                for (int n = 0; n < 4; ++n) cs[n] = __builtin_amdgcn_mfma_f32_16x16x32_bf16(B1[n], ones, cs[n], 0, 0, 0);
            } else {
#pragma unroll
                for (int n = 0; n < 4; ++n) cs[n] = __builtin_amdgcn_mfma_f32_16x16x32_bf16(B1[3 + n], ones, cs[n], 0, 0, 0);
            }
            SCHED0();
            load_frags<PH_M1, 0, 7>(smem + SLOTM, c, A0, B0);
            mma<PH_M1, 7>(acc, A1, B1);
            sched_half<18, 28>();
            LDS_FENCE();
            if ((lane & 15) == 0) {
#pragma unroll
                for (int n = 0; n < 4; ++n) { const int nb = n + 3 * wm, nbt = nb == 6 ? 6 + wn : nb + 8 * wn;
                    *(LAS f32x4*)(smem + LDS_BC + 128 + (nbt * 16 + (lane >> 4) * 4) * 4) = cs[n]; }
            }
            SCHED0();
        }
        int sc = 1;
#define M1STEP() do { const int sn_ = sc == 2 ? 0 : sc + 1; cstep<PH_M1, PH_M1, false, 7>(smem + sc * SLOTM, smem + sn_ * SLOTM, c, c, acc, A0, B0, A1, B1); sc = sn_; } while (0)
_Pragma("unroll 1")
        for (int t = 1; t < 15; ++t) M1STEP();
        cstep<PH_M1, PH_M1, true, 7>(smem + sc * SLOTM, smem, c, c, acc, A0, B0, A1, B1);
        STAMP(3);
        m1_epilogue(acc, smem, rs, rb, cb, wm, wn, lane);
    }
    u64 tag;
    { const u32x2 tv = *(const LAS u32x2*)(smem + LDS_TAG);
      tag = ((u64)(unsigned)__builtin_amdgcn_readfirstlane(tv.y) << 32) | (unsigned)__builtin_amdgcn_readfirstlane(tv.x); }
    f32x4 sv[4][4];
    bf16x8 B0[4], B1[4];
    const int ch = lane & 15, q = lane >> 4, seg = wm * 4 + q;
    {
#pragma unroll
        for (int n = 0; n < 4; ++n)
#pragma unroll
            for (int m = 0; m < 4; ++m) sv[n][m] = (f32x4){0.f, 0.f, 0.f, 0.f};
        const LaneC c = lane_consts<PH_S1>(wm, wn, lane);
        STAMP(4);
        BAR();
        STAMP(5);
        load_frags<PH_S1, 0, 4>(smem, c, A0, B0);
        SCHED0();
#define S1STEP(t) cstep<PH_S1, PH_S1, false, 4>(smem + ((t) & 3) * SLOT, smem + (((t) + 1) & 3) * SLOT, c, c, sv, A0, B0, A1, B1)
_Pragma("unroll 1")
        for (int t = 0; t < 4; ++t) S1STEP(t);
        signal_flag((gu64*)(ws + WS_FM) + (rb * 2 + kh) * 32 + c8 * 4 + wid, tag, lane);
_Pragma("unroll 1")
        for (int t = 4; t < 15; ++t) S1STEP(t);
        f32x2 lam[2];
#pragma unroll
        for (int cbk = 0; cbk < 2; ++cbk) lam[cbk] = *(const LAS f32x2*)(smem + LDS_COEF + (wn * 32 + cbk * 16 + ch) * 16);
        SCHED0();
        cstep<PH_S1, PH_S1, true, 4>(smem + 3 * SLOT, smem, c, c, sv, A0, B0, A1, B1);
        STAMP(6);
        asm volatile("s_nop 15");
        SCHED0();
        float er[2], ei[2];
#pragma unroll
        for (int cbk = 0; cbk < 2; ++cbk) { er[cbk] = sv[cbk][0][0]; ei[cbk] = sv[2 + cbk][0][0]; }
#pragma unroll
        for (int i = 1; i < 16; ++i)
#pragma unroll
            for (int cbk = 0; cbk < 2; ++cbk) cfma(lam[cbk].x, lam[cbk].y, er[cbk], ei[cbk], sv[cbk][i >> 2][i & 3], sv[2 + cbk][i >> 2][i & 3]);
#pragma unroll
        for (int cbk = 0; cbk < 2; ++cbk) *(LAS f32x2*)(smem + LDS_E + (seg * 64 + wn * 32 + cbk * 16 + ch) * 8) = (f32x2){er[cbk], ei[cbk]};
        STAMP(7);
    }
    {
        f32x4 acc[4][4];
#pragma unroll
        for (int n = 0; n < 4; ++n)
#pragma unroll
            for (int m = 0; m < 4; ++m) acc[n][m] = (f32x4){0.f, 0.f, 0.f, 0.f};
        const LaneC cm = lane_consts<PH_GM>(wm, wn, lane);
        __builtin_amdgcn_s_waitcnt(0xC07F);
        STAMP(8);
        BAR();
        STAMP(9);
        load_frags<PH_GM, 0, 4>(smem, cm, A0, B0);
        SCHED0();
#define GMSTEP(t) cstep<PH_GM, PH_GM, false, 4>(smem + ((t) & 3) * SLOT, smem + (((t) + 1) & 3) * SLOT, cm, cm, acc, A0, B0, A1, B1)
_Pragma("unroll 1")
        for (int t = 0; t < 13; ++t) GMSTEP(t);
        STAMP(10);
        {
            f32x4 g2[4], k0[2]; float zr[2], zi[2];
#pragma unroll
            for (int cbk = 0; cbk < 2; ++cbk) {
                const int cl = wn * 32 + cbk * 16 + ch;
                const f32x2 c0 = *(const LAS f32x2*)(smem + LDS_E + (seg * 64 + cl) * 8), ps = *(const LAS f32x2*)(smem + LDS_PSEG + (seg * 64 + cl) * 8);
                const f32x4 sp = *(const LAS f32x4*)(smem + LDS_BC + cl * 32) + *(const LAS f32x4*)(smem + LDS_BC + cl * 32 + 16);
                zr[cbk] = sp[0] + sp[2]; zi[cbk] = sp[1] + sp[3];
                cfma(ps.x, ps.y, zr[cbk], zi[cbk], c0.x, c0.y);
                k0[cbk] = *(const LAS f32x4*)(smem + LDS_COEF + (wn * 32 + cbk * 16 + ch) * 16);
            }
#pragma unroll
            for (int m = 0; m < 4; ++m) g2[m] = *(const LAS f32x4*)(smem + LDS_G2 + (seg * 16 + m * 4) * 4);
            const unsigned so = WS_A2 + (unsigned)(rb * 88 + 2 * cb) * 16384u;
#pragma unroll
            for (int th = 0; th < 2; ++th) {
#pragma unroll
                for (int i = th * 8; i < th * 8 + 8; ++i)
#pragma unroll
                    for (int cbk = 0; cbk < 2; ++cbk) {
                        cfma(k0[cbk][0], k0[cbk][1], zr[cbk], zi[cbk], sv[cbk][i >> 2][i & 3], sv[2 + cbk][i >> 2][i & 3]);
                        sv[cbk][i >> 2][i & 3] = zr[cbk]; sv[2 + cbk][i >> 2][i & 3] = zi[cbk];
                    }
#pragma unroll
                for (int cbk = 0; cbk < 2; ++cbk) {
                    const int kb = 4 * wn + 2 * cbk + (ch >> 3);
                    const unsigned vo = (unsigned)((kb * 8 + seg) * 256 + (((ch & 7) ^ (4 * (kb & 1))) * 16));
                    u32x4 vr, vi;
#pragma unroll
                    for (int mh = 0; mh < 2; ++mh) {
                        const f32x4 re = sv[cbk][th * 2 + mh], im = sv[2 + cbk][th * 2 + mh];
                        const f32x4 hr4 = (re * k0[cbk][2] - im * k0[cbk][3]) * g2[th * 2 + mh], hi4 = (im * -k0[cbk][2] - re * k0[cbk][3]) * g2[th * 2 + mh];
                        vr[mh * 2] = pkbf(hr4[0], hr4[1]); vr[mh * 2 + 1] = pkbf(hr4[2], hr4[3]);
                        vi[mh * 2] = pkbf(hi4[0], hi4[1]); vi[mh * 2 + 1] = pkbf(hi4[2], hi4[3]);
                    }
                    st128(vr, rs, vo + th * 128, so, 16);
                    st128(vi, rs, vo + th * 128, so + 16384u, 16);
                }
            }
            SCHED0();
        }
        STAMP(11);
_Pragma("unroll 1")
        for (int t = 13; t < 16; ++t) GMSTEP(t);
        signal_flag((gu64*)(ws + WS_FS) + (rb * 2 + kh) * 32 + c8 * 4 + wid, tag, lane);
_Pragma("unroll 1")
        for (int t = 16; t < 27; ++t) GMSTEP(t);
        const LaneC cs = lane_consts<PH_GS>(wm, wn, lane);
        float og[4];
#pragma unroll
        for (int m = 0; m < 4; ++m) og[m] = 1.0f - *(const LAS float*)(smem + LDS_STAT + (wm * 64 + m * 16 + ch) * 16 + 8);
        SCHED0();
        cstep<PH_GM, PH_GS, false, 4>(smem + 3 * SLOT, smem, cm, cs, acc, A0, B0, A1, B1);
#pragma unroll
        for (int n = 0; n < 4; ++n)
#pragma unroll
            for (int m = 0; m < 4; ++m) acc[n][m] *= og[m];
        SCHED0();
        STAMP(12);
_Pragma("unroll 1")
        for (int t = 28; t < 43; ++t) cstep<PH_GS, PH_GS, false, 4>(smem + (t & 3) * SLOT, smem + ((t + 1) & 3) * SLOT, cs, cs, acc, A0, B0, A1, B1);
        cstep<PH_GS, PH_GS, true, 4>(smem + 3 * SLOT, smem, cs, cs, acc, A0, B0, A1, B1);
        const int r = ch;
        const int t0r = rb * 128 + wm * 64 + kh * 32 + r, d0 = c8 * 128 + wn * 64 + q * 4;

        STAMP(13);
        if (kh) {
#pragma unroll
            for (int n = 0; n < 4; ++n)
#pragma unroll
                for (int mm = 0; mm < 2; ++mm) { const f32x4 t = acc[n][mm]; acc[n][mm] = acc[n][2 + mm]; acc[n][2 + mm] = t; }
        }
        const unsigned xo = WS_XB + (unsigned)(bid * 4 + wid) * 8192u, xi = WS_XB + (unsigned)((bid ^ 1) * 4 + wid) * 8192u;
#pragma unroll
        for (int n = 0; n < 4; ++n)
#pragma unroll
            for (int mm = 0; mm < 2; ++mm) st128(__builtin_bit_cast(u32x4, acc[n][2 + mm]), rs, lane * 16 + (n * 2 + mm) * 1024, xo, 16);
        const __amdgpu_buffer_rsrc_t ro = __builtin_amdgcn_make_buffer_rsrc(a.out, 0, 8u << 20, 0x00020000);
        f32x4 o[4][2];
#pragma unroll
        for (int mm = 0; mm < 2; ++mm) {
            const float g = *(const LAS float*)(smem + LDS_STAT + (wm * 64 + kh * 32 + mm * 16 + r) * 16 + 8);
#pragma unroll
            for (int n = 0; n < 4; ++n) {
                const u32x2 xw = *(const LAS u32x2*)(smem + (wm * 32 + mm * 16 + r) * 256 + (((wn * 8 + n * 2 + (q >> 1)) ^ r) * 16) + (q & 1) * 8);
                const u32x2 dw = *(const LAS u32x2*)(smem + 16384 + (wn * 64 + n * 16 + q * 4) * 2), bw = *(const LAS u32x2*)(smem + 16384 + 256 + (wn * 64 + n * 16 + q * 4) * 2);
                const f32x4 own = acc[n][mm];
                o[n][mm][0] = own[0] + g * (bflo(xw.x) * bflo(dw.x)) + (1.0f - g) * bflo(bw.x);
                o[n][mm][1] = own[1] + g * (bfhi(xw.x) * bfhi(dw.x)) + (1.0f - g) * bfhi(bw.x);
                o[n][mm][2] = own[2] + g * (bflo(xw.y) * bflo(dw.y)) + (1.0f - g) * bflo(bw.y);
                o[n][mm][3] = own[3] + g * (bfhi(xw.y) * bfhi(dw.y)) + (1.0f - g) * bfhi(bw.y);
                asm volatile("" : "+v"(o[n][mm]));
            }
        }
        signal_flag((gu64*)(ws + WS_XFLAG) + (bid * 4 + wid) * 8, tag, lane);
        STAMP(14);
        {
            gu64* fl = (gu64*)(ws + WS_XFLAG) + ((bid ^ 1) * 4 + wid) * 8;
            wait_flags(fl, __hip_atomic_load(fl, RLX_AGENT), tag);
        }
        STAMP(15);
#pragma unroll
        for (int n = 0; n < 4; ++n)
#pragma unroll
            for (int mm = 0; mm < 2; ++mm) {
                const u32x4 v = __builtin_amdgcn_raw_buffer_load_b128(rs, lane * 16 + (n * 2 + mm) * 1024, xi, 16);
                o[n][mm] += __builtin_bit_cast(f32x4, v);
            }
#pragma unroll
        for (int mm = 0; mm < 2; ++mm)
#pragma unroll
            for (int n = 0; n < 4; ++n) __builtin_amdgcn_raw_buffer_store_b128(__builtin_bit_cast(u32x4, o[n][mm]), ro, ((t0r + mm * 16) * 1024 + d0 + n * 16) * 4, 0, 16);
    }
}
}
extern "C" __global__ void gm_code_end() {}

extern "C" void kernel_launch(void* const* d_in, const int* in_sizes, int n_in,
                              void* d_out, int out_size, void* d_ws, size_t ws_size,
                              hipStream_t stream) {
    using namespace gm;
    Args a;
    a.x = (const u16*)d_in[0]; a.lre = (const u16*)d_in[1]; a.lim = (const u16*)d_in[2]; a.bre = (const u16*)d_in[3]; a.bim = (const u16*)d_in[4];
    a.cre = (const u16*)d_in[5]; a.cim = (const u16*)d_in[6]; a.ldt = (const u16*)d_in[7]; a.dsk = (const u16*)d_in[8];
    a.w1 = (const u16*)d_in[10]; a.b1 = (const u16*)d_in[11]; a.w2 = (const u16*)d_in[12]; a.b2 = (const u16*)d_in[13]; a.gw = (const u16*)d_in[14];
    a.out = (float*)d_out; a.ws = (char*)d_ws;
    (void)hipFuncSetAttribute((const void*)fused_kernel, hipFuncAttributeMaxDynamicSharedMemorySize, LDS_BYTES);
    fused_kernel<<<256, 512, LDS_BYTES, stream>>>(a);
}
```

```cpp
#include <hip/hip_runtime.h>
#include <hip/hip_bf16.h>

namespace gm {
#define LAS __attribute__((address_space(3)))
#define GAS __attribute__((address_space(1)))
typedef unsigned short u16;
typedef short bf16x8 __attribute__((ext_vector_type(8)));
typedef short s16x4 __attribute__((ext_vector_type(4)));
typedef float f32x4 __attribute__((ext_vector_type(4)));
typedef float f32x2 __attribute__((ext_vector_type(2)));
typedef unsigned u32x4 __attribute__((ext_vector_type(4)));
typedef unsigned u32x2 __attribute__((ext_vector_type(2)));
typedef LAS char* ldsp;
typedef GAS unsigned gu32;
typedef unsigned long long u64;
typedef GAS u64 gu64;

#define BAR() __builtin_amdgcn_s_barrier()
#define WAITV(n) asm volatile("s_waitcnt vmcnt(%0)" ::"i"(n) : "memory")
#define SCHED0() __builtin_amdgcn_sched_barrier(0)
#define LDS_FENCE() do { SCHED0(); __builtin_amdgcn_s_waitcnt(0xC07F); asm volatile("" ::: "memory"); SCHED0(); } while (0)
#define RLX_AGENT __ATOMIC_RELAXED, __HIP_MEMORY_SCOPE_AGENT

constexpr unsigned WS_EPOCH = 0;
constexpr unsigned WS_FY = 4096;
constexpr unsigned WS_FC = 12288;
constexpr unsigned WS_FS = 16384;
constexpr unsigned WS_FM = 24576;
constexpr unsigned WS_XFLAG = 32768;
constexpr unsigned WS_GRAN = 131072;
constexpr unsigned WS_STAT = 1u << 20;
constexpr unsigned WS_CSP = 2u << 20;
constexpr unsigned WS_A2 = 8u << 20;
constexpr unsigned WS_XB = 32u << 20;
constexpr unsigned WS_END = 40u << 20;

constexpr int SLOT = 32768, SLOTM = 45056;
constexpr int LDS_PSEG = 131072;
constexpr int LDS_E = 135168;
constexpr int LDS_COEF = LDS_E + 4096;
constexpr int LDS_BC = LDS_COEF + 1024;
constexpr int LDS_STAT = LDS_BC + 2048;
constexpr int LDS_G2 = LDS_STAT + 2048;
constexpr int LDS_DUMMY = LDS_G2;
constexpr int LDS_TAG = LDS_G2 + 512;
constexpr int LDS_GATH = LDS_TAG + 64;
constexpr int LDS_SM = LDS_GATH + 16384;
constexpr int LDS_BYTES = LDS_SM + 1024;
static_assert(LDS_BYTES <= 160 * 1024, "LDS");

constexpr unsigned XSENT = 0xFFFFFFFFu;

enum { PH_S1 = 0, PH_M1 = 1, PH_GS = 2, PH_GM = 3 };

struct Args {
    const u16 *x, *lre, *lim, *bre, *bim, *cre, *cim, *ldt, *dsk, *w1, *b1, *w2, *b2, *gw;
    float* out;
    char* ws;
};

__device__ __forceinline__ bf16x8 lds128(ldsp p) { return *(const LAS bf16x8*)p; }
__device__ __forceinline__ s16x4 ldstr(ldsp p) { return __builtin_amdgcn_ds_read_tr16_b64_v4i16((LAS s16x4*)p); }
__device__ __forceinline__ bf16x8 cat(s16x4 a, s16x4 b) { return __builtin_shufflevector(a, b, 0, 1, 2, 3, 4, 5, 6, 7); }
template <bool C> __device__ __forceinline__ void dma16(const void* sb, unsigned vo, unsigned la) {
    if constexpr (C) asm volatile("s_mov_b32 m0, %0\n\ts_nop 0\n\tglobal_load_lds_dwordx4 %1, %2 sc1" ::"s"(la), "v"(vo), "s"(sb) : "memory");
    else asm volatile("s_mov_b32 m0, %0\n\ts_nop 0\n\tglobal_load_lds_dwordx4 %1, %2" ::"s"(la), "v"(vo), "s"(sb) : "memory");
}
__device__ __forceinline__ void dma64c(const void* sb, unsigned vo, unsigned la) {
    asm volatile("s_mov_b32 m0, %0\n\ts_nop 0\n\tglobal_load_lds_dwordx4 %1, %2 sc1\n\tglobal_load_lds_dwordx4 %1, %2 offset:1024 sc1\n\t"
                 "global_load_lds_dwordx4 %1, %2 offset:2048 sc1\n\tglobal_load_lds_dwordx4 %1, %2 offset:3072 sc1" ::"s"(la), "v"(vo), "s"(sb) : "memory");
}
template <bool C> __device__ __forceinline__ void dma4(const void* sb, unsigned vo, unsigned la) {
    if constexpr (C) asm volatile("s_mov_b32 m0, %0\n\ts_nop 0\n\tglobal_load_lds_dword %1, %2 sc1" ::"s"(la), "v"(vo), "s"(sb) : "memory");
    else asm volatile("s_mov_b32 m0, %0\n\ts_nop 0\n\tglobal_load_lds_dword %1, %2" ::"s"(la), "v"(vo), "s"(sb) : "memory");
}
__device__ __forceinline__ void swarm8(unsigned sd, const void* p) {
    asm volatile("s_load_dword %0, %1, 0x0\n\ts_load_dword %0, %1, 0x40\n\ts_load_dword %0, %1, 0x80\n\ts_load_dword %0, %1, 0xc0\n\t"
                 "s_load_dword %0, %1, 0x100\n\ts_load_dword %0, %1, 0x140\n\ts_load_dword %0, %1, 0x180\n\ts_load_dword %0, %1, 0x1c0" ::"s"(sd), "s"(p));
}
__device__ __forceinline__ void swarm4(unsigned sd, const void* p) {
    asm volatile("s_load_dword %0, %1, 0x0\n\ts_load_dword %0, %1, 0x40\n\ts_load_dword %0, %1, 0x80\n\ts_load_dword %0, %1, 0xc0" ::"s"(sd), "s"(p));
}
__device__ __forceinline__ void swarm2x2(unsigned sd, const void* p) {
    asm volatile("s_load_dword %0, %1, 0x0\n\ts_load_dword %0, %1, 0x40\n\ts_load_dword %0, %1, 0x800\n\ts_load_dword %0, %1, 0x840" ::"s"(sd), "s"(p));
}
__device__ __forceinline__ void st128(u32x4 v, __amdgpu_buffer_rsrc_t rs, unsigned voff, unsigned base, int) { __builtin_amdgcn_raw_buffer_store_b128(v, rs, voff + base, 0, 16); }
__device__ __forceinline__ void st64(u32x2 v, __amdgpu_buffer_rsrc_t rs, unsigned voff, unsigned base, int) { __builtin_amdgcn_raw_buffer_store_b64(v, rs, voff + base, 0, 16); }
__device__ __forceinline__ float bflo(unsigned w) { return __uint_as_float(w << 16); }
__device__ __forceinline__ float bfhi(unsigned w) { return __uint_as_float(w & 0xffff0000u); }
__device__ __forceinline__ float bf1(u16 b) { return __uint_as_float((unsigned)b << 16); }
__device__ __forceinline__ unsigned pkbf(float lo, float hi) {
    typedef __bf16 bf2_t __attribute__((ext_vector_type(2)));
    bf2_t v = {(__bf16)lo, (__bf16)hi};
    return __builtin_bit_cast(unsigned, v);
}
__device__ __forceinline__ void cfma(float lr, float li, float& zr, float& zi, float ur, float ui) {
    float tr, ti;
    asm("v_fma_f32 %0, -%3, %5, %6\n\tv_fma_f32 %1, %3, %4, %7\n\tv_fma_f32 %0, %2, %4, %0\n\tv_fma_f32 %1, %2, %5, %1"
        : "=&v"(tr), "=&v"(ti) : "v"(lr), "v"(li), "v"(zr), "v"(zi), "v"(ur), "v"(ui));
    zr = tr; zi = ti;
}
__device__ __forceinline__ bool deadline(unsigned long long t0) { return __builtin_amdgcn_s_memrealtime() - t0 > 400000ull; }

struct LaneC { unsigned a0, a1, b0, b1, b2; };
template <int PH>
__device__ __forceinline__ LaneC lane_consts(int wm, int wn, int lane) {
    const int r = lane & 15, q = lane >> 4, rr = r >> 2, p = lane & 3;
    const unsigned rl = r * 128 + ((q ^ (r >> 1)) * 16);
    LaneC c; c.b2 = 0;
    if constexpr (PH == PH_S1) { c.a0 = wm * 8192 + rl; c.a1 = c.a0 ^ 64; c.b0 = 16384 + wn * 8192 + rl; c.b1 = c.b0 ^ 64; }
    if constexpr (PH == PH_M1) {
        c.a0 = wm * 8192 + rl; c.a1 = c.a0 ^ 64;
        const unsigned t0 = 16384 + q * 7 * 512 + rr * 64 + ((q & 1) * 32) + p * 8;
        c.b0 = t0 + wn * 2048;
        c.b1 = (t0 ^ 32) + wn * 2048;
        c.b2 = t0 ^ (wn * 32);
    }
    if constexpr (PH == PH_GS) {
        c.a0 = (q * 8 + wm * 4) * 256 + (p >> 1) * 128 + (4 * (q & 1) + rr) * 16 + (p & 1) * 8; c.a1 = c.a0 ^ 64;
        c.b0 = 16384 + wn * 8192 + rl; c.b1 = c.b0 ^ 64;
    }
    if constexpr (PH == PH_GM) {
        c.a0 = wm * 8192 + lane * 16; c.a1 = c.a0;
        c.b0 = 16384 + q * 4 * 512 + wn * 1024 + rr * 64 + ((q & 1) * 32) + p * 8; c.b1 = c.b0 ^ 32;
    }
    return c;
}
template <int PH, int KK, int NB>
__device__ __forceinline__ void load_frags(ldsp slot, const LaneC& c, bf16x8 (&A)[4], bf16x8 (&B)[NB]) {
    if constexpr (PH == PH_S1 || PH == PH_M1) {
        ldsp pa = slot + (KK ? c.a1 : c.a0);
#pragma unroll
        for (int m = 0; m < 4; ++m) A[m] = lds128(pa + m * 2048);
    } else if constexpr (PH == PH_GS) {
        ldsp p0 = slot + c.a0, p1 = slot + c.a1;
#pragma unroll
        for (int m = 0; m < 4; ++m) A[m] = cat(ldstr(p0 + (KK * 32 + m) * 256), ldstr(p1 + (KK * 32 + m) * 256));
    } else {
        ldsp pa = slot + c.a0;
#pragma unroll
        for (int m = 0; m < 4; ++m) A[m] = lds128(pa + (m * 2 + KK) * 1024);
    }
    if constexpr (PH == PH_S1 || PH == PH_GS) {
        ldsp pb = slot + (KK ? c.b1 : c.b0);
#pragma unroll
        for (int n = 0; n < NB; ++n) B[n] = lds128(pb + n * 2048);
    } else {
        constexpr int NS2 = PH == PH_M1 ? 7 : 4;
        ldsp p0 = slot + c.b0, p1 = slot + c.b1;
#pragma unroll
        for (int n = 0; n < NB; ++n) { ldsp p = (n == 6 ? slot + c.b2 : (n & 1) ? p1 : p0) + KK * 4 * NS2 * 512 + (n >> 1) * 512; B[n] = cat(ldstr(p), ldstr(p + 256)); }
    }
}
template <int PH, int NB>
__device__ __forceinline__ void mma(f32x4 (&acc)[NB][4], const bf16x8 (&A)[4], const bf16x8 (&B)[NB]) {
#pragma unroll
    for (int n = 0; n < NB; ++n)
#pragma unroll
        for (int m = 0; m < 4; ++m) {
            if constexpr (PH == PH_S1) acc[n][m] = __builtin_amdgcn_mfma_f32_16x16x32_bf16(A[m], B[n], acc[n][m], 0, 0, 0);
            else acc[n][m] = __builtin_amdgcn_mfma_f32_16x16x32_bf16(B[n], A[m], acc[n][m], 0, 0, 0);
        }
}
template <int PH> constexpr int nreads() { return PH == PH_S1 ? 8 : PH == PH_M1 ? 18 : 12; }
template <int NR, int NM>
__device__ __forceinline__ void sched_half() {
#pragma unroll
    for (int i = 0; i < NR; ++i) { __builtin_amdgcn_sched_group_barrier(0x8, 1, 0); __builtin_amdgcn_sched_group_barrier(0x100, 1, 0); }
    __builtin_amdgcn_sched_group_barrier(0x8, NM - NR, 0);
}
template <int PC, int PN, bool LAST, int NB>
__device__ __forceinline__ void cstep(ldsp cur, ldsp nxt, const LaneC& cc, const LaneC& cn, f32x4 (&acc)[NB][4], bf16x8 (&A0)[4], bf16x8 (&B0)[NB], bf16x8 (&A1)[4], bf16x8 (&B1)[NB]) {
    load_frags<PC, 1, NB>(cur, cc, A1, B1);
    mma<PC, NB>(acc, A0, B0);
    sched_half<nreads<PC>(), 4 * NB>();
    SCHED0();
    __builtin_amdgcn_s_waitcnt(0xC07F);
    BAR();
    SCHED0();
    if constexpr (!LAST) load_frags<PN, 0, NB>(nxt, cn, A0, B0);
    mma<PC, NB>(acc, A1, B1);
    if constexpr (!LAST) sched_half<nreads<PN>(), 4 * NB>();
    SCHED0();
}

__device__ __forceinline__ u64 next_tag(u64 e) { return e * 0x9E3779B97F4A7C15ull + 0xD1B54A32D192ED03ull; }
__device__ __forceinline__ void wait_flags(gu64* fl, u64 v, u64 tag) {
    if (!__all(v == tag)) {
        const unsigned long long t0 = __builtin_amdgcn_s_memrealtime();
        do { __builtin_amdgcn_s_sleep(2); v = __hip_atomic_load(fl, RLX_AGENT); } while (!__all(v == tag) && !deadline(t0));
    }
    asm volatile("" ::: "memory");
}
__device__ __forceinline__ void signal_flag(gu64* fl, u64 tag, int lane) {
    WAITV(0);
    if (lane == 0) __hip_atomic_store(fl, tag, RLX_AGENT);
}
template <int CTRL, int RMASK> __device__ __forceinline__ float dpp_add(float v) {
    return v + __builtin_bit_cast(float, __builtin_amdgcn_update_dpp(0, __builtin_bit_cast(int, v), CTRL, RMASK, 0xf, true));
}
__device__ __forceinline__ float wave_sum(float v) {
    v = dpp_add<0x111, 0xf>(v); v = dpp_add<0x112, 0xf>(v); v = dpp_add<0x114, 0xf>(v); v = dpp_add<0x118, 0xf>(v);
    v = dpp_add<0x142, 0xa>(v); v = dpp_add<0x143, 0xc>(v);
    return __builtin_bit_cast(float, __builtin_amdgcn_readlane(__builtin_bit_cast(int, v), 63));
}
#define STAMP(i) do { } while (0)

__device__ __forceinline__ unsigned gelu2(f32x2 v) {
    const f32x2 t = (v * v) * -0.1029432397f + -2.3022081986f;
    const f32x2 u = v * t;
    f32x2 d; d.x = __builtin_amdgcn_exp2f(u.x); d.y = __builtin_amdgcn_exp2f(u.y);
    d = d + 1.0f;
    f32x2 r; r.x = __builtin_amdgcn_rcpf(d.x); r.y = __builtin_amdgcn_rcpf(d.y);
    const f32x2 o = v * r;
    return pkbf(o.x, o.y);
}

__device__ __forceinline__ void m1_epilogue(f32x4 (&acc)[7][4], ldsp smem, __amdgpu_buffer_rsrc_t rs, int rb, int cb, int wm, int wn, int lane) {
    f32x2 st[4];
#pragma unroll
    for (int m = 0; m < 4; ++m) st[m] = *(const LAS f32x2*)(smem + LDS_STAT + (wm * 64 + m * 16 + (lane & 15)) * 16);
    u32x2 pk[7][4];
#pragma unroll
    for (int n = 0; n < 7; ++n) {
        const int nbt = n == 6 ? 6 + wn : n + 8 * wn;
        const f32x4 b = *(const LAS f32x4*)(smem + LDS_BC + (nbt * 16 + (lane >> 4) * 4) * 4), cs = *(const LAS f32x4*)(smem + LDS_BC + 1024 + (nbt * 16 + (lane >> 4) * 4) * 4);
#pragma unroll
        for (int m = 0; m < 4; ++m) {
            const f32x4 v = acc[n][m] * st[m].x + (cs * st[m].y + b);
            pk[n][m].x = gelu2((f32x2){v[0], v[1]});
            pk[n][m].y = gelu2((f32x2){v[2], v[3]});
        }
        if ((n & 1) || n == 6) {
            const int pair = 7 * cb + (nbt >> 1);
            const unsigned base = WS_A2 + (unsigned)(rb * 88 + 32 + (pair >> 1)) * 16384u + (unsigned)(pair & 1) * 1024u + (unsigned)(wm * 4) * 2048u;
#pragma unroll
            for (int m = 0; m < 4; ++m) {
                if (n == 6) st64(pk[n][m], rs, lane * 16 + m * 2048, base + wn * 8, 16);
                else { const u32x4 v = {pk[n - (n & 1)][m].x, pk[n - (n & 1)][m].y, pk[n][m].x, pk[n][m].y}; st128(v, rs, lane * 16 + m * 2048, base, 16); }
            }
        }
    }
}

__device__ __forceinline__ void check_flags(ldsp area, const char* ws, unsigned off, u64 tag, int lane) {
    const u32x4 v = *(const LAS u32x4*)(area + lane * 16);
    const unsigned lo = (unsigned)tag, hi = (unsigned)(tag >> 32);
    if (!__all(v[0] == lo && v[1] == hi && v[2] == lo && v[3] == hi)) {
        const unsigned long long t0 = __builtin_amdgcn_s_memrealtime();
        bool ok;
        do { __builtin_amdgcn_s_sleep(2);
             ok = __hip_atomic_load((gu64*)(ws + off), RLX_AGENT) == tag && __hip_atomic_load((gu64*)(ws + off + 8), RLX_AGENT) == tag;
        } while (!__all(ok) && !deadline(t0));
    }
    asm volatile("" ::: "memory");
}

__global__ __launch_bounds__(512, 2) void fused_kernel(Args a) {
    extern __shared__ __attribute__((aligned(16))) char smem_[];
    ldsp smem = (ldsp)smem_;
    const int wid = __builtin_amdgcn_readfirstlane(threadIdx.x >> 6), lane = threadIdx.x & 63;
    const int bid = blockIdx.x, xcd = bid & 7, rk = bid >> 3;
    const int kh = xcd & 1, rb = (xcd >> 1) * 4 + (rk >> 3), c8 = rk & 7, cb = kh * 8 + c8;
    char* const ws = a.ws;
    const char* pc; unsigned code_len;
    asm volatile("s_getpc_b64 %0\n.Lgm_pc%=:\n\ts_mov_b32 %1, gm_code_end-.Lgm_pc%=" : "=s"(pc), "=s"(code_len));
    if (wid == 3) {
        const unsigned o = (unsigned)(rk + 32 * (lane & 15)) * 128u;
        dma4<false>(pc, o < code_len && code_len < (1u << 20) ? o : 0u, (unsigned)(size_t)smem + LDS_DUMMY);
    }
    asm volatile("" ::"s"(a.x), "s"(a.lre), "s"(a.lim), "s"(a.bre), "s"(a.bim), "s"(a.cre), "s"(a.cim), "s"(a.ldt), "s"(a.dsk), "s"(a.w1), "s"(a.b1), "s"(a.w2), "s"(a.b2), "s"(a.gw), "s"(a.out), "s"(a.ws));

    u32x4 xv[2][2], gv[2]; u64 ep = 0;
    if (wid < 4) {
        const int lnrow = 512 * (xcd >> 1) + 256 * kh + 8 * rk + 2 * wid;
#pragma unroll
        for (int i = 0; i < 2; ++i)
#pragma unroll
            for (int h = 0; h < 2; ++h) xv[i][h] = *(const u32x4*)(a.x + (size_t)(lnrow + i) * 1024 + h * 512 + lane * 8);
#pragma unroll
        for (int h = 0; h < 2; ++h) gv[h] = *(const u32x4*)(a.gw + h * 512 + lane * 8);
        ep = __hip_atomic_load((gu64*)(ws + WS_EPOCH), RLX_AGENT);
    }
    int wid2 = wid;
    asm volatile("" : "+s"(wid2)::"memory");
    if (__builtin_expect(wid2 >= 4, 1)) {
        __builtin_amdgcn_s_waitcnt(0x0F70);
        const int w = wid - 4, un = (xcd >> 1) * 32 + rk;
        const unsigned lb = (unsigned)(size_t)smem;
        const unsigned swz = (unsigned)(((lane & 7) ^ ((4 * (w & 1) + (lane >> 4)) & 7)) * 16);
        const unsigned vl = lane * 16;
        unsigned sd;
        asm volatile("s_mov_b32 %0, 0" : "=s"(sd));
        ldsp const parea = smem + LDS_E + w * 1024;
        const char* const pw2 = (const char*)a.w2 + (size_t)(1792 * kh + un) * 2048 + w * 512;
        constexpr int W2C = 128 * 2048;
        const __amdgpu_buffer_rsrc_t rs = __builtin_amdgcn_make_buffer_rsrc(ws, 0, WS_END, 0x00020000);
        u64 tag;
        f32x2 l16, wt[4];
        const unsigned vsa = (unsigned)((rb * 128 + 16 * (lane >> 5) + ((lane >> 3) & 3) + 32 * (w & 1) + 4 * (w >> 1)) * 2048) + swz;
        const unsigned vsb = (unsigned)((cb * 64 + 16 * (w >> 1) + 8 * (w & 1) + (lane >> 3)) * 2048) + swz;
        auto issue_s = [&](int t) {
            const unsigned s = lb + ((t + 2) & 3) * SLOT + w * 1024;
            const int ko = ((t + rb + cb) & 15) * 128;
#pragma unroll
            for (int n = 0; n < 4; ++n) dma16<false>((const char*)a.x + ko + (64 * (n >> 1) + 8 * (n & 1)) * 2048, vsa, s + n * 4096);
#pragma unroll
            for (int n = 0; n < 4; ++n) dma16<false>((const char*)((n & 1) ? a.bim : a.bre) + ko + (n >> 1) * 32 * 2048, vsb, s + 16384 + n * 4096);
        };
        {
            const unsigned va = (unsigned)((rb * 128 + 8 * w + (lane >> 3)) * 2048) + swz;
            unsigned vb[7];
#pragma unroll
            for (int i = 0; i < 7; ++i) {
                const int sl = 2 * i + (lane >> 5), kq = 2 * w + (sl >= 7 ? 1 : 0), nb2 = sl >= 7 ? sl - 7 : sl;
                vb[i] = (unsigned)((8 * kq + ((lane >> 2) & 7)) * 7168 + cb * 448 + nb2 * 64 + (((lane & 3) ^ (2 * (kq & 1))) * 16));
            }
            int slot = 0;
            auto issue = [&](int t) {
                const unsigned s = lb + slot * SLOTM;
                slot = slot == 2 ? 0 : slot + 1;
                const int ks = (t + rb) & 15;
                const char* pb = (const char*)a.w1 + (size_t)ks * 64 * 7168;
#pragma unroll
                for (int i = 0; i < 7; ++i) dma16<false>(pb, vb[i], s + 16384 + (w * 7 + i) * 1024);
#pragma unroll
                for (int n = 0; n < 4; ++n) dma16<false>((const char*)a.x + ks * 128 + n * 32 * 2048, va, s + w * 1024 + n * 4096);
            };
            issue(0);
            { const char* sp = w == 0 ? (const char*)a.b1 + cb * 448 : w == 1 ? (const char*)a.lre + cb * 128 : w == 2 ? (const char*)a.lim + cb * 128 : (const char*)a.ldt + cb * 128;
              if (lane < (w == 0 ? 28 : 8)) dma16<false>(sp, vl, lb + LDS_SM + (w == 0 ? 0 : 384 + w * 128)); }
            issue(1);
            WAITV(12); BAR();
            issue(2);
            { const u32x2 tv = *(const LAS u32x2*)(smem + LDS_TAG);
              tag = ((u64)(unsigned)__builtin_amdgcn_readfirstlane(tv.y) << 32) | (unsigned)__builtin_amdgcn_readfirstlane(tv.x); }
            const char* const pbw = (const char*)((w & 1) ? a.bim : a.bre) + (size_t)(512 * kh + 4 * un + 2 * (w >> 1)) * 2048;
            const unsigned fo = (lane & 8) ? WS_FC + cb * 128 + (lane & 7) * 16 : WS_FY + rb * 128 + (lane & 7) * 16;
            const int lnrow = 512 * (xcd >> 1) + 256 * kh + 8 * rk;
_Pragma("unroll 1")
            for (int t = 0; t < 13; ++t) {
                WAITV(11); BAR();
                issue(t + 3);
                if (t == 1 && w == 0) {
                    const u32x4 v = *(const LAS u32x4*)(smem + LDS_BC + lane * 16);
                    st128(v, rs, lane < 8 ? WS_STAT + (unsigned)(lnrow + lane) * 16u : WS_CSP + (unsigned)(cb * 16 + rb) * 896u + (lane - 8) * 16, 0, 16);
                }
                if (t == 3 && w == 0) __hip_atomic_store((gu64*)(ws + ((lane & 1) ? WS_FC + (cb * 16 + rb) * 8 : WS_FY + ((lnrow >> 7) * 16 + (rk & 15)) * 8)), tag, RLX_AGENT);
                if (t >= 2 && t < 12 && ((t ^ w) & 1)) st128((u32x4){XSENT, XSENT, XSENT, XSENT}, rs, vl + ((t - 2) >> 1) * 1024, WS_XB + (unsigned)(bid * 4 + w) * 8192u, 16);
                if (t == 11) dma16<true>(ws, fo, lb + LDS_E + w * 1024);
                if (t != 0 && t != 12) swarm8(sd, t < 9 ? pbw + (t - 1) * 512 : pw2 + (t - 9) * W2C);
            }
#pragma unroll
            for (int i = 5; i < 8; ++i) st128((u32x4){XSENT, XSENT, XSENT, XSENT}, rs, vl + i * 1024, WS_XB + (unsigned)(bid * 4 + w) * 8192u, 16);
            WAITV(14); BAR();
            check_flags(parea, ws, fo, tag, lane);
#pragma unroll
            for (int n = 0; n < 3; ++n) dma16<true>(ws + WS_CSP + cb * 14336 + (w + 4 * n) * 1024, vl, lb + LDS_GATH + (w + 4 * n) * 1024);
            if (w < 2) dma16<true>(ws + WS_CSP + cb * 14336 + (w + 12) * 1024, vl, lb + LDS_GATH + (w + 12) * 1024);
            else dma16<true>(ws + WS_STAT + rb * 2048 + (w - 2) * 1024, vl, lb + LDS_STAT + (w - 2) * 1024);
            WAITV(0); BAR();
            __hip_atomic_store((gu64*)(ws + WS_XFLAG) + bid * 4 + w, tag, RLX_AGENT);
            {
                const int col = w * 56 + (lane < 56 ? lane : 0);
                float s = 0.f;
#pragma unroll
                for (int i = 0; i < 16; ++i) s += *(const LAS float*)(smem + LDS_GATH + i * 896 + col * 4);
                const float gg = *(const LAS float*)(smem + LDS_STAT + (w * 32 + (lane & 31)) * 16 + 8);
                const float b1v = bf1(*(const LAS u16*)(smem + LDS_SM + col * 2));
                *(LAS float*)(smem + LDS_BC + 1024 + col * 4) = s;
                *(LAS float*)(smem + LDS_BC + col * 4) = b1v;
                *(LAS float*)(smem + LDS_G2 + (w * 32 + (lane & 31)) * 4) = 2.0f * gg;
            }
            issue_s(0);
            __builtin_amdgcn_s_waitcnt(0xC07F);
            BAR();
        }
        const char* const a2 = ws + WS_A2 + (size_t)rb * 88 * 16384 + w * 4096;
        const unsigned vr = (unsigned)((c8 * 128 + 8 * w + (lane >> 3)) * 2048) + swz;
        unsigned vt[2];
        { const int rho = (lane >> 2) & 7;
#pragma unroll
          for (int qq = 0; qq < 2; ++qq) { const int q = 2 * (w & 1) + qq; const int k = 32 * (w >> 1) + (rho < 4 ? 4 * q + rho : 16 + 4 * q + rho - 4);
              vt[qq] = (unsigned)(k * 2048 + c8 * 256 + (lane >> 5) * 64 + (((lane & 3) ^ (2 * qq)) * 16)); } }
        auto issue_gm = [&](int t) {
            const unsigned s = lb + ((t + 2) & 3) * SLOT;
            const int km = kh * 28 + t;
            dma64c(a2 + (size_t)(32 + km) * 16384, vl, s + w * 4096);
            const char* pb = (const char*)a.w2 + (size_t)km * 64 * 2048;
#pragma unroll
            for (int n = 0; n < 4; ++n) dma16<false>(pb + (n & 1) * 128, vt[n >> 1], s + 16384 + (w * 4 + n) * 1024);
        };
        auto issue_gs = [&](int t) {
            const unsigned s = lb + ((t + 2) & 3) * SLOT;
            const int ks = kh * 16 + (t - 28);
            dma64c(a2 + (size_t)ks * 16384, vl, s + w * 4096);
            const char* pb = (const char*)((ks & 1) ? a.cim : a.cre) + (ks >> 1) * 128;
#pragma unroll
            for (int n = 0; n < 4; ++n) dma16<false>(pb + n * 32 * 2048, vr, s + 16384 + (w + 4 * n) * 1024);
        };
        {
_Pragma("unroll 1")
            for (int t = 1; t < 4; ++t) issue_s(t);
            {
                const float lr = bf1(*(const LAS u16*)(smem + LDS_SM + 512 + lane * 2)), li = bf1(*(const LAS u16*)(smem + LDS_SM + 640 + lane * 2));
                const float dt = (float)(__bf16)expf(bf1(*(const LAS u16*)(smem + LDS_SM + 768 + lane * 2)));
                const float zr = dt * lr, zi = dt * li;
                float sn, cs; sincosf(zi, &sn, &cs);
                const float er = expf(zr), Lr = er * cs, Li = er * sn;
                float pr = Lr, pi = Li;
#pragma unroll
                for (int i = 0; i < 4; ++i) cfma(pr, pi, pr, pi, 0.f, 0.f);
                l16 = (f32x2){pr, pi};
                if (w == 0) {
                    const float den = 1.0f / (lr * lr + li * li), nr = Lr - 1.0f;
                    *(LAS f32x4*)(smem + LDS_COEF + lane * 16) = (f32x4){Lr, Li, (nr * lr + Li * li) * den, (Li * lr - nr * li) * den};
                }
                if (w == 2) {
                    float qr = 1.0f, qi = 0.f;
#pragma unroll
                    for (int i = 0; i < 8; ++i) { *(LAS f32x2*)(smem + LDS_PSEG + (i * 64 + lane) * 8) = (f32x2){qr, qi}; cfma(pr, pi, qr, qi, 0.f, 0.f); }
                }
#pragma unroll
                for (int i = 0; i < 3; ++i) cfma(pr, pi, pr, pi, 0.f, 0.f);
                float p2r = pr, p2i = pi; cfma(pr, pi, p2r, p2i, 0.f, 0.f);
                float p4r = p2r, p4i = p2i; cfma(p2r, p2i, p4r, p4i, 0.f, 0.f);
                const int e0 = (rb - 1 - w) & 3, top = (rb - 1 - w) >> 2;
                float qr = (e0 & 1) ? pr : 1.0f, qi = (e0 & 1) ? pi : 0.f;
                if (e0 & 2) cfma(p2r, p2i, qr, qi, 0.f, 0.f);
#pragma unroll
                for (int j = 3; j >= 0; --j) {
                    wt[j] = j <= top ? (f32x2){qr, qi} : (f32x2){0.f, 0.f};
                    if (j <= top) cfma(p4r, p4i, qr, qi, 0.f, 0.f);
                }
                asm volatile("" : "+v"(wt[0]), "+v"(wt[1]), "+v"(wt[2]), "+v"(wt[3]), "+v"(l16));
                __builtin_amdgcn_s_waitcnt(0xC07F);
            }
            WAITV(24); BAR();
            const unsigned fo = WS_FM + (rb * 2 + kh) * 256 + (lane & 15) * 16;
_Pragma("unroll 1")
            for (int t = 0; t < 12; ++t) { WAITV(16); BAR(); issue_s(t + 4); swarm4(sd, pw2 + (3 + (t >> 1)) * W2C + (t & 1) * 256); }
            dma16<true>(ws, fo, lb + LDS_E + w * 1024);
            WAITV(17); BAR();
            WAITV(0); BAR();
            check_flags(parea, ws, fo, tag, lane);
            issue_gm(0);
            BAR();
            issue_gm(1);
            BAR();
            issue_gm(2); issue_gm(3);
        }
        {
            const unsigned fo = (lane & 16) ? WS_XFLAG + (bid ^ 1) * 32 + (lane & 1) * 16 : WS_FS + (rb * 2 + kh) * 256 + (lane & 15) * 16;
            const char* const pcr = (const char*)a.cre + (8 * un + 2 * w) * 2048 + kh * 1024;
            const char* const pci = (const char*)a.cim + (8 * un + 2 * w) * 2048 + kh * 1024;
            const unsigned tag_lo = (unsigned)tag, tag_hi = (unsigned)(tag >> 32);
            WAITV(24); BAR();
            if (w == 1) {
                f32x2 e[8];
#pragma unroll
                for (int i = 0; i < 8; ++i) e[i] = *(const LAS f32x2*)(smem + LDS_E + (i * 64 + lane) * 8);
                LDS_FENCE();
                float er = 0.f, ei = 0.f;
#pragma unroll
                for (int i = 0; i < 8; ++i) {
                    *(LAS f32x2*)(smem + LDS_E + (i * 64 + lane) * 8) = (f32x2){er, ei};
                    cfma(l16.x, l16.y, er, ei, e[i].x, e[i].y);
                }
                st128((u32x4){__float_as_uint(er), tag_lo, __float_as_uint(ei), tag_hi}, rs, vl, WS_GRAN + (unsigned)(cb * 16 + rb) * 1024u, 16);
                __builtin_amdgcn_s_waitcnt(0xC07F);
            }
_Pragma("unroll 1")
            for (int t = 0; t < 9; ++t) { WAITV(16); BAR(); issue_gm(t + 4); swarm4(sd, pw2 + (9 + (t >> 1)) * W2C + (t & 1) * 256); }
#pragma unroll
            for (int j = 0; j < 4; ++j) dma16<true>(ws + WS_GRAN + cb * 16384 + (w + 4 * j) * 1024, vl, lb + LDS_GATH + (w * 4 + j) * 1024);
            WAITV(16); BAR(); issue_gm(13); swarm4(sd, pw2 + 13 * W2C + 256);
            WAITV(16); BAR(); issue_gm(14);
            WAITV(16); BAR();
            {
                u32x4 g[4];
                bool ok = true;
#pragma unroll
                for (int j = 0; j < 4; ++j) {
                    g[j] = *(const LAS u32x4*)(smem + LDS_GATH + (w * 4 + j) * 1024 + lane * 16);
                    ok &= (w + 4 * j >= rb) | ((g[j][1] == tag_lo) & (g[j][3] == tag_hi));
                }
                if (!__all(ok)) {
                    const unsigned long long t0 = __builtin_amdgcn_s_memrealtime();
                    do {
                        __builtin_amdgcn_s_sleep(1);
                        ok = true;
                        asm volatile("" ::: "memory");
#pragma unroll
                        for (int j = 0; j < 4; ++j) {
                            g[j] = __builtin_amdgcn_raw_buffer_load_b128(rs, vl + (w + 4 * j) * 1024, WS_GRAN + (unsigned)cb * 16384u, 16);
                            ok &= (w + 4 * j >= rb) | ((g[j][1] == tag_lo) & (g[j][3] == tag_hi));
                        }
                    } while (!__all(ok) && !deadline(t0));
                }
                float sr = 0.f, si = 0.f;
#pragma unroll
                for (int j = 0; j < 4; ++j) {
                    float vr_ = w + 4 * j < rb ? __uint_as_float(g[j][0]) : 0.f, vi_ = w + 4 * j < rb ? __uint_as_float(g[j][2]) : 0.f;
                    cfma(wt[j].x, wt[j].y, vr_, vi_, sr, si); sr = vr_; si = vi_;
                }
                *(LAS f32x2*)(smem + LDS_BC + lane * 32 + w * 8) = (f32x2){sr, si};
                __builtin_amdgcn_s_waitcnt(0xC07F);
            }
            WAITV(8); BAR(); issue_gm(15); issue_gm(16); swarm2x2(sd, pcr);
_Pragma("unroll 1")
            for (int t = 13; t < 21; ++t) { WAITV(16); BAR(); issue_gm(t + 4); if (t < 20) swarm2x2(sd, ((t & 1) ? pci : pcr) + ((t - 12) >> 1) * 128); }
            dma16<true>(ws, fo, lb + LDS_E + w * 1024);
            WAITV(16); BAR(); issue_gm(25);
            WAITV(16); BAR(); issue_gm(26);
            WAITV(16); BAR(); issue_gm(27);
            check_flags(parea, ws, fo, tag, lane);
_Pragma("unroll 1")
            for (int t = 24; t < 40; ++t) { WAITV(16); BAR(); issue_gs(t + 4); if (t < 28) { swarm2x2(sd, pcr + (t - 20) * 128); swarm2x2(sd, pci + (t - 20) * 128); } }
            WAITV(16); BAR();
            {
                const int rl = 16 * w + (lane >> 4);
#pragma unroll
                for (int n = 0; n < 4; ++n) {
                    const int rr = rl + 4 * n;
                    dma16<false>((const char*)a.x + (size_t)(rb * 128 + kh * 32) * 2048 + c8 * 256, (unsigned)((64 * (rr >> 5) + (rr & 31)) * 2048 + (((lane & 15) ^ (rr & 15)) * 16)), lb + 2 * SLOT + (w * 4 + n) * 1024);
                }
                dma4<false>((const char*)((w & 1) ? a.b2 : a.dsk) + c8 * 256, (unsigned)lane * 4u, lb + 2 * SLOT + 16384 + w * 256);
            }
            WAITV(13); BAR();
            WAITV(5); BAR();
            WAITV(0); BAR();
            asm volatile("s_waitcnt lgkmcnt(0)" ::"s"(sd));
        }
        if (bid == 0 && w == 0) {
            const unsigned long long t0 = __builtin_amdgcn_s_memrealtime();
            for (;;) {
                bool ok = true;
#pragma unroll
                for (int i = 0; i < 4; ++i) ok &= __hip_atomic_load((gu64*)(ws + WS_FY) + i * 64 + lane, RLX_AGENT) == tag;
                if (__all(ok) || deadline(t0)) break;
                __builtin_amdgcn_s_sleep(2);
            }
            if (lane == 0) __hip_atomic_store((gu64*)(ws + WS_EPOCH), tag, RLX_AGENT);
        }
        return;
    }

    const int wm = wid >> 1, wn = wid & 1;
    const __amdgpu_buffer_rsrc_t rs = __builtin_amdgcn_make_buffer_rsrc(ws, 0, WS_END, 0x00020000);
    STAMP(0);
    {
        if (wid == 0) {
            const u64 t = next_tag(ep);
            if (lane == 0) *(LAS u32x2*)(smem + LDS_TAG) = (u32x2){(unsigned)t, (unsigned)(t >> 32)};
        }
        f32x2 xf[2][8];
        float s[2], gd[2], vs[2];
#pragma unroll
        for (int i = 0; i < 2; ++i) {
            f32x2 s2 = {0.f, 0.f}, g2 = {0.f, 0.f};
#pragma unroll
            for (int h = 0; h < 2; ++h)
#pragma unroll
                for (int j = 0; j < 4; ++j) {
                    const f32x2 v = {bflo(xv[i][h][j]), bfhi(xv[i][h][j])}, gw = {bflo(gv[h][j]), bfhi(gv[h][j])};
                    xf[i][h * 4 + j] = v; s2 += v; g2 += v * gw;
                }
            s[i] = wave_sum(s2.x + s2.y) * (1.0f / 1024.0f); gd[i] = wave_sum(g2.x + g2.y);
        }
#pragma unroll
        for (int i = 0; i < 2; ++i) {
            f32x2 v2 = {0.f, 0.f};
#pragma unroll
            for (int j = 0; j < 8; ++j) { const f32x2 d = xf[i][j] - s[i]; v2 += d * d; }
            vs[i] = wave_sum(v2.x + v2.y);
        }
        const float gl = lane ? gd[1] : gd[0], ml = lane ? s[1] : s[0], vl = lane ? vs[1] : vs[0];
        const float rstd = 1.0f / sqrtf(vl * (1.0f / 1024.0f) + 1e-5f), g = 1.0f / (1.0f + expf(-gl));
        if (lane < 2) *(LAS f32x4*)(smem + LDS_BC + (wid * 2 + lane) * 16) = (f32x4){rstd, -rstd * ml, g, 0.f};
    }
    bf16x8 A0[4], A1[4];
    {
        f32x4 acc[7][4];
        bf16x8 B0[7], B1[7];
        const LaneC c = lane_consts<PH_M1>(wm, wn, lane);
        __builtin_amdgcn_s_waitcnt(0xC07F);
        STAMP(1);
        BAR();
        STAMP(2);
        load_frags<PH_M1, 0, 7>(smem, c, A0, B0);
#pragma unroll
        for (int n = 0; n < 7; ++n)
#pragma unroll
            for (int m = 0; m < 4; ++m) acc[n][m] = (f32x4){0.f, 0.f, 0.f, 0.f};
        SCHED0();
        {
            const bf16x8 ones = {0x3F80, 0x3F80, 0x3F80, 0x3F80, 0x3F80, 0x3F80, 0x3F80, 0x3F80};
            const f32x4 z4 = {0.f, 0.f, 0.f, 0.f};
            f32x4 cs[4];
            load_frags<PH_M1, 1, 7>(smem, c, A1, B1);
            mma<PH_M1, 7>(acc, A0, B0);
            sched_half<18, 28>();
            SCHED0();
            if (wm == 0) {
#pragma unroll
                for (int n = 0; n < 4; ++n) cs[n] = __builtin_amdgcn_mfma_f32_16x16x32_bf16(B0[n], ones, z4, 0, 0, 0);
            } else {
#pragma unroll
                for (int n = 0; n < 4; ++n) cs[n] = __builtin_amdgcn_mfma_f32_16x16x32_bf16(B0[3 + n], ones, z4, 0, 0, 0);
            }
            SCHED0();
            __builtin_amdgcn_s_waitcnt(0xC07F);
            BAR();
            SCHED0();
            if (wm == 0) {
#pragma unroll
                for (int n = 0; n < 4; ++n) cs[n] = __builtin_amdgcn_mfma_f32_16x16x32_bf16(B1[n], ones, cs[n], 0, 0, 0);
            } else {
#pragma unroll
                for (int n = 0; n < 4; ++n) cs[n] = __builtin_amdgcn_mfma_f32_16x16x32_bf16(B1[3 + n], ones, cs[n], 0, 0, 0);
            }
            SCHED0();
            load_frags<PH_M1, 0, 7>(smem + SLOTM, c, A0, B0);
            mma<PH_M1, 7>(acc, A1, B1);
            sched_half<18, 28>();
            LDS_FENCE();
            if ((lane & 15) == 0) {
#pragma unroll
                for (int n = 0; n < 4; ++n) { const int nb = n + 3 * wm, nbt = nb == 6 ? 6 + wn : nb + 8 * wn;
                    *(LAS f32x4*)(smem + LDS_BC + 128 + (nbt * 16 + (lane >> 4) * 4) * 4) = cs[n]; }
            }
            SCHED0();
        }
        int sc = 1;
#define M1STEP() do { const int sn_ = sc == 2 ? 0 : sc + 1; cstep<PH_M1, PH_M1, false, 7>(smem + sc * SLOTM, smem + sn_ * SLOTM, c, c, acc, A0, B0, A1, B1); sc = sn_; } while (0)
_Pragma("unroll 1")
        for (int t = 1; t < 15; ++t) M1STEP();
        cstep<PH_M1, PH_M1, true, 7>(smem + sc * SLOTM, smem, c, c, acc, A0, B0, A1, B1);
        STAMP(3);
        m1_epilogue(acc, smem, rs, rb, cb, wm, wn, lane);
    }
    u64 tag;
    { const u32x2 tv = *(const LAS u32x2*)(smem + LDS_TAG);
      tag = ((u64)(unsigned)__builtin_amdgcn_readfirstlane(tv.y) << 32) | (unsigned)__builtin_amdgcn_readfirstlane(tv.x); }
    f32x4 sv[4][4];
    bf16x8 B0[4], B1[4];
    const int ch = lane & 15, q = lane >> 4, seg = wm * 4 + q;
    {
#pragma unroll
        for (int n = 0; n < 4; ++n)
#pragma unroll
            for (int m = 0; m < 4; ++m) sv[n][m] = (f32x4){0.f, 0.f, 0.f, 0.f};
        const LaneC c = lane_consts<PH_S1>(wm, wn, lane);
        STAMP(4);
        BAR();
        STAMP(5);
        load_frags<PH_S1, 0, 4>(smem + 2 * SLOT, c, A0, B0);
        SCHED0();
#define S1STEP(t) cstep<PH_S1, PH_S1, false, 4>(smem + (((t) + 2) & 3) * SLOT, smem + (((t) + 3) & 3) * SLOT, c, c, sv, A0, B0, A1, B1)
_Pragma("unroll 1")
        for (int t = 0; t < 4; ++t) S1STEP(t);
        signal_flag((gu64*)(ws + WS_FM) + (rb * 2 + kh) * 32 + c8 * 4 + wid, tag, lane);
_Pragma("unroll 1")
        for (int t = 4; t < 15; ++t) S1STEP(t);
        f32x2 lam[2];
#pragma unroll
        for (int cbk = 0; cbk < 2; ++cbk) lam[cbk] = *(const LAS f32x2*)(smem + LDS_COEF + (wn * 32 + cbk * 16 + ch) * 16);
        SCHED0();
        cstep<PH_S1, PH_S1, true, 4>(smem + 1 * SLOT, smem, c, c, sv, A0, B0, A1, B1);
        STAMP(6);
        asm volatile("s_nop 15");
        SCHED0();
        float er[2], ei[2];
#pragma unroll
        for (int cbk = 0; cbk < 2; ++cbk) { er[cbk] = sv[cbk][0][0]; ei[cbk] = sv[2 + cbk][0][0]; }
#pragma unroll
        for (int i = 1; i < 16; ++i)
#pragma unroll
            for (int cbk = 0; cbk < 2; ++cbk) cfma(lam[cbk].x, lam[cbk].y, er[cbk], ei[cbk], sv[cbk][i >> 2][i & 3], sv[2 + cbk][i >> 2][i & 3]);
#pragma unroll
        for (int cbk = 0; cbk < 2; ++cbk) *(LAS f32x2*)(smem + LDS_E + (seg * 64 + wn * 32 + cbk * 16 + ch) * 8) = (f32x2){er[cbk], ei[cbk]};
        STAMP(7);
    }
    {
        f32x4 acc[4][4];
#pragma unroll
        for (int n = 0; n < 4; ++n)
#pragma unroll
            for (int m = 0; m < 4; ++m) acc[n][m] = (f32x4){0.f, 0.f, 0.f, 0.f};
        const LaneC cm = lane_consts<PH_GM>(wm, wn, lane);
        __builtin_amdgcn_s_waitcnt(0xC07F);
        STAMP(8);
        BAR();
        STAMP(9);
        load_frags<PH_GM, 0, 4>(smem + 2 * SLOT, cm, A0, B0);
        SCHED0();
#define GMSTEP(t) cstep<PH_GM, PH_GM, false, 4>(smem + (((t) + 2) & 3) * SLOT, smem + (((t) + 3) & 3) * SLOT, cm, cm, acc, A0, B0, A1, B1)
_Pragma("unroll 1")
        for (int t = 0; t < 13; ++t) GMSTEP(t);
        STAMP(10);
        {
            f32x4 g2[4], k0[2]; float zr[2], zi[2];
#pragma unroll
            for (int cbk = 0; cbk < 2; ++cbk) {
                const int cl = wn * 32 + cbk * 16 + ch;
                const f32x2 c0 = *(const LAS f32x2*)(smem + LDS_E + (seg * 64 + cl) * 8), ps = *(const LAS f32x2*)(smem + LDS_PSEG + (seg * 64 + cl) * 8);
                const f32x4 sp = *(const LAS f32x4*)(smem + LDS_BC + cl * 32) + *(const LAS f32x4*)(smem + LDS_BC + cl * 32 + 16);
                zr[cbk] = sp[0] + sp[2]; zi[cbk] = sp[1] + sp[3];
                cfma(ps.x, ps.y, zr[cbk], zi[cbk], c0.x, c0.y);
                k0[cbk] = *(const LAS f32x4*)(smem + LDS_COEF + (wn * 32 + cbk * 16 + ch) * 16);
            }
#pragma unroll
            for (int m = 0; m < 4; ++m) g2[m] = *(const LAS f32x4*)(smem + LDS_G2 + (seg * 16 + m * 4) * 4);
            const unsigned so = WS_A2 + (unsigned)(rb * 88 + 2 * cb) * 16384u;
#pragma unroll
            for (int th = 0; th < 2; ++th) {
#pragma unroll
                for (int i = th * 8; i < th * 8 + 8; ++i)
#pragma unroll
                    for (int cbk = 0; cbk < 2; ++cbk) {
                        cfma(k0[cbk][0], k0[cbk][1], zr[cbk], zi[cbk], sv[cbk][i >> 2][i & 3], sv[2 + cbk][i >> 2][i & 3]);
                        sv[cbk][i >> 2][i & 3] = zr[cbk]; sv[2 + cbk][i >> 2][i & 3] = zi[cbk];
                    }
#pragma unroll
                for (int cbk = 0; cbk < 2; ++cbk) {
                    const int kb = 4 * wn + 2 * cbk + (ch >> 3);
                    const unsigned vo = (unsigned)((kb * 8 + seg) * 256 + (((ch & 7) ^ (4 * (kb & 1))) * 16));
                    u32x4 vr, vi;
#pragma unroll
                    for (int mh = 0; mh < 2; ++mh) {
                        const f32x4 re = sv[cbk][th * 2 + mh], im = sv[2 + cbk][th * 2 + mh];
                        const f32x4 hr4 = (re * k0[cbk][2] - im * k0[cbk][3]) * g2[th * 2 + mh], hi4 = (im * -k0[cbk][2] - re * k0[cbk][3]) * g2[th * 2 + mh];
                        vr[mh * 2] = pkbf(hr4[0], hr4[1]); vr[mh * 2 + 1] = pkbf(hr4[2], hr4[3]);
                        vi[mh * 2] = pkbf(hi4[0], hi4[1]); vi[mh * 2 + 1] = pkbf(hi4[2], hi4[3]);
                    }
                    st128(vr, rs, vo + th * 128, so, 16);
                    st128(vi, rs, vo + th * 128, so + 16384u, 16);
                }
            }
            SCHED0();
        }
        STAMP(11);
_Pragma("unroll 1")
        for (int t = 13; t < 16; ++t) GMSTEP(t);
        signal_flag((gu64*)(ws + WS_FS) + (rb * 2 + kh) * 32 + c8 * 4 + wid, tag, lane);
_Pragma("unroll 1")
        for (int t = 16; t < 27; ++t) GMSTEP(t);
        const LaneC cs = lane_consts<PH_GS>(wm, wn, lane);
        float og[4];
#pragma unroll
        for (int m = 0; m < 4; ++m) og[m] = 1.0f - *(const LAS float*)(smem + LDS_STAT + (wm * 64 + m * 16 + ch) * 16 + 8);
        SCHED0();
        cstep<PH_GM, PH_GS, false, 4>(smem + 1 * SLOT, smem + 2 * SLOT, cm, cs, acc, A0, B0, A1, B1);
#pragma unroll
        for (int n = 0; n < 4; ++n)
#pragma unroll
            for (int m = 0; m < 4; ++m) acc[n][m] *= og[m];
        SCHED0();
        STAMP(12);
_Pragma("unroll 1")
        for (int t = 28; t < 43; ++t) cstep<PH_GS, PH_GS, false, 4>(smem + ((t + 2) & 3) * SLOT, smem + ((t + 3) & 3) * SLOT, cs, cs, acc, A0, B0, A1, B1);
        cstep<PH_GS, PH_GS, true, 4>(smem + 1 * SLOT, smem, cs, cs, acc, A0, B0, A1, B1);
        const int r = ch;
        const int t0r = rb * 128 + wm * 64 + kh * 32 + r, d0 = c8 * 128 + wn * 64 + q * 4;

        STAMP(13);
        if (kh) {
#pragma unroll
            for (int n = 0; n < 4; ++n)
#pragma unroll
                for (int mm = 0; mm < 2; ++mm) { const f32x4 t = acc[n][mm]; acc[n][mm] = acc[n][2 + mm]; acc[n][2 + mm] = t; }
        }
        const unsigned xo = WS_XB + (unsigned)(bid * 4 + wid) * 8192u, xi = WS_XB + (unsigned)((bid ^ 1) * 4 + wid) * 8192u;
#pragma unroll
        for (int n = 0; n < 4; ++n)
#pragma unroll
            for (int mm = 0; mm < 2; ++mm) st128(__builtin_bit_cast(u32x4, acc[n][2 + mm]), rs, lane * 16 + (n * 2 + mm) * 1024, xo, 16);
        const __amdgpu_buffer_rsrc_t ro = __builtin_amdgcn_make_buffer_rsrc(a.out, 0, 8u << 20, 0x00020000);
        f32x4 o[4][2];
#pragma unroll
        for (int mm = 0; mm < 2; ++mm) {
            const float g = *(const LAS float*)(smem + LDS_STAT + (wm * 64 + kh * 32 + mm * 16 + r) * 16 + 8);
#pragma unroll
            for (int n = 0; n < 4; ++n) {
                const u32x2 xw = *(const LAS u32x2*)(smem + 2 * SLOT + (wm * 32 + mm * 16 + r) * 256 + (((wn * 8 + n * 2 + (q >> 1)) ^ r) * 16) + (q & 1) * 8);
                const u32x2 dw = *(const LAS u32x2*)(smem + 2 * SLOT + 16384 + (wn * 64 + n * 16 + q * 4) * 2), bw = *(const LAS u32x2*)(smem + 2 * SLOT + 16384 + 256 + (wn * 64 + n * 16 + q * 4) * 2);
                const f32x4 own = acc[n][mm];
                o[n][mm][0] = own[0] + g * (bflo(xw.x) * bflo(dw.x)) + (1.0f - g) * bflo(bw.x);
                o[n][mm][1] = own[1] + g * (bfhi(xw.x) * bfhi(dw.x)) + (1.0f - g) * bfhi(bw.x);
                o[n][mm][2] = own[2] + g * (bflo(xw.y) * bflo(dw.y)) + (1.0f - g) * bflo(bw.y);
                o[n][mm][3] = own[3] + g * (bfhi(xw.y) * bfhi(dw.y)) + (1.0f - g) * bfhi(bw.y);
                asm volatile("" : "+v"(o[n][mm]));
            }
        }
        STAMP(14);
        {
            u32x4 pv[8];
            const unsigned long long t0 = __builtin_amdgcn_s_memrealtime();
            for (;;) {
                asm volatile("" ::: "memory");
                unsigned mx = 0;
#pragma unroll
                for (int i = 0; i < 8; ++i) pv[i] = __builtin_amdgcn_raw_buffer_load_b128(rs, lane * 16 + i * 1024, xi, 16);
#pragma unroll
                for (int i = 0; i < 8; ++i) mx = max(max(mx, max(pv[i][0], pv[i][1])), max(pv[i][2], pv[i][3]));
                if (__all(mx != XSENT) || deadline(t0)) break;
                unsigned last;
                do { __builtin_amdgcn_s_sleep(2); asm volatile("" ::: "memory");
                     last = __builtin_amdgcn_raw_buffer_load_b32(rs, 8188, xi, 16);
                } while (__builtin_amdgcn_readfirstlane(last) == XSENT && !deadline(t0));
            }
            STAMP(15);
#pragma unroll
            for (int n = 0; n < 4; ++n)
#pragma unroll
                for (int mm = 0; mm < 2; ++mm) o[n][mm] += __builtin_bit_cast(f32x4, pv[n * 2 + mm]);
        }
#pragma unroll
        for (int mm = 0; mm < 2; ++mm)
#pragma unroll
            for (int n = 0; n < 4; ++n) __builtin_amdgcn_raw_buffer_store_b128(__builtin_bit_cast(u32x4, o[n][mm]), ro, ((t0r + mm * 16) * 1024 + d0 + n * 16) * 4, 0, 17);
    }
}
}
extern "C" __global__ void gm_code_end() {}

extern "C" void kernel_launch(void* const* d_in, const int* in_sizes, int n_in,
                              void* d_out, int out_size, void* d_ws, size_t ws_size,
                              hipStream_t stream) {
    using namespace gm;
    Args a;
    a.x = (const u16*)d_in[0]; a.lre = (const u16*)d_in[1]; a.lim = (const u16*)d_in[2]; a.bre = (const u16*)d_in[3]; a.bim = (const u16*)d_in[4];
    a.cre = (const u16*)d_in[5]; a.cim = (const u16*)d_in[6]; a.ldt = (const u16*)d_in[7]; a.dsk = (const u16*)d_in[8];
    a.w1 = (const u16*)d_in[10]; a.b1 = (const u16*)d_in[11]; a.w2 = (const u16*)d_in[12]; a.b2 = (const u16*)d_in[13]; a.gw = (const u16*)d_in[14];
    a.out = (float*)d_out; a.ws = (char*)d_ws;
    (void)hipFuncSetAttribute((const void*)fused_kernel, hipFuncAttributeMaxDynamicSharedMemorySize, LDS_BYTES);
    fused_kernel<<<256, 512, LDS_BYTES, stream>>>(a);
}
```

```cpp
#include <hip/hip_runtime.h>
#include <hip/hip_bf16.h>

namespace gm {
#define LAS __attribute__((address_space(3)))
#define GAS __attribute__((address_space(1)))
typedef unsigned short u16;
typedef short bf16x8 __attribute__((ext_vector_type(8)));
typedef short s16x4 __attribute__((ext_vector_type(4)));
typedef float f32x4 __attribute__((ext_vector_type(4)));
typedef float f32x2 __attribute__((ext_vector_type(2)));
typedef unsigned u32x4 __attribute__((ext_vector_type(4)));
typedef unsigned u32x2 __attribute__((ext_vector_type(2)));
typedef LAS char* ldsp;
typedef GAS unsigned gu32;
typedef unsigned long long u64;
typedef GAS u64 gu64;

#define BAR() __builtin_amdgcn_s_barrier()
#define WAITV(n) asm volatile("s_waitcnt vmcnt(%0)" ::"i"(n) : "memory")
#define SCHED0() __builtin_amdgcn_sched_barrier(0)
#define LDS_FENCE() do { SCHED0(); __builtin_amdgcn_s_waitcnt(0xC07F); asm volatile("" ::: "memory"); SCHED0(); } while (0)
#define RLX_AGENT __ATOMIC_RELAXED, __HIP_MEMORY_SCOPE_AGENT

constexpr unsigned WS_EPOCH = 0;
constexpr unsigned WS_FY = 4096;
constexpr unsigned WS_FC = 12288;
constexpr unsigned WS_FS = 16384;
constexpr unsigned WS_FM = 24576;
constexpr unsigned WS_XFLAG = 32768;
constexpr unsigned WS_GRAN = 131072;
constexpr unsigned WS_STAT = 1u << 20;
constexpr unsigned WS_CSP = 2u << 20;
constexpr unsigned WS_A2 = 8u << 20;
constexpr unsigned WS_XB = 32u << 20;
constexpr unsigned WS_END = 40u << 20;

constexpr int SLOT = 32768, SLOTM = 45056;
constexpr int LDS_PSEG = 131072;
constexpr int LDS_E = 135168;
constexpr int LDS_COEF = LDS_E + 4096;
constexpr int LDS_BC = LDS_COEF + 1024;
constexpr int LDS_STAT = LDS_BC + 2048;
constexpr int LDS_G2 = LDS_STAT + 2048;
constexpr int LDS_DUMMY = LDS_G2;
constexpr int LDS_TAG = LDS_G2 + 512;
constexpr int LDS_GATH = LDS_TAG + 64;
constexpr int LDS_SM = LDS_GATH + 16384;
constexpr int LDS_BYTES = LDS_SM + 1024;
static_assert(LDS_BYTES <= 160 * 1024, "LDS");

constexpr unsigned XSENT = 0xFFFFFFFFu;

enum { PH_S1 = 0, PH_M1 = 1, PH_GS = 2, PH_GM = 3 };

struct Args {
    const u16 *x, *lre, *lim, *bre, *bim, *cre, *cim, *ldt, *dsk, *w1, *b1, *w2, *b2, *gw;
    float* out;
    char* ws;
};

__device__ __forceinline__ bf16x8 lds128(ldsp p) { return *(const LAS bf16x8*)p; }
__device__ __forceinline__ s16x4 ldstr(ldsp p) { return __builtin_amdgcn_ds_read_tr16_b64_v4i16((LAS s16x4*)p); }
__device__ __forceinline__ bf16x8 cat(s16x4 a, s16x4 b) { return __builtin_shufflevector(a, b, 0, 1, 2, 3, 4, 5, 6, 7); }
template <bool C> __device__ __forceinline__ void dma16(const void* sb, unsigned vo, unsigned la) {
    if constexpr (C) asm volatile("s_mov_b32 m0, %0\n\ts_nop 0\n\tglobal_load_lds_dwordx4 %1, %2 sc1" ::"s"(la), "v"(vo), "s"(sb) : "memory");
    else asm volatile("s_mov_b32 m0, %0\n\ts_nop 0\n\tglobal_load_lds_dwordx4 %1, %2" ::"s"(la), "v"(vo), "s"(sb) : "memory");
}
__device__ __forceinline__ void dma64c(const void* sb, unsigned vo, unsigned la) {
    asm volatile("s_mov_b32 m0, %0\n\ts_nop 0\n\tglobal_load_lds_dwordx4 %1, %2 sc1\n\tglobal_load_lds_dwordx4 %1, %2 offset:1024 sc1\n\t"
                 "global_load_lds_dwordx4 %1, %2 offset:2048 sc1\n\tglobal_load_lds_dwordx4 %1, %2 offset:3072 sc1" ::"s"(la), "v"(vo), "s"(sb) : "memory");
}
template <bool C> __device__ __forceinline__ void dma4(const void* sb, unsigned vo, unsigned la) {
    if constexpr (C) asm volatile("s_mov_b32 m0, %0\n\ts_nop 0\n\tglobal_load_lds_dword %1, %2 sc1" ::"s"(la), "v"(vo), "s"(sb) : "memory");
    else asm volatile("s_mov_b32 m0, %0\n\ts_nop 0\n\tglobal_load_lds_dword %1, %2" ::"s"(la), "v"(vo), "s"(sb) : "memory");
}
__device__ __forceinline__ void swarm8(unsigned sd, const void* p) {
    asm volatile("s_load_dword %0, %1, 0x0\n\ts_load_dword %0, %1, 0x40\n\ts_load_dword %0, %1, 0x80\n\ts_load_dword %0, %1, 0xc0\n\t"
                 "s_load_dword %0, %1, 0x100\n\ts_load_dword %0, %1, 0x140\n\ts_load_dword %0, %1, 0x180\n\ts_load_dword %0, %1, 0x1c0" ::"s"(sd), "s"(p));
}
__device__ __forceinline__ void swarm4(unsigned sd, const void* p) {
    asm volatile("s_load_dword %0, %1, 0x0\n\ts_load_dword %0, %1, 0x40\n\ts_load_dword %0, %1, 0x80\n\ts_load_dword %0, %1, 0xc0" ::"s"(sd), "s"(p));
}
__device__ __forceinline__ void swarm2x2(unsigned sd, const void* p) {
    asm volatile("s_load_dword %0, %1, 0x0\n\ts_load_dword %0, %1, 0x40\n\ts_load_dword %0, %1, 0x800\n\ts_load_dword %0, %1, 0x840" ::"s"(sd), "s"(p));
}
__device__ __forceinline__ void st128(u32x4 v, __amdgpu_buffer_rsrc_t rs, unsigned voff, unsigned base, int) { __builtin_amdgcn_raw_buffer_store_b128(v, rs, voff + base, 0, 16); }
__device__ __forceinline__ void st64(u32x2 v, __amdgpu_buffer_rsrc_t rs, unsigned voff, unsigned base, int) { __builtin_amdgcn_raw_buffer_store_b64(v, rs, voff + base, 0, 16); }
__device__ __forceinline__ float bflo(unsigned w) { return __uint_as_float(w << 16); }
__device__ __forceinline__ float bfhi(unsigned w) { return __uint_as_float(w & 0xffff0000u); }
__device__ __forceinline__ float bf1(u16 b) { return __uint_as_float((unsigned)b << 16); }
__device__ __forceinline__ unsigned pkbf(float lo, float hi) {
    typedef __bf16 bf2_t __attribute__((ext_vector_type(2)));
    bf2_t v = {(__bf16)lo, (__bf16)hi};
    return __builtin_bit_cast(unsigned, v);
}
__device__ __forceinline__ void cfma(float lr, float li, float& zr, float& zi, float ur, float ui) {
    float tr, ti;
    asm("v_fma_f32 %0, -%3, %5, %6\n\tv_fma_f32 %1, %3, %4, %7\n\tv_fma_f32 %0, %2, %4, %0\n\tv_fma_f32 %1, %2, %5, %1"
        : "=&v"(tr), "=&v"(ti) : "v"(lr), "v"(li), "v"(zr), "v"(zi), "v"(ur), "v"(ui));
    zr = tr; zi = ti;
}
__device__ __forceinline__ bool deadline(unsigned long long t0) { return __builtin_amdgcn_s_memrealtime() - t0 > 400000ull; }

struct LaneC { unsigned a0, a1, b0, b1, b2; };
template <int PH>
__device__ __forceinline__ LaneC lane_consts(unsigned base, int wm, int wn, int lane) {
    const int r = lane & 15, q = lane >> 4, rr = r >> 2, p = lane & 3;
    const unsigned rl = r * 128 + ((q ^ (r >> 1)) * 16);
    LaneC c; c.b2 = 0;
    if constexpr (PH == PH_S1) { c.a0 = wm * 8192 + rl; c.a1 = c.a0 ^ 64; c.b0 = 16384 + wn * 8192 + rl; c.b1 = c.b0 ^ 64; }
    if constexpr (PH == PH_M1) {
        c.a0 = wm * 8192 + rl; c.a1 = c.a0 ^ 64;
        const unsigned t0 = 16384 + q * 7 * 512 + rr * 64 + ((q & 1) * 32) + p * 8;
        c.b0 = t0 + wn * 2048;
        c.b1 = (t0 ^ 32) + wn * 2048;
        c.b2 = t0 ^ (wn * 32);
    }
    if constexpr (PH == PH_GS) {
        c.a0 = (q * 8 + wm * 4) * 256 + (p >> 1) * 128 + (4 * (q & 1) + rr) * 16 + (p & 1) * 8; c.a1 = c.a0 ^ 64;
        c.b0 = 16384 + wn * 8192 + rl; c.b1 = c.b0 ^ 64;
    }
    if constexpr (PH == PH_GM) {
        c.a0 = wm * 8192 + lane * 16; c.a1 = c.a0;
        c.b0 = 16384 + q * 4 * 512 + wn * 1024 + rr * 64 + ((q & 1) * 32) + p * 8; c.b1 = c.b0 ^ 32;
    }
    c.a0 += base; c.a1 += base; c.b0 += base; c.b1 += base; c.b2 += base;
    return c;
}
template <int PH> __device__ __forceinline__ void advance(LaneC& c, unsigned d) {
    c.a0 += d; c.b0 += d; c.b1 += d;
    if constexpr (PH != PH_GM) c.a1 += d;
    if constexpr (PH == PH_M1) c.b2 += d;
    if constexpr (PH == PH_M1) asm volatile("" : "+v"(c.a0), "+v"(c.a1), "+v"(c.b0), "+v"(c.b1), "+v"(c.b2));
    else if constexpr (PH == PH_GM) asm volatile("" : "+v"(c.a0), "+v"(c.b0), "+v"(c.b1));
    else asm volatile("" : "+v"(c.a0), "+v"(c.a1), "+v"(c.b0), "+v"(c.b1));
}
template <int PH, int KK, int NB>
__device__ __forceinline__ void load_frags(const LaneC& c, bf16x8 (&A)[4], bf16x8 (&B)[NB]) {
    if constexpr (PH == PH_S1 || PH == PH_M1) {
        ldsp pa = (ldsp)(size_t)(KK ? c.a1 : c.a0);
#pragma unroll
        for (int m = 0; m < 4; ++m) A[m] = lds128(pa + m * 2048);
    } else if constexpr (PH == PH_GS) {
        ldsp p0 = (ldsp)(size_t)c.a0, p1 = (ldsp)(size_t)c.a1;
#pragma unroll
        for (int m = 0; m < 4; ++m) A[m] = cat(ldstr(p0 + (KK * 32 + m) * 256), ldstr(p1 + (KK * 32 + m) * 256));
    } else {
        ldsp pa = (ldsp)(size_t)c.a0;
#pragma unroll
        for (int m = 0; m < 4; ++m) A[m] = lds128(pa + (m * 2 + KK) * 1024);
    }
    if constexpr (PH == PH_S1 || PH == PH_GS) {
        ldsp pb = (ldsp)(size_t)(KK ? c.b1 : c.b0);
#pragma unroll
        for (int n = 0; n < NB; ++n) B[n] = lds128(pb + n * 2048);
    } else {
        constexpr int NS2 = PH == PH_M1 ? 7 : 4;
#pragma unroll
        for (int n = 0; n < NB; ++n) { ldsp p = (ldsp)(size_t)(n == 6 ? c.b2 : (n & 1) ? c.b1 : c.b0) + KK * 4 * NS2 * 512 + (n >> 1) * 512; B[n] = cat(ldstr(p), ldstr(p + 256)); }
    }
}
template <int PH, int NB>
__device__ __forceinline__ void mma(f32x4 (&acc)[NB][4], const bf16x8 (&A)[4], const bf16x8 (&B)[NB]) {
#pragma unroll
    for (int n = 0; n < NB; ++n)
#pragma unroll
        for (int mi = 0; mi < 4; ++mi) {
            const int m = (n & 1) ? 3 - mi : mi;
            if constexpr (PH == PH_S1) acc[n][m] = __builtin_amdgcn_mfma_f32_16x16x32_bf16(A[m], B[n], acc[n][m], 0, 0, 0);
            else acc[n][m] = __builtin_amdgcn_mfma_f32_16x16x32_bf16(B[n], A[m], acc[n][m], 0, 0, 0);
        }
}
template <int PH> constexpr int nreads() { return PH == PH_S1 ? 8 : PH == PH_M1 ? 18 : 12; }
template <int NR, int NM, int NV = 0>
__device__ __forceinline__ void sched_half() {
#pragma unroll
    for (int i = 0; i < NR; ++i) { __builtin_amdgcn_sched_group_barrier(0x8, 1, 0); __builtin_amdgcn_sched_group_barrier(0x100, 1, 0); }
    if constexpr (NV > 0) {
#pragma unroll
        for (int i = 0; i < (NV + 1) / 2; ++i) { __builtin_amdgcn_sched_group_barrier(0x8, 1, 0); __builtin_amdgcn_sched_group_barrier(0x2, 2, 0); }
    }
    __builtin_amdgcn_sched_group_barrier(0x8, NM - NR - (NV + 1) / 2, 0);
}
template <int PH, int NB>
__device__ __forceinline__ void cstep(LaneC& c, unsigned d, f32x4 (&acc)[NB][4], bf16x8 (&A0)[4], bf16x8 (&B0)[NB], bf16x8 (&A1)[4], bf16x8 (&B1)[NB]) {
    load_frags<PH, 1, NB>(c, A1, B1);
    mma<PH, NB>(acc, A0, B0);
    advance<PH>(c, d);
    sched_half<nreads<PH>(), 4 * NB, PH == PH_M1 ? 5 : 4>();
    SCHED0();
    __builtin_amdgcn_s_waitcnt(0xC07F);
    BAR();
    SCHED0();
    load_frags<PH, 0, NB>(c, A0, B0);
    mma<PH, NB>(acc, A1, B1);
    sched_half<nreads<PH>(), 4 * NB>();
}
template <int PC, int PN, bool LAST, int NB>
__device__ __forceinline__ void cstep2(const LaneC& cc, const LaneC& cn, f32x4 (&acc)[NB][4], bf16x8 (&A0)[4], bf16x8 (&B0)[NB], bf16x8 (&A1)[4], bf16x8 (&B1)[NB]) {
    SCHED0();
    load_frags<PC, 1, NB>(cc, A1, B1);
    mma<PC, NB>(acc, A0, B0);
    sched_half<nreads<PC>(), 4 * NB>();
    SCHED0();
    __builtin_amdgcn_s_waitcnt(0xC07F);
    BAR();
    SCHED0();
    if constexpr (!LAST) load_frags<PN, 0, NB>(cn, A0, B0);
    mma<PC, NB>(acc, A1, B1);
    if constexpr (!LAST) sched_half<nreads<PN>(), 4 * NB>();
    SCHED0();
}
__device__ __forceinline__ unsigned ringd(int t) { return ((t + 3) & 3) ? (unsigned)SLOT : (unsigned)(-3 * SLOT); }

__device__ __forceinline__ u64 next_tag(u64 e) { return e * 0x9E3779B97F4A7C15ull + 0xD1B54A32D192ED03ull; }
__device__ __forceinline__ void wait_flags(gu64* fl, u64 v, u64 tag) {
    if (!__all(v == tag)) {
        const unsigned long long t0 = __builtin_amdgcn_s_memrealtime();
        do { __builtin_amdgcn_s_sleep(2); v = __hip_atomic_load(fl, RLX_AGENT); } while (!__all(v == tag) && !deadline(t0));
    }
    asm volatile("" ::: "memory");
}
__device__ __forceinline__ void signal_flag(gu64* fl, u64 tag, int lane) {
    WAITV(0);
    if (lane == 0) __hip_atomic_store(fl, tag, RLX_AGENT);
}
template <int CTRL, int RMASK> __device__ __forceinline__ float dpp_add(float v) {
    return v + __builtin_bit_cast(float, __builtin_amdgcn_update_dpp(0, __builtin_bit_cast(int, v), CTRL, RMASK, 0xf, true));
}
__device__ __forceinline__ float wave_sum(float v) {
    v = dpp_add<0x111, 0xf>(v); v = dpp_add<0x112, 0xf>(v); v = dpp_add<0x114, 0xf>(v); v = dpp_add<0x118, 0xf>(v);
    v = dpp_add<0x142, 0xa>(v); v = dpp_add<0x143, 0xc>(v);
    return __builtin_bit_cast(float, __builtin_amdgcn_readlane(__builtin_bit_cast(int, v), 63));
}
#define STAMP(i) do { } while (0)

__device__ __forceinline__ unsigned gelu2(f32x2 v) {
    const f32x2 t = (v * v) * -0.1029432397f + -2.3022081986f;
    const f32x2 u = v * t;
    f32x2 d; d.x = __builtin_amdgcn_exp2f(u.x); d.y = __builtin_amdgcn_exp2f(u.y);
    d = d + 1.0f;
    f32x2 r; r.x = __builtin_amdgcn_rcpf(d.x); r.y = __builtin_amdgcn_rcpf(d.y);
    const f32x2 o = v * r;
    return pkbf(o.x, o.y);
}

__device__ __forceinline__ void m1_epilogue(f32x4 (&acc)[7][4], ldsp smem, __amdgpu_buffer_rsrc_t rs, int rb, int cb, int wm, int wn, int lane) {
    f32x2 st[4];
#pragma unroll
    for (int m = 0; m < 4; ++m) st[m] = *(const LAS f32x2*)(smem + LDS_STAT + (wm * 64 + m * 16 + (lane & 15)) * 16);
    u32x2 pk[7][4];
#pragma unroll
    for (int n = 0; n < 7; ++n) {
        const int nbt = n == 6 ? 6 + wn : n + 8 * wn;
        const f32x4 b = *(const LAS f32x4*)(smem + LDS_BC + (nbt * 16 + (lane >> 4) * 4) * 4), cs = *(const LAS f32x4*)(smem + LDS_BC + 1024 + (nbt * 16 + (lane >> 4) * 4) * 4);
#pragma unroll
        for (int m = 0; m < 4; ++m) {
            const f32x4 v = acc[n][m] * st[m].x + (cs * st[m].y + b);
            pk[n][m].x = gelu2((f32x2){v[0], v[1]});
            pk[n][m].y = gelu2((f32x2){v[2], v[3]});
        }
        if ((n & 1) || n == 6) {
            const int pair = 7 * cb + (nbt >> 1);
            const unsigned base = WS_A2 + (unsigned)(rb * 88 + 32 + (pair >> 1)) * 16384u + (unsigned)(pair & 1) * 1024u + (unsigned)(wm * 4) * 2048u;
#pragma unroll
            for (int m = 0; m < 4; ++m) {
                if (n == 6) st64(pk[n][m], rs, lane * 16 + m * 2048, base + wn * 8, 16);
                else { const u32x4 v = {pk[n - (n & 1)][m].x, pk[n - (n & 1)][m].y, pk[n][m].x, pk[n][m].y}; st128(v, rs, lane * 16 + m * 2048, base, 16); }
            }
        }
    }
}

__device__ __forceinline__ void colsum_half(const bf16x8 (&B)[7], ldsp dst, int wm, int wn, int lane) {
    const bf16x8 ones = {0x3F80, 0x3F80, 0x3F80, 0x3F80, 0x3F80, 0x3F80, 0x3F80, 0x3F80};
    const f32x4 z4 = {0.f, 0.f, 0.f, 0.f};
    f32x4 cs[4];
    if (wm == 0) {
#pragma unroll
        for (int n = 0; n < 4; ++n) cs[n] = __builtin_amdgcn_mfma_f32_16x16x32_bf16(B[n], ones, z4, 0, 0, 0);
    } else {
#pragma unroll
        for (int n = 0; n < 4; ++n) cs[n] = __builtin_amdgcn_mfma_f32_16x16x32_bf16(B[3 + n], ones, z4, 0, 0, 0);
    }
    if ((lane & 15) == 0) {
#pragma unroll
        for (int n = 0; n < 4; ++n) { const int nb = n + 3 * wm, nbt = nb == 6 ? 6 + wn : nb + 8 * wn;
            *(LAS f32x4*)(dst + (nbt * 16 + (lane >> 4) * 4) * 4) = cs[n]; }
    }
    SCHED0();
}

__device__ __forceinline__ void check_flags(ldsp area, const char* ws, unsigned off, u64 tag, int lane) {
    const u32x4 v = *(const LAS u32x4*)(area + lane * 16);
    const unsigned lo = (unsigned)tag, hi = (unsigned)(tag >> 32);
    if (!__all(v[0] == lo && v[1] == hi && v[2] == lo && v[3] == hi)) {
        const unsigned long long t0 = __builtin_amdgcn_s_memrealtime();
        bool ok;
        do { __builtin_amdgcn_s_sleep(2);
             ok = __hip_atomic_load((gu64*)(ws + off), RLX_AGENT) == tag && __hip_atomic_load((gu64*)(ws + off + 8), RLX_AGENT) == tag;
        } while (!__all(ok) && !deadline(t0));
    }
    asm volatile("" ::: "memory");
}

__global__ __launch_bounds__(512, 2) void fused_kernel(Args a) {
    extern __shared__ __attribute__((aligned(16))) char smem_[];
    ldsp smem = (ldsp)smem_;
    const int wid = __builtin_amdgcn_readfirstlane(threadIdx.x >> 6), lane = threadIdx.x & 63;
    const int bid = blockIdx.x, xcd = bid & 7, rk = bid >> 3;
    const int kh = xcd & 1, rb = (xcd >> 1) * 4 + (rk >> 3), c8 = rk & 7, cb = kh * 8 + c8;
    char* const ws = a.ws;
    const char* pc; unsigned code_len;
    asm volatile("s_getpc_b64 %0\n.Lgm_pc%=:\n\ts_mov_b32 %1, gm_code_end-.Lgm_pc%=" : "=s"(pc), "=s"(code_len));
    if (wid == 3) {
        const unsigned o = (unsigned)(rk + 32 * (lane & 15)) * 128u;
        dma4<false>(pc, o < code_len && code_len < (1u << 20) ? o : 0u, (unsigned)(size_t)smem + LDS_DUMMY);
    }
    asm volatile("" ::"s"(a.x), "s"(a.lre), "s"(a.lim), "s"(a.bre), "s"(a.bim), "s"(a.cre), "s"(a.cim), "s"(a.ldt), "s"(a.dsk), "s"(a.w1), "s"(a.b1), "s"(a.w2), "s"(a.b2), "s"(a.gw), "s"(a.out), "s"(a.ws));

    u32x4 xv[2][2], gv[2]; u64 ep = 0;
    if (wid < 4) {
        const int lnrow = 512 * (xcd >> 1) + 256 * kh + 8 * rk + 2 * wid;
#pragma unroll
        for (int i = 0; i < 2; ++i)
#pragma unroll
            for (int h = 0; h < 2; ++h) xv[i][h] = *(const u32x4*)(a.x + (size_t)(lnrow + i) * 1024 + h * 512 + lane * 8);
#pragma unroll
        for (int h = 0; h < 2; ++h) gv[h] = *(const u32x4*)(a.gw + h * 512 + lane * 8);
        ep = __hip_atomic_load((gu64*)(ws + WS_EPOCH), RLX_AGENT);
    }
    int wid2 = wid;
    asm volatile("" : "+s"(wid2)::"memory");
    if (__builtin_expect(wid2 >= 4, 1)) {
        __builtin_amdgcn_s_waitcnt(0x0F70);
        const int w = wid - 4, un = (xcd >> 1) * 32 + rk;
        const unsigned lb = (unsigned)(size_t)smem;
        const unsigned swz = (unsigned)(((lane & 7) ^ ((4 * (w & 1) + (lane >> 4)) & 7)) * 16);
        const unsigned vl = lane * 16;
        unsigned sd;
        asm volatile("s_mov_b32 %0, 0" : "=s"(sd));
        ldsp const parea = smem + LDS_E + w * 1024;
        const char* const pw2 = (const char*)a.w2 + (size_t)(1792 * kh + un) * 2048 + w * 512;
        constexpr int W2C = 128 * 2048;
        const __amdgpu_buffer_rsrc_t rs = __builtin_amdgcn_make_buffer_rsrc(ws, 0, WS_END, 0x00020000);
        u64 tag;
        f32x2 l16, wt[4];
        const unsigned vsa = (unsigned)((rb * 128 + 16 * (lane >> 5) + ((lane >> 3) & 3) + 32 * (w & 1) + 4 * (w >> 1)) * 2048) + swz;
        const unsigned vsb = (unsigned)((cb * 64 + 16 * (w >> 1) + 8 * (w & 1) + (lane >> 3)) * 2048) + swz;
        auto issue_s = [&](int t) {
            const unsigned s = lb + ((t + 2) & 3) * SLOT + w * 1024;
            const int ko = ((t + rb + cb) & 15) * 128;
#pragma unroll
            for (int n = 0; n < 4; ++n) dma16<false>((const char*)a.x + ko + (64 * (n >> 1) + 8 * (n & 1)) * 2048, vsa, s + n * 4096);
#pragma unroll
            for (int n = 0; n < 4; ++n) dma16<false>((const char*)((n & 1) ? a.bim : a.bre) + ko + (n >> 1) * 32 * 2048, vsb, s + 16384 + n * 4096);
        };
        {
            const unsigned va = (unsigned)((rb * 128 + 8 * w + (lane >> 3)) * 2048) + swz;
            unsigned vb[7];
#pragma unroll
            for (int i = 0; i < 7; ++i) {
                const int sl = 2 * i + (lane >> 5), kq = 2 * w + (sl >= 7 ? 1 : 0), nb2 = sl >= 7 ? sl - 7 : sl;
                vb[i] = (unsigned)((8 * kq + ((lane >> 2) & 7)) * 7168 + cb * 448 + nb2 * 64 + (((lane & 3) ^ (2 * (kq & 1))) * 16));
            }
            int slot = 0;
            auto issue = [&](int t) {
                const unsigned s = lb + slot * SLOTM;
                slot = slot == 2 ? 0 : slot + 1;
                const int ks = (t + rb) & 15;
                const char* pb = (const char*)a.w1 + (size_t)ks * 64 * 7168;
#pragma unroll
                for (int i = 0; i < 7; ++i) dma16<false>(pb, vb[i], s + 16384 + (w * 7 + i) * 1024);
#pragma unroll
                for (int n = 0; n < 4; ++n) dma16<false>((const char*)a.x + ks * 128 + n * 32 * 2048, va, s + w * 1024 + n * 4096);
            };
            issue(0);
            { const char* sp = w == 0 ? (const char*)a.b1 + cb * 448 : w == 1 ? (const char*)a.lre + cb * 128 : w == 2 ? (const char*)a.lim + cb * 128 : (const char*)a.ldt + cb * 128;
              if (lane < (w == 0 ? 28 : 8)) dma16<false>(sp, vl, lb + LDS_SM + (w == 0 ? 0 : 384 + w * 128)); }
            issue(1);
            WAITV(12); BAR();
            issue(2);
            { const u32x2 tv = *(const LAS u32x2*)(smem + LDS_TAG);
              tag = ((u64)(unsigned)__builtin_amdgcn_readfirstlane(tv.y) << 32) | (unsigned)__builtin_amdgcn_readfirstlane(tv.x); }
            const char* const pbw = (const char*)((w & 1) ? a.bim : a.bre) + (size_t)(512 * kh + 4 * un + 2 * (w >> 1)) * 2048;
            const unsigned fo = (lane & 8) ? WS_FC + cb * 128 + (lane & 7) * 16 : WS_FY + rb * 128 + (lane & 7) * 16;
            const int lnrow = 512 * (xcd >> 1) + 256 * kh + 8 * rk;
_Pragma("unroll 1")
            for (int t = 0; t < 13; ++t) {
                WAITV(11); BAR();
                issue(t + 3);
                if (t == 1 && w == 0) {
                    f32x4 v = *(const LAS f32x4*)(smem + LDS_BC + lane * 16);
                    const f32x4 v2 = *(const LAS f32x4*)(smem + LDS_BC + 1024 + lane * 16);
#pragma unroll
                    for (int j = 0; j < 4; ++j) v[j] = lane < 8 ? v[j] : v[j] + v2[j];
                    st128(__builtin_bit_cast(u32x4, v), rs, lane < 8 ? WS_STAT + (unsigned)(lnrow + lane) * 16u : WS_CSP + (unsigned)(cb * 16 + rb) * 896u + (lane - 8) * 16, 0, 16);
                }
                if (t == 3 && w == 0) __hip_atomic_store((gu64*)(ws + ((lane & 1) ? WS_FC + (cb * 16 + rb) * 8 : WS_FY + ((lnrow >> 7) * 16 + (rk & 15)) * 8)), tag, RLX_AGENT);
                if (t >= 2 && t < 12 && ((t ^ w) & 1)) st128((u32x4){XSENT, XSENT, XSENT, XSENT}, rs, vl + ((t - 2) >> 1) * 1024, WS_XB + (unsigned)(bid * 4 + w) * 8192u, 16);
                if (t == 11) dma16<true>(ws, fo, lb + LDS_E + w * 1024);
                if (t != 0 && t != 12) swarm8(sd, t < 9 ? pbw + (t - 1) * 512 : pw2 + (t - 9) * W2C);
            }
#pragma unroll
            for (int i = 5; i < 8; ++i) st128((u32x4){XSENT, XSENT, XSENT, XSENT}, rs, vl + i * 1024, WS_XB + (unsigned)(bid * 4 + w) * 8192u, 16);
            WAITV(14); BAR();
            check_flags(parea, ws, fo, tag, lane);
#pragma unroll
            for (int n = 0; n < 3; ++n) dma16<true>(ws + WS_CSP + cb * 14336 + (w + 4 * n) * 1024, vl, lb + LDS_GATH + (w + 4 * n) * 1024);
            if (w < 2) dma16<true>(ws + WS_CSP + cb * 14336 + (w + 12) * 1024, vl, lb + LDS_GATH + (w + 12) * 1024);
            else dma16<true>(ws + WS_STAT + rb * 2048 + (w - 2) * 1024, vl, lb + LDS_STAT + (w - 2) * 1024);
            WAITV(0); BAR();
            __hip_atomic_store((gu64*)(ws + WS_XFLAG) + bid * 4 + w, tag, RLX_AGENT);
            {
                const int col = w * 56 + (lane < 56 ? lane : 0);
                float s = 0.f;
#pragma unroll
                for (int i = 0; i < 16; ++i) s += *(const LAS float*)(smem + LDS_GATH + i * 896 + col * 4);
                const float gg = *(const LAS float*)(smem + LDS_STAT + (w * 32 + (lane & 31)) * 16 + 8);
                const float b1v = bf1(*(const LAS u16*)(smem + LDS_SM + col * 2));
                *(LAS float*)(smem + LDS_BC + 1024 + col * 4) = s;
                *(LAS float*)(smem + LDS_BC + col * 4) = b1v;
                *(LAS float*)(smem + LDS_G2 + (w * 32 + (lane & 31)) * 4) = 2.0f * gg;
            }
            issue_s(0);
            __builtin_amdgcn_s_waitcnt(0xC07F);
            BAR();
        }
        const char* const a2 = ws + WS_A2 + (size_t)rb * 88 * 16384 + w * 4096;
        const unsigned vr = (unsigned)((c8 * 128 + 8 * w + (lane >> 3)) * 2048) + swz;
        unsigned vt[2];
        { const int rho = (lane >> 2) & 7;
#pragma unroll
          for (int qq = 0; qq < 2; ++qq) { const int q = 2 * (w & 1) + qq; const int k = 32 * (w >> 1) + (rho < 4 ? 4 * q + rho : 16 + 4 * q + rho - 4);
              vt[qq] = (unsigned)(k * 2048 + c8 * 256 + (lane >> 5) * 64 + (((lane & 3) ^ (2 * qq)) * 16)); } }
        auto issue_gm = [&](int t) {
            const unsigned s = lb + ((t + 2) & 3) * SLOT;
            const int km = kh * 28 + t;
            dma64c(a2 + (size_t)(32 + km) * 16384, vl, s + w * 4096);
            const char* pb = (const char*)a.w2 + (size_t)km * 64 * 2048;
#pragma unroll
            for (int n = 0; n < 4; ++n) dma16<false>(pb + (n & 1) * 128, vt[n >> 1], s + 16384 + (w * 4 + n) * 1024);
        };
        auto issue_gs = [&](int t) {
            const unsigned s = lb + ((t + 2) & 3) * SLOT;
            const int ks = kh * 16 + (t - 28);
            dma64c(a2 + (size_t)ks * 16384, vl, s + w * 4096);
            const char* pb = (const char*)((ks & 1) ? a.cim : a.cre) + (ks >> 1) * 128;
#pragma unroll
            for (int n = 0; n < 4; ++n) dma16<false>(pb + n * 32 * 2048, vr, s + 16384 + (w + 4 * n) * 1024);
        };
        {
_Pragma("unroll 1")
            for (int t = 1; t < 4; ++t) issue_s(t);
            {
                const float lr = bf1(*(const LAS u16*)(smem + LDS_SM + 512 + lane * 2)), li = bf1(*(const LAS u16*)(smem + LDS_SM + 640 + lane * 2));
                const float dt = (float)(__bf16)expf(bf1(*(const LAS u16*)(smem + LDS_SM + 768 + lane * 2)));
                const float zr = dt * lr, zi = dt * li;
                float sn, cs; sincosf(zi, &sn, &cs);
                const float er = expf(zr), Lr = er * cs, Li = er * sn;
                float pr = Lr, pi = Li;
#pragma unroll
                for (int i = 0; i < 4; ++i) cfma(pr, pi, pr, pi, 0.f, 0.f);
                l16 = (f32x2){pr, pi};
                if (w == 0) {
                    const float den = 1.0f / (lr * lr + li * li), nr = Lr - 1.0f;
                    *(LAS f32x4*)(smem + LDS_COEF + lane * 16) = (f32x4){Lr, Li, (nr * lr + Li * li) * den, (Li * lr - nr * li) * den};
                }
                if (w == 2) {
                    float qr = 1.0f, qi = 0.f;
#pragma unroll
                    for (int i = 0; i < 8; ++i) { *(LAS f32x2*)(smem + LDS_PSEG + (i * 64 + lane) * 8) = (f32x2){qr, qi}; cfma(pr, pi, qr, qi, 0.f, 0.f); }
                }
#pragma unroll
                for (int i = 0; i < 3; ++i) cfma(pr, pi, pr, pi, 0.f, 0.f);
                float p2r = pr, p2i = pi; cfma(pr, pi, p2r, p2i, 0.f, 0.f);
                float p4r = p2r, p4i = p2i; cfma(p2r, p2i, p4r, p4i, 0.f, 0.f);
                const int e0 = (rb - 1 - w) & 3, top = (rb - 1 - w) >> 2;
                float qr = (e0 & 1) ? pr : 1.0f, qi = (e0 & 1) ? pi : 0.f;
                if (e0 & 2) cfma(p2r, p2i, qr, qi, 0.f, 0.f);
#pragma unroll
                for (int j = 3; j >= 0; --j) {
                    wt[j] = j <= top ? (f32x2){qr, qi} : (f32x2){0.f, 0.f};
                    if (j <= top) cfma(p4r, p4i, qr, qi, 0.f, 0.f);
                }
                asm volatile("" : "+v"(wt[0]), "+v"(wt[1]), "+v"(wt[2]), "+v"(wt[3]), "+v"(l16));
                __builtin_amdgcn_s_waitcnt(0xC07F);
            }
            WAITV(24); BAR();
            const unsigned fo = WS_FM + (rb * 2 + kh) * 256 + (lane & 15) * 16;
_Pragma("unroll 1")
            for (int t = 0; t < 12; ++t) { WAITV(16); BAR(); issue_s(t + 4); swarm4(sd, pw2 + (3 + (t >> 1)) * W2C + (t & 1) * 256); }
            dma16<true>(ws, fo, lb + LDS_E + w * 1024);
            WAITV(17); BAR();
            WAITV(0); BAR();
            check_flags(parea, ws, fo, tag, lane);
            issue_gm(0);
            BAR();
            issue_gm(1);
            BAR();
            issue_gm(2); issue_gm(3);
        }
        {
            const unsigned fo = (lane & 16) ? WS_XFLAG + (bid ^ 1) * 32 + (lane & 1) * 16 : WS_FS + (rb * 2 + kh) * 256 + (lane & 15) * 16;
            const char* const pcr = (const char*)a.cre + (8 * un + 2 * w) * 2048 + kh * 1024;
            const char* const pci = (const char*)a.cim + (8 * un + 2 * w) * 2048 + kh * 1024;
            const unsigned tag_lo = (unsigned)tag, tag_hi = (unsigned)(tag >> 32);
            WAITV(24); BAR();
            if (w == 1) {
                f32x2 e[8];
#pragma unroll
                for (int i = 0; i < 8; ++i) e[i] = *(const LAS f32x2*)(smem + LDS_E + (i * 64 + lane) * 8);
                LDS_FENCE();
                float er = 0.f, ei = 0.f;
#pragma unroll
                for (int i = 0; i < 8; ++i) {
                    *(LAS f32x2*)(smem + LDS_E + (i * 64 + lane) * 8) = (f32x2){er, ei};
                    cfma(l16.x, l16.y, er, ei, e[i].x, e[i].y);
                }
                st128((u32x4){__float_as_uint(er), tag_lo, __float_as_uint(ei), tag_hi}, rs, vl, WS_GRAN + (unsigned)(cb * 16 + rb) * 1024u, 16);
                __builtin_amdgcn_s_waitcnt(0xC07F);
            }
_Pragma("unroll 1")
            for (int t = 0; t < 9; ++t) { WAITV(16); BAR(); issue_gm(t + 4); swarm4(sd, pw2 + (9 + (t >> 1)) * W2C + (t & 1) * 256); }
#pragma unroll
            for (int j = 0; j < 4; ++j) dma16<true>(ws + WS_GRAN + cb * 16384 + (w + 4 * j) * 1024, vl, lb + LDS_GATH + (w * 4 + j) * 1024);
            WAITV(16); BAR(); issue_gm(13); swarm4(sd, pw2 + 13 * W2C + 256);
            WAITV(16); BAR(); issue_gm(14);
            WAITV(16); BAR();
            {
                u32x4 g[4];
                bool ok = true;
#pragma unroll
                for (int j = 0; j < 4; ++j) {
                    g[j] = *(const LAS u32x4*)(smem + LDS_GATH + (w * 4 + j) * 1024 + lane * 16);
                    ok &= (w + 4 * j >= rb) | ((g[j][1] == tag_lo) & (g[j][3] == tag_hi));
                }
                if (!__all(ok)) {
                    const unsigned long long t0 = __builtin_amdgcn_s_memrealtime();
                    do {
                        __builtin_amdgcn_s_sleep(1);
                        ok = true;
                        asm volatile("" ::: "memory");
#pragma unroll
                        for (int j = 0; j < 4; ++j) {
                            g[j] = __builtin_amdgcn_raw_buffer_load_b128(rs, vl + (w + 4 * j) * 1024, WS_GRAN + (unsigned)cb * 16384u, 16);
                            ok &= (w + 4 * j >= rb) | ((g[j][1] == tag_lo) & (g[j][3] == tag_hi));
                        }
                    } while (!__all(ok) && !deadline(t0));
                }
                float sr = 0.f, si = 0.f;
#pragma unroll
                for (int j = 0; j < 4; ++j) {
                    float vr_ = w + 4 * j < rb ? __uint_as_float(g[j][0]) : 0.f, vi_ = w + 4 * j < rb ? __uint_as_float(g[j][2]) : 0.f;
                    cfma(wt[j].x, wt[j].y, vr_, vi_, sr, si); sr = vr_; si = vi_;
                }
                *(LAS f32x2*)(smem + LDS_BC + lane * 32 + w * 8) = (f32x2){sr, si};
                __builtin_amdgcn_s_waitcnt(0xC07F);
            }
            WAITV(8); BAR(); issue_gm(15); issue_gm(16); swarm2x2(sd, pcr);
_Pragma("unroll 1")
            for (int t = 13; t < 21; ++t) { WAITV(16); BAR(); issue_gm(t + 4); if (t < 20) swarm2x2(sd, ((t & 1) ? pci : pcr) + ((t - 12) >> 1) * 128); }
            dma16<true>(ws, fo, lb + LDS_E + w * 1024);
            WAITV(16); BAR(); issue_gm(25);
            WAITV(16); BAR(); issue_gm(26);
            WAITV(16); BAR(); issue_gm(27);
            check_flags(parea, ws, fo, tag, lane);
_Pragma("unroll 1")
            for (int t = 24; t < 40; ++t) { WAITV(16); BAR(); issue_gs(t + 4); if (t < 28) { swarm2x2(sd, pcr + (t - 20) * 128); swarm2x2(sd, pci + (t - 20) * 128); } }
            WAITV(16); BAR();
            {
                const int rl = 16 * w + (lane >> 4);
#pragma unroll
                for (int n = 0; n < 4; ++n) {
                    const int rr = rl + 4 * n;
                    dma16<false>((const char*)a.x + (size_t)(rb * 128 + kh * 32) * 2048 + c8 * 256, (unsigned)((64 * (rr >> 5) + (rr & 31)) * 2048 + (((lane & 15) ^ (rr & 15)) * 16)), lb + 2 * SLOT + (w * 4 + n) * 1024);
                }
                dma4<false>((const char*)((w & 1) ? a.b2 : a.dsk) + c8 * 256, (unsigned)lane * 4u, lb + 2 * SLOT + 16384 + w * 256);
            }
            WAITV(13); BAR();
            WAITV(5); BAR();
            WAITV(0); BAR();
            asm volatile("s_waitcnt lgkmcnt(0)" ::"s"(sd));
        }
        if (bid == 0 && w == 0) {
            const unsigned long long t0 = __builtin_amdgcn_s_memrealtime();
            for (;;) {
                bool ok = true;
#pragma unroll
                for (int i = 0; i < 4; ++i) ok &= __hip_atomic_load((gu64*)(ws + WS_FY) + i * 64 + lane, RLX_AGENT) == tag;
                if (__all(ok) || deadline(t0)) break;
                __builtin_amdgcn_s_sleep(2);
            }
            if (lane == 0) __hip_atomic_store((gu64*)(ws + WS_EPOCH), tag, RLX_AGENT);
        }
        return;
    }

    const int wm = wid >> 1, wn = wid & 1;
    const unsigned lb = (unsigned)(size_t)smem;
    const __amdgpu_buffer_rsrc_t rs = __builtin_amdgcn_make_buffer_rsrc(ws, 0, WS_END, 0x00020000);
    STAMP(0);
    {
        if (wid == 0) {
            const u64 t = next_tag(ep);
            if (lane == 0) *(LAS u32x2*)(smem + LDS_TAG) = (u32x2){(unsigned)t, (unsigned)(t >> 32)};
        }
        f32x2 xf[2][8];
        float s[2], gd[2], vs[2];
#pragma unroll
        for (int i = 0; i < 2; ++i) {
            f32x2 s2 = {0.f, 0.f}, g2 = {0.f, 0.f};
#pragma unroll
            for (int h = 0; h < 2; ++h)
#pragma unroll
                for (int j = 0; j < 4; ++j) {
                    const f32x2 v = {bflo(xv[i][h][j]), bfhi(xv[i][h][j])}, gw = {bflo(gv[h][j]), bfhi(gv[h][j])};
                    xf[i][h * 4 + j] = v; s2 += v; g2 += v * gw;
                }
            s[i] = wave_sum(s2.x + s2.y) * (1.0f / 1024.0f); gd[i] = wave_sum(g2.x + g2.y);
        }
#pragma unroll
        for (int i = 0; i < 2; ++i) {
            f32x2 v2 = {0.f, 0.f};
#pragma unroll
            for (int j = 0; j < 8; ++j) { const f32x2 d = xf[i][j] - s[i]; v2 += d * d; }
            vs[i] = wave_sum(v2.x + v2.y);
        }
        const float gl = lane ? gd[1] : gd[0], ml = lane ? s[1] : s[0], vl = lane ? vs[1] : vs[0];
        const float rstd = 1.0f / sqrtf(vl * (1.0f / 1024.0f) + 1e-5f), g = 1.0f / (1.0f + expf(-gl));
        if (lane < 2) *(LAS f32x4*)(smem + LDS_BC + (wid * 2 + lane) * 16) = (f32x4){rstd, -rstd * ml, g, 0.f};
    }
    bf16x8 A0[4], A1[4];
    {
        f32x4 acc[7][4];
        bf16x8 B0[7], B1[7];
        LaneC c = lane_consts<PH_M1>(lb, wm, wn, lane);
        __builtin_amdgcn_s_waitcnt(0xC07F);
        STAMP(1);
        BAR();
        STAMP(2);
        load_frags<PH_M1, 0, 7>(c, A0, B0);
#pragma unroll
        for (int n = 0; n < 7; ++n)
#pragma unroll
            for (int m = 0; m < 4; ++m) acc[n][m] = (f32x4){0.f, 0.f, 0.f, 0.f};
        SCHED0();
        {
            load_frags<PH_M1, 1, 7>(c, A1, B1);
            mma<PH_M1, 7>(acc, A0, B0);
            advance<PH_M1>(c, SLOTM);
            sched_half<18, 28, 5>();
            SCHED0();
            colsum_half(B0, smem + LDS_BC + 128, wm, wn, lane);
            __builtin_amdgcn_s_waitcnt(0xC07F);
            BAR();
            SCHED0();
            load_frags<PH_M1, 0, 7>(c, A0, B0);
            mma<PH_M1, 7>(acc, A1, B1);
            sched_half<18, 28>();
            SCHED0();
            colsum_half(B1, smem + LDS_BC + 1024 + 128, wm, wn, lane);
        }
        int sc = 1;
_Pragma("unroll 1")
        for (int t = 1; t < 15; ++t) { cstep<PH_M1, 7>(c, sc == 2 ? (unsigned)(-2 * SLOTM) : (unsigned)SLOTM, acc, A0, B0, A1, B1); sc = sc == 2 ? 0 : sc + 1; }
        cstep2<PH_M1, PH_M1, true, 7>(c, c, acc, A0, B0, A1, B1);
        STAMP(3);
        m1_epilogue(acc, smem, rs, rb, cb, wm, wn, lane);
    }
    u64 tag;
    { const u32x2 tv = *(const LAS u32x2*)(smem + LDS_TAG);
      tag = ((u64)(unsigned)__builtin_amdgcn_readfirstlane(tv.y) << 32) | (unsigned)__builtin_amdgcn_readfirstlane(tv.x); }
    f32x4 sv[4][4];
    bf16x8 B0[4], B1[4];
    const int ch = lane & 15, q = lane >> 4, seg = wm * 4 + q;
    {
#pragma unroll
        for (int n = 0; n < 4; ++n)
#pragma unroll
            for (int m = 0; m < 4; ++m) sv[n][m] = (f32x4){0.f, 0.f, 0.f, 0.f};
        LaneC c = lane_consts<PH_S1>(lb + 2 * SLOT, wm, wn, lane);
        STAMP(4);
        BAR();
        STAMP(5);
        load_frags<PH_S1, 0, 4>(c, A0, B0);
        SCHED0();
#define S1STEP(t) cstep<PH_S1, 4>(c, ringd(t), sv, A0, B0, A1, B1)
_Pragma("unroll 1")
        for (int t = 0; t < 4; ++t) S1STEP(t);
        signal_flag((gu64*)(ws + WS_FM) + (rb * 2 + kh) * 32 + c8 * 4 + wid, tag, lane);
_Pragma("unroll 1")
        for (int t = 4; t < 15; ++t) S1STEP(t);
        f32x2 lam[2];
#pragma unroll
        for (int cbk = 0; cbk < 2; ++cbk) lam[cbk] = *(const LAS f32x2*)(smem + LDS_COEF + (wn * 32 + cbk * 16 + ch) * 16);
        SCHED0();
        cstep2<PH_S1, PH_S1, true, 4>(c, c, sv, A0, B0, A1, B1);
        STAMP(6);
        asm volatile("s_nop 15");
        SCHED0();
        float er[2], ei[2];
#pragma unroll
        for (int cbk = 0; cbk < 2; ++cbk) { er[cbk] = sv[cbk][0][0]; ei[cbk] = sv[2 + cbk][0][0]; }
#pragma unroll
        for (int i = 1; i < 16; ++i)
#pragma unroll
            for (int cbk = 0; cbk < 2; ++cbk) cfma(lam[cbk].x, lam[cbk].y, er[cbk], ei[cbk], sv[cbk][i >> 2][i & 3], sv[2 + cbk][i >> 2][i & 3]);
#pragma unroll
        for (int cbk = 0; cbk < 2; ++cbk) *(LAS f32x2*)(smem + LDS_E + (seg * 64 + wn * 32 + cbk * 16 + ch) * 8) = (f32x2){er[cbk], ei[cbk]};
        STAMP(7);
    }
    {
        f32x4 acc[4][4];
#pragma unroll
        for (int n = 0; n < 4; ++n)
#pragma unroll
            for (int m = 0; m < 4; ++m) acc[n][m] = (f32x4){0.f, 0.f, 0.f, 0.f};
        LaneC cm = lane_consts<PH_GM>(lb + 2 * SLOT, wm, wn, lane);
        __builtin_amdgcn_s_waitcnt(0xC07F);
        STAMP(8);
        BAR();
        STAMP(9);
        load_frags<PH_GM, 0, 4>(cm, A0, B0);
        SCHED0();
#define GMSTEP(t) cstep<PH_GM, 4>(cm, ringd(t), acc, A0, B0, A1, B1)
_Pragma("unroll 1")
        for (int t = 0; t < 13; ++t) GMSTEP(t);
        STAMP(10);
        {
            f32x4 g2[4], k0[2]; float zr[2], zi[2];
#pragma unroll
            for (int cbk = 0; cbk < 2; ++cbk) {
                const int cl = wn * 32 + cbk * 16 + ch;
                const f32x2 c0 = *(const LAS f32x2*)(smem + LDS_E + (seg * 64 + cl) * 8), ps = *(const LAS f32x2*)(smem + LDS_PSEG + (seg * 64 + cl) * 8);
                const f32x4 sp = *(const LAS f32x4*)(smem + LDS_BC + cl * 32) + *(const LAS f32x4*)(smem + LDS_BC + cl * 32 + 16);
                zr[cbk] = sp[0] + sp[2]; zi[cbk] = sp[1] + sp[3];
                cfma(ps.x, ps.y, zr[cbk], zi[cbk], c0.x, c0.y);
                k0[cbk] = *(const LAS f32x4*)(smem + LDS_COEF + (wn * 32 + cbk * 16 + ch) * 16);
            }
#pragma unroll
            for (int m = 0; m < 4; ++m) g2[m] = *(const LAS f32x4*)(smem + LDS_G2 + (seg * 16 + m * 4) * 4);
            const unsigned so = WS_A2 + (unsigned)(rb * 88 + 2 * cb) * 16384u;
#pragma unroll
            for (int th = 0; th < 2; ++th) {
#pragma unroll
                for (int i = th * 8; i < th * 8 + 8; ++i)
#pragma unroll
                    for (int cbk = 0; cbk < 2; ++cbk) {
                        cfma(k0[cbk][0], k0[cbk][1], zr[cbk], zi[cbk], sv[cbk][i >> 2][i & 3], sv[2 + cbk][i >> 2][i & 3]);
                        sv[cbk][i >> 2][i & 3] = zr[cbk]; sv[2 + cbk][i >> 2][i & 3] = zi[cbk];
                    }
#pragma unroll
                for (int cbk = 0; cbk < 2; ++cbk) {
                    const int kb = 4 * wn + 2 * cbk + (ch >> 3);
                    const unsigned vo = (unsigned)((kb * 8 + seg) * 256 + (((ch & 7) ^ (4 * (kb & 1))) * 16));
                    u32x4 vr, vi;
#pragma unroll
                    for (int mh = 0; mh < 2; ++mh) {
                        const f32x4 re = sv[cbk][th * 2 + mh], im = sv[2 + cbk][th * 2 + mh];
                        const f32x4 hr4 = (re * k0[cbk][2] - im * k0[cbk][3]) * g2[th * 2 + mh], hi4 = (im * -k0[cbk][2] - re * k0[cbk][3]) * g2[th * 2 + mh];
                        vr[mh * 2] = pkbf(hr4[0], hr4[1]); vr[mh * 2 + 1] = pkbf(hr4[2], hr4[3]);
                        vi[mh * 2] = pkbf(hi4[0], hi4[1]); vi[mh * 2 + 1] = pkbf(hi4[2], hi4[3]);
                    }
                    st128(vr, rs, vo + th * 128, so, 16);
                    st128(vi, rs, vo + th * 128, so + 16384u, 16);
                }
            }
            SCHED0();
        }
        STAMP(11);
_Pragma("unroll 1")
        for (int t = 13; t < 16; ++t) GMSTEP(t);
        signal_flag((gu64*)(ws + WS_FS) + (rb * 2 + kh) * 32 + c8 * 4 + wid, tag, lane);
_Pragma("unroll 1")
        for (int t = 16; t < 27; ++t) GMSTEP(t);
        LaneC cs = lane_consts<PH_GS>(lb + 2 * SLOT, wm, wn, lane);
        float og[4];
#pragma unroll
        for (int m = 0; m < 4; ++m) og[m] = 1.0f - *(const LAS float*)(smem + LDS_STAT + (wm * 64 + m * 16 + ch) * 16 + 8);
        SCHED0();
        cstep2<PH_GM, PH_GS, false, 4>(cm, cs, acc, A0, B0, A1, B1);
#pragma unroll
        for (int n = 0; n < 4; ++n)
#pragma unroll
            for (int m = 0; m < 4; ++m) acc[n][m] *= og[m];
        SCHED0();
        STAMP(12);
_Pragma("unroll 1")
        for (int t = 28; t < 43; ++t) cstep<PH_GS, 4>(cs, ringd(t), acc, A0, B0, A1, B1);
        cstep2<PH_GS, PH_GS, true, 4>(cs, cs, acc, A0, B0, A1, B1);
        const int r = ch;
        const int t0r = rb * 128 + wm * 64 + kh * 32 + r, d0 = c8 * 128 + wn * 64 + q * 4;

        STAMP(13);
        if (kh) {
#pragma unroll
            for (int n = 0; n < 4; ++n)
#pragma unroll
                for (int mm = 0; mm < 2; ++mm) { const f32x4 t = acc[n][mm]; acc[n][mm] = acc[n][2 + mm]; acc[n][2 + mm] = t; }
        }
        const unsigned xo = WS_XB + (unsigned)(bid * 4 + wid) * 8192u, xi = WS_XB + (unsigned)((bid ^ 1) * 4 + wid) * 8192u;
#pragma unroll
        for (int n = 0; n < 4; ++n)
#pragma unroll
            for (int mm = 0; mm < 2; ++mm) st128(__builtin_bit_cast(u32x4, acc[n][2 + mm]), rs, lane * 16 + (n * 2 + mm) * 1024, xo, 16);
        const __amdgpu_buffer_rsrc_t ro = __builtin_amdgcn_make_buffer_rsrc(a.out, 0, 8u << 20, 0x00020000);
        f32x4 o[4][2];
#pragma unroll
        for (int mm = 0; mm < 2; ++mm) {
            const float g = *(const LAS float*)(smem + LDS_STAT + (wm * 64 + kh * 32 + mm * 16 + r) * 16 + 8);
#pragma unroll
            for (int n = 0; n < 4; ++n) {
                const u32x2 xw = *(const LAS u32x2*)(smem + 2 * SLOT + (wm * 32 + mm * 16 + r) * 256 + (((wn * 8 + n * 2 + (q >> 1)) ^ r) * 16) + (q & 1) * 8);
                const u32x2 dw = *(const LAS u32x2*)(smem + 2 * SLOT + 16384 + (wn * 64 + n * 16 + q * 4) * 2), bw = *(const LAS u32x2*)(smem + 2 * SLOT + 16384 + 256 + (wn * 64 + n * 16 + q * 4) * 2);
                const f32x4 own = acc[n][mm];
                o[n][mm][0] = own[0] + g * (bflo(xw.x) * bflo(dw.x)) + (1.0f - g) * bflo(bw.x);
                o[n][mm][1] = own[1] + g * (bfhi(xw.x) * bfhi(dw.x)) + (1.0f - g) * bfhi(bw.x);
                o[n][mm][2] = own[2] + g * (bflo(xw.y) * bflo(dw.y)) + (1.0f - g) * bflo(bw.y);
                o[n][mm][3] = own[3] + g * (bfhi(xw.y) * bfhi(dw.y)) + (1.0f - g) * bfhi(bw.y);
                asm volatile("" : "+v"(o[n][mm]));
            }
        }
        STAMP(14);
        {
            u32x4 pv[8];
            const unsigned long long t0 = __builtin_amdgcn_s_memrealtime();
            for (;;) {
                asm volatile("" ::: "memory");
                unsigned mx = 0;
#pragma unroll
                for (int i = 0; i < 8; ++i) pv[i] = __builtin_amdgcn_raw_buffer_load_b128(rs, lane * 16 + i * 1024, xi, 16);
#pragma unroll
                for (int i = 0; i < 8; ++i) mx = max(max(mx, max(pv[i][0], pv[i][1])), max(pv[i][2], pv[i][3]));
                if (__all(mx != XSENT) || deadline(t0)) break;
                unsigned last;
                do { __builtin_amdgcn_s_sleep(2); asm volatile("" ::: "memory");
                     last = __builtin_amdgcn_raw_buffer_load_b32(rs, 8188, xi, 16);
                } while (__builtin_amdgcn_readfirstlane(last) == XSENT && !deadline(t0));
            }
            STAMP(15);
#pragma unroll
            for (int n = 0; n < 4; ++n)
#pragma unroll
                for (int mm = 0; mm < 2; ++mm) o[n][mm] += __builtin_bit_cast(f32x4, pv[n * 2 + mm]);
        }
#pragma unroll
        for (int mm = 0; mm < 2; ++mm)
#pragma unroll
            for (int n = 0; n < 4; ++n) __builtin_amdgcn_raw_buffer_store_b128(__builtin_bit_cast(u32x4, o[n][mm]), ro, ((t0r + mm * 16) * 1024 + d0 + n * 16) * 4, 0, 16);
    }
}
}
extern "C" __global__ void gm_code_end() {}

extern "C" void kernel_launch(void* const* d_in, const int* in_sizes, int n_in,
                              void* d_out, int out_size, void* d_ws, size_t ws_size,
                              hipStream_t stream) {
    using namespace gm;
    Args a;
    a.x = (const u16*)d_in[0]; a.lre = (const u16*)d_in[1]; a.lim = (const u16*)d_in[2]; a.bre = (const u16*)d_in[3]; a.bim = (const u16*)d_in[4];
    a.cre = (const u16*)d_in[5]; a.cim = (const u16*)d_in[6]; a.ldt = (const u16*)d_in[7]; a.dsk = (const u16*)d_in[8];
    a.w1 = (const u16*)d_in[10]; a.b1 = (const u16*)d_in[11]; a.w2 = (const u16*)d_in[12]; a.b2 = (const u16*)d_in[13]; a.gw = (const u16*)d_in[14];
    a.out = (float*)d_out; a.ws = (char*)d_ws;
    (void)hipFuncSetAttribute((const void*)fused_kernel, hipFuncAttributeMaxDynamicSharedMemorySize, LDS_BYTES);
    fused_kernel<<<256, 512, LDS_BYTES, stream>>>(a);
}
```

```cpp
#include <hip/hip_runtime.h>
#include <hip/hip_bf16.h>

namespace gm {
#define LAS __attribute__((address_space(3)))
#define GAS __attribute__((address_space(1)))
typedef unsigned short u16;
typedef short bf16x8 __attribute__((ext_vector_type(8)));
typedef short s16x4 __attribute__((ext_vector_type(4)));
typedef float f32x4 __attribute__((ext_vector_type(4)));
typedef float f32x2 __attribute__((ext_vector_type(2)));
typedef unsigned u32x4 __attribute__((ext_vector_type(4)));
typedef unsigned u32x2 __attribute__((ext_vector_type(2)));
typedef LAS char* ldsp;
typedef GAS unsigned gu32;
typedef unsigned long long u64;
typedef GAS u64 gu64;

#define BAR() __builtin_amdgcn_s_barrier()
#define WAITV(n) asm volatile("s_waitcnt vmcnt(%0)" ::"i"(n) : "memory")
#define SCHED0() __builtin_amdgcn_sched_barrier(0)
#define LDS_FENCE() do { SCHED0(); __builtin_amdgcn_s_waitcnt(0xC07F); asm volatile("" ::: "memory"); SCHED0(); } while (0)
#define ONCE() int once_ = 1; asm volatile("" : "+s"(once_)); _Pragma("unroll 1") for (int i_ = 0; i_ < once_; ++i_)
#define RLX_AGENT __ATOMIC_RELAXED, __HIP_MEMORY_SCOPE_AGENT

constexpr unsigned WS_EPOCH = 0;
constexpr unsigned WS_FY = 4096;
constexpr unsigned WS_FC = 12288;
constexpr unsigned WS_FS = 16384;
constexpr unsigned WS_FM = 24576;
constexpr unsigned WS_XFLAG = 32768;
constexpr unsigned WS_GRAN = 131072;
constexpr unsigned WS_STAT = 1u << 20;
constexpr unsigned WS_CSP = 2u << 20;
constexpr unsigned WS_A2 = 8u << 20;
constexpr unsigned WS_XB = 32u << 20;
constexpr unsigned WS_END = 40u << 20;

constexpr int SLOT = 32768, SLOTM = 45056;
constexpr int LDS_PSEG = 131072;
constexpr int LDS_E = 135168;
constexpr int LDS_COEF = LDS_E + 4096;
constexpr int LDS_BC = LDS_COEF + 1024;
constexpr int LDS_STAT = LDS_BC + 2048;
constexpr int LDS_G2 = LDS_STAT + 2048;
constexpr int LDS_DUMMY = LDS_G2;
constexpr int LDS_TAG = LDS_G2 + 512;
constexpr int LDS_GATH = LDS_TAG + 64;
constexpr int LDS_SM = LDS_GATH + 16384;
constexpr int LDS_BYTES = LDS_SM + 1024;
static_assert(LDS_BYTES <= 160 * 1024, "LDS");

constexpr int TSP = 2;
enum { PH_S1 = 0, PH_M1 = 1, PH_GS = 2, PH_GM = 3 };

struct Args {
    const u16 *x, *lre, *lim, *bre, *bim, *cre, *cim, *ldt, *dsk, *w1, *b1, *w2, *b2, *gw;
    float* out;
    char* ws;
};

__device__ __forceinline__ bf16x8 lds128(ldsp p) { return *(const LAS bf16x8*)p; }
__device__ __forceinline__ s16x4 ldstr(ldsp p) { return __builtin_amdgcn_ds_read_tr16_b64_v4i16((LAS s16x4*)p); }
__device__ __forceinline__ bf16x8 cat(s16x4 a, s16x4 b) { return __builtin_shufflevector(a, b, 0, 1, 2, 3, 4, 5, 6, 7); }
template <bool C> __device__ __forceinline__ void dma16(const void* sb, unsigned vo, unsigned la) {
    if constexpr (C) asm volatile("s_mov_b32 m0, %0\n\ts_nop 0\n\tglobal_load_lds_dwordx4 %1, %2 sc1" ::"s"(la), "v"(vo), "s"(sb) : "memory");
    else asm volatile("s_mov_b32 m0, %0\n\ts_nop 0\n\tglobal_load_lds_dwordx4 %1, %2" ::"s"(la), "v"(vo), "s"(sb) : "memory");
}
__device__ __forceinline__ void dma64c(const void* sb, unsigned vo, unsigned la) {
    asm volatile("s_mov_b32 m0, %0\n\ts_nop 0\n\tglobal_load_lds_dwordx4 %1, %2 sc1\n\tglobal_load_lds_dwordx4 %1, %2 offset:1024 sc1\n\t"
                 "global_load_lds_dwordx4 %1, %2 offset:2048 sc1\n\tglobal_load_lds_dwordx4 %1, %2 offset:3072 sc1" ::"s"(la), "v"(vo), "s"(sb) : "memory");
}
template <bool C> __device__ __forceinline__ void dma4(const void* sb, unsigned vo, unsigned la) {
    if constexpr (C) asm volatile("s_mov_b32 m0, %0\n\ts_nop 0\n\tglobal_load_lds_dword %1, %2 sc1" ::"s"(la), "v"(vo), "s"(sb) : "memory");
    else asm volatile("s_mov_b32 m0, %0\n\ts_nop 0\n\tglobal_load_lds_dword %1, %2" ::"s"(la), "v"(vo), "s"(sb) : "memory");
}
__device__ __forceinline__ void swarm8(unsigned sd, const void* p) {
    asm volatile("s_load_dword %0, %1, 0x0\n\ts_load_dword %0, %1, 0x40\n\ts_load_dword %0, %1, 0x80\n\ts_load_dword %0, %1, 0xc0\n\t"
                 "s_load_dword %0, %1, 0x100\n\ts_load_dword %0, %1, 0x140\n\ts_load_dword %0, %1, 0x180\n\ts_load_dword %0, %1, 0x1c0" ::"s"(sd), "s"(p));
}
__device__ __forceinline__ void swarm4(unsigned sd, const void* p) {
    asm volatile("s_load_dword %0, %1, 0x0\n\ts_load_dword %0, %1, 0x40\n\ts_load_dword %0, %1, 0x80\n\ts_load_dword %0, %1, 0xc0" ::"s"(sd), "s"(p));
}
__device__ __forceinline__ void swarm2x2(unsigned sd, const void* p) {
    asm volatile("s_load_dword %0, %1, 0x0\n\ts_load_dword %0, %1, 0x40\n\ts_load_dword %0, %1, 0x800\n\ts_load_dword %0, %1, 0x840" ::"s"(sd), "s"(p));
}
__device__ __forceinline__ void st128(u32x4 v, __amdgpu_buffer_rsrc_t rs, unsigned voff, unsigned base, int) { __builtin_amdgcn_raw_buffer_store_b128(v, rs, voff + base, 0, 16); }
__device__ __forceinline__ void st64(u32x2 v, __amdgpu_buffer_rsrc_t rs, unsigned voff, unsigned base, int) { __builtin_amdgcn_raw_buffer_store_b64(v, rs, voff + base, 0, 16); }
__device__ __forceinline__ float bflo(unsigned w) { return __uint_as_float(w << 16); }
__device__ __forceinline__ float bfhi(unsigned w) { return __uint_as_float(w & 0xffff0000u); }
__device__ __forceinline__ float bf1(u16 b) { return __uint_as_float((unsigned)b << 16); }
__device__ __forceinline__ unsigned pkbf(float lo, float hi) {
    typedef __bf16 bf2_t __attribute__((ext_vector_type(2)));
    bf2_t v = {(__bf16)lo, (__bf16)hi};
    return __builtin_bit_cast(unsigned, v);
}
__device__ __forceinline__ void cfma(float lr, float li, float& zr, float& zi, float ur, float ui) {
    float tr, ti;
    asm("v_fma_f32 %0, -%3, %5, %6\n\tv_fma_f32 %1, %3, %4, %7\n\tv_fma_f32 %0, %2, %4, %0\n\tv_fma_f32 %1, %2, %5, %1"
        : "=&v"(tr), "=&v"(ti) : "v"(lr), "v"(li), "v"(zr), "v"(zi), "v"(ur), "v"(ui));
    zr = tr; zi = ti;
}
__device__ __forceinline__ bool deadline(unsigned long long t0) { return __builtin_amdgcn_s_memrealtime() - t0 > 400000ull; }

struct LaneC { unsigned a0, a1, b0, b1, b2; };
template <int PH>
__device__ __forceinline__ LaneC lane_consts(unsigned base, int wm, int wn, int lane) {
    const int r = lane & 15, q = lane >> 4, rr = r >> 2, p = lane & 3;
    const unsigned rl = r * 128 + ((q ^ (r >> 1)) * 16);
    LaneC c; c.b2 = 0;
    if constexpr (PH == PH_S1) { c.a0 = wm * 8192 + rl; c.a1 = c.a0 ^ 64; c.b0 = 16384 + wn * 8192 + rl; c.b1 = c.b0 ^ 64; }
    if constexpr (PH == PH_M1) {
        c.a0 = wm * 8192 + rl; c.a1 = c.a0 ^ 64;
        const unsigned t0 = 16384 + q * 7 * 512 + rr * 64 + ((q & 1) * 32) + p * 8;
        c.b0 = t0 + wn * 2048;
        c.b1 = (t0 ^ 32) + wn * 2048;
        c.b2 = t0 ^ (wn * 32);
    }
    if constexpr (PH == PH_GS) {
        c.a0 = (q * 8 + wm * 4) * 256 + (p >> 1) * 128 + (4 * (q & 1) + rr) * 16 + (p & 1) * 8; c.a1 = c.a0 ^ 64;
        c.b0 = 16384 + wn * 8192 + rl; c.b1 = c.b0 ^ 64;
    }
    if constexpr (PH == PH_GM) {
        c.a0 = wm * 8192 + lane * 16; c.a1 = c.a0;
        c.b0 = 16384 + q * 4 * 512 + wn * 1024 + rr * 64 + ((q & 1) * 32) + p * 8; c.b1 = c.b0 ^ 32;
    }
    c.a0 += base; c.a1 += base; c.b0 += base; c.b1 += base; c.b2 += base;
    return c;
}
template <int PH> __device__ __forceinline__ void advance(LaneC& c, unsigned d) {
    c.a0 += d; c.b0 += d; c.b1 += d;
    if constexpr (PH != PH_GM) c.a1 += d;
    if constexpr (PH == PH_M1) c.b2 += d;
    if constexpr (PH == PH_M1) asm volatile("" : "+v"(c.a0), "+v"(c.a1), "+v"(c.b0), "+v"(c.b1), "+v"(c.b2));
    else if constexpr (PH == PH_GM) asm volatile("" : "+v"(c.a0), "+v"(c.b0), "+v"(c.b1));
    else asm volatile("" : "+v"(c.a0), "+v"(c.a1), "+v"(c.b0), "+v"(c.b1));
}
template <int PH, int KK, int NB>
__device__ __forceinline__ void load_frags(const LaneC& c, bf16x8 (&A)[4], bf16x8 (&B)[NB]) {
    if constexpr (PH == PH_S1 || PH == PH_M1) {
        ldsp pa = (ldsp)(size_t)(KK ? c.a1 : c.a0);
#pragma unroll
        for (int m = 0; m < 4; ++m) A[m] = lds128(pa + m * 2048);
    } else if constexpr (PH == PH_GS) {
        ldsp p0 = (ldsp)(size_t)c.a0, p1 = (ldsp)(size_t)c.a1;
#pragma unroll
        for (int m = 0; m < 4; ++m) A[m] = cat(ldstr(p0 + (KK * 32 + m) * 256), ldstr(p1 + (KK * 32 + m) * 256));
    } else {
        ldsp pa = (ldsp)(size_t)c.a0;
#pragma unroll
        for (int m = 0; m < 4; ++m) A[m] = lds128(pa + (m * 2 + KK) * 1024);
    }
    if constexpr (PH == PH_S1 || PH == PH_GS) {
        ldsp pb = (ldsp)(size_t)(KK ? c.b1 : c.b0);
#pragma unroll
        for (int n = 0; n < NB; ++n) B[n] = lds128(pb + n * 2048);
    } else {
        constexpr int NS2 = PH == PH_M1 ? 7 : 4;
#pragma unroll
        for (int n = 0; n < NB; ++n) { ldsp p = (ldsp)(size_t)(n == 6 ? c.b2 : (n & 1) ? c.b1 : c.b0) + KK * 4 * NS2 * 512 + (n >> 1) * 512; B[n] = cat(ldstr(p), ldstr(p + 256)); }
    }
}
template <int PH, int NB>
__device__ __forceinline__ void mma(f32x4 (&acc)[NB][4], const bf16x8 (&A)[4], const bf16x8 (&B)[NB]) {
#pragma unroll
    for (int n = 0; n < NB; ++n)
#pragma unroll
        for (int mi = 0; mi < 4; ++mi) {
            const int m = (n & 1) ? 3 - mi : mi;
            if constexpr (PH == PH_S1) acc[n][m] = __builtin_amdgcn_mfma_f32_16x16x32_bf16(A[m], B[n], acc[n][m], 0, 0, 0);
            else acc[n][m] = __builtin_amdgcn_mfma_f32_16x16x32_bf16(B[n], A[m], acc[n][m], 0, 0, 0);
        }
}
template <int PH> constexpr int nreads() { return PH == PH_S1 ? 8 : PH == PH_M1 ? 18 : 12; }
template <int NR, int NM, int NV = 0>
__device__ __forceinline__ void sched_half() {
#pragma unroll
    for (int i = 0; i < NR; ++i) { __builtin_amdgcn_sched_group_barrier(0x8, 1, 0); __builtin_amdgcn_sched_group_barrier(0x100, 1, 0); }
    if constexpr (NV > 0) {
#pragma unroll
        for (int i = 0; i < (NV + 1) / 2; ++i) { __builtin_amdgcn_sched_group_barrier(0x8, 1, 0); __builtin_amdgcn_sched_group_barrier(0x2, 2, 0); }
    }
    __builtin_amdgcn_sched_group_barrier(0x8, NM - NR - (NV + 1) / 2, 0);
}
template <int PH, int NB>
__device__ __forceinline__ void cstep(LaneC& c, unsigned d, f32x4 (&acc)[NB][4], bf16x8 (&A0)[4], bf16x8 (&B0)[NB], bf16x8 (&A1)[4], bf16x8 (&B1)[NB]) {
    load_frags<PH, 1, NB>(c, A1, B1);
    mma<PH, NB>(acc, A0, B0);
    advance<PH>(c, d);
    sched_half<nreads<PH>(), 4 * NB, PH == PH_M1 ? 5 : 4>();
    SCHED0();
    __builtin_amdgcn_s_waitcnt(0xC07F);
    BAR();
    SCHED0();
    load_frags<PH, 0, NB>(c, A0, B0);
    mma<PH, NB>(acc, A1, B1);
    sched_half<nreads<PH>(), 4 * NB>();
}
template <int PC, int PN, bool LAST, int NB>
__device__ __forceinline__ void cstep2(const LaneC& cc, const LaneC& cn, f32x4 (&acc)[NB][4], bf16x8 (&A0)[4], bf16x8 (&B0)[NB], bf16x8 (&A1)[4], bf16x8 (&B1)[NB]) {
    SCHED0();
    load_frags<PC, 1, NB>(cc, A1, B1);
    mma<PC, NB>(acc, A0, B0);
    sched_half<nreads<PC>(), 4 * NB>();
    SCHED0();
    __builtin_amdgcn_s_waitcnt(0xC07F);
    BAR();
    SCHED0();
    if constexpr (!LAST) load_frags<PN, 0, NB>(cn, A0, B0);
    mma<PC, NB>(acc, A1, B1);
    if constexpr (!LAST) sched_half<nreads<PN>(), 4 * NB>();
    SCHED0();
}
__device__ __forceinline__ unsigned ringd(int t) { return ((t + 3) & 3) ? (unsigned)SLOT : (unsigned)(-3 * SLOT); }

__device__ __forceinline__ u64 next_tag(u64 e) { return e * 0x9E3779B97F4A7C15ull + 0xD1B54A32D192ED03ull; }
__device__ __forceinline__ void wait_flags(gu64* fl, u64 v, u64 tag) {
    if (!__all(v == tag)) {
        const unsigned long long t0 = __builtin_amdgcn_s_memrealtime();
        do { __builtin_amdgcn_s_sleep(2); v = __hip_atomic_load(fl, RLX_AGENT); } while (!__all(v == tag) && !deadline(t0));
    }
    asm volatile("" ::: "memory");
}
__device__ __forceinline__ void signal_flag(gu64* fl, u64 tag, int lane) {
    WAITV(0);
    if (lane == 0) __hip_atomic_store(fl, tag, RLX_AGENT);
}
template <int CTRL, int RMASK> __device__ __forceinline__ float dpp_add(float v) {
    return v + __builtin_bit_cast(float, __builtin_amdgcn_update_dpp(0, __builtin_bit_cast(int, v), CTRL, RMASK, 0xf, true));
}
__device__ __forceinline__ float wave_sum(float v) {
    v = dpp_add<0x111, 0xf>(v); v = dpp_add<0x112, 0xf>(v); v = dpp_add<0x114, 0xf>(v); v = dpp_add<0x118, 0xf>(v);
    v = dpp_add<0x142, 0xa>(v); v = dpp_add<0x143, 0xc>(v);
    return __builtin_bit_cast(float, __builtin_amdgcn_readlane(__builtin_bit_cast(int, v), 63));
}
#define STAMP(i) do { } while (0)

__device__ __forceinline__ unsigned gelu2(f32x2 v) {
    const f32x2 t = (v * v) * -0.1029432397f + -2.3022081986f;
    const f32x2 u = v * t;
    f32x2 d; d.x = __builtin_amdgcn_exp2f(u.x); d.y = __builtin_amdgcn_exp2f(u.y);
    d = d + 1.0f;
    f32x2 r; r.x = __builtin_amdgcn_rcpf(d.x); r.y = __builtin_amdgcn_rcpf(d.y);
    const f32x2 o = v * r;
    return pkbf(o.x, o.y);
}

__device__ __forceinline__ void m1_epilogue(f32x4 (&acc)[7][4], ldsp smem, __amdgpu_buffer_rsrc_t rs, int rb, int cb, int wm, int wn, int lane) {
    f32x2 st[4];
#pragma unroll
    for (int m = 0; m < 4; ++m) st[m] = *(const LAS f32x2*)(smem + LDS_STAT + (wm * 64 + m * 16 + (lane & 15)) * 16);
    u32x2 pk[7][4];
#pragma unroll
    for (int n = 0; n < 7; ++n) {
        const int nbt = n == 6 ? 6 + wn : n + 8 * wn;
        const f32x4 b = *(const LAS f32x4*)(smem + LDS_BC + (nbt * 16 + (lane >> 4) * 4) * 4), cs = *(const LAS f32x4*)(smem + LDS_BC + 1024 + (nbt * 16 + (lane >> 4) * 4) * 4);
#pragma unroll
        for (int m = 0; m < 4; ++m) {
            const f32x4 v = acc[n][m] * st[m].x + (cs * st[m].y + b);
            pk[n][m].x = gelu2((f32x2){v[0], v[1]});
            pk[n][m].y = gelu2((f32x2){v[2], v[3]});
        }
        if ((n & 1) || n == 6) {
            const int pair = 7 * cb + (nbt >> 1);
            const unsigned base = WS_A2 + (unsigned)(rb * 88 + 32 + (pair >> 1)) * 16384u + (unsigned)(pair & 1) * 1024u + (unsigned)(wm * 4) * 2048u;
#pragma unroll
            for (int m = 0; m < 4; ++m) {
                if (n == 6) st64(pk[n][m], rs, lane * 16 + m * 2048, base + wn * 8, 16);
                else { const u32x4 v = {pk[n - (n & 1)][m].x, pk[n - (n & 1)][m].y, pk[n][m].x, pk[n][m].y}; st128(v, rs, lane * 16 + m * 2048, base, 16); }
            }
        }
    }
}

__device__ __forceinline__ void colsum_half(const bf16x8 (&B)[7], f32x4& cs, int lane) {
    int r = lane & 15;
    SCHED0();
    asm volatile("" : "+v"(r));
#pragma unroll
    for (int n = 0; n < 7; ++n) {
        const unsigned o = r == n ? 0x3F803F80u : 0u;
        cs = __builtin_amdgcn_mfma_f32_16x16x32_bf16(B[n], __builtin_bit_cast(bf16x8, (u32x4){o, o, o, o}), cs, 0, 0, 0);
    }
}

__device__ __forceinline__ void check_flags(u32x4 v, const char* ws, unsigned off, u64 tag) {
    const unsigned lo = (unsigned)tag, hi = (unsigned)(tag >> 32);
    if (!__all((((v[0] ^ lo) | (v[1] ^ hi)) | ((v[2] ^ lo) | (v[3] ^ hi))) == 0u)) {
        const unsigned long long t0 = __builtin_amdgcn_s_memrealtime();
        bool ok;
        do { __builtin_amdgcn_s_sleep(2);
             ok = __hip_atomic_load((gu64*)(ws + off), RLX_AGENT) == tag && __hip_atomic_load((gu64*)(ws + off + 8), RLX_AGENT) == tag;
        } while (!__all(ok) && !deadline(t0));
    }
    asm volatile("" ::: "memory");
}

__global__ __launch_bounds__(512, 2) void fused_kernel(Args a) {
    extern __shared__ __attribute__((aligned(16))) char smem_[];
    ldsp smem = (ldsp)smem_;
    const int wid = __builtin_amdgcn_readfirstlane(threadIdx.x >> 6), lane = threadIdx.x & 63;
    const int bid = blockIdx.x, xcd = bid & 7, rk = bid >> 3;
    const int kh = xcd & 1, rb = (xcd >> 1) * 4 + (rk >> 3), c8 = rk & 7, cb = kh * 8 + c8;
    char* const ws = a.ws;
    const char* pc; unsigned code_len;
    asm volatile("s_getpc_b64 %0\n.Lgm_pc%=:\n\ts_mov_b32 %1, gm_code_end-.Lgm_pc%=" : "=s"(pc), "=s"(code_len));
    if (wid == 3) {
        const unsigned o = (unsigned)(rk + 32 * (lane & 15)) * 128u;
        dma4<false>(pc, o < code_len && code_len < (1u << 20) ? o : 0u, (unsigned)(size_t)smem + LDS_DUMMY);
    }
    asm volatile("" ::"s"(a.x), "s"(a.lre), "s"(a.lim), "s"(a.bre), "s"(a.bim), "s"(a.cre), "s"(a.cim), "s"(a.ldt), "s"(a.dsk), "s"(a.w1), "s"(a.b1), "s"(a.w2), "s"(a.b2), "s"(a.gw), "s"(a.out), "s"(a.ws));

    u32x4 xv[2][2], gv[2]; u64 ep = 0;
    if (wid < 4) {
        const int lnrow = 512 * (xcd >> 1) + 256 * kh + 8 * rk + 2 * wid;
#pragma unroll
        for (int i = 0; i < 2; ++i)
#pragma unroll
            for (int h = 0; h < 2; ++h) xv[i][h] = *(const u32x4*)(a.x + (size_t)(lnrow + i) * 1024 + h * 512 + lane * 8);
#pragma unroll
        for (int h = 0; h < 2; ++h) gv[h] = *(const u32x4*)(a.gw + h * 512 + lane * 8);
        ep = __hip_atomic_load((gu64*)(ws + WS_EPOCH), RLX_AGENT);
    }
    int wid2 = wid;
    asm volatile("" : "+s"(wid2)::"memory");
    if (__builtin_expect(wid2 >= 4, 1)) {
        __builtin_amdgcn_s_waitcnt(0x0F70);
        const int w = wid - 4, un = (xcd >> 1) * 32 + rk;
        const unsigned lb = (unsigned)(size_t)smem;
        const unsigned swz = (unsigned)(((lane & 7) ^ ((4 * (w & 1) + (lane >> 4)) & 7)) * 16);
        const unsigned vl = lane * 16;
        unsigned sd;
        asm volatile("s_mov_b32 %0, 0" : "=s"(sd));
        const LAS u32x4* const parea = (const LAS u32x4*)(smem + LDS_E + w * 1024 + lane * 16);
        const char* const pw2 = (const char*)a.w2 + (size_t)(1792 * kh + un) * 2048 + w * 512;
        constexpr int W2C = 128 * 2048;
        const __amdgpu_buffer_rsrc_t rs = __builtin_amdgcn_make_buffer_rsrc(ws, 0, WS_END, 0x00020000);
        u64 tag;
        f32x2 l16, wt[4];
        const unsigned vsa = (unsigned)((rb * 128 + 16 * (lane >> 5) + ((lane >> 3) & 3) + 32 * (w & 1) + 4 * (w >> 1)) * 2048) + swz;
        const unsigned vsb = (unsigned)((cb * 64 + 16 * (w >> 1) + 8 * (w & 1) + (lane >> 3)) * 2048) + swz;
        auto none = [] {};
        auto issue_s = [&](int t, auto mid) {
            const unsigned s = lb + ((t + 2) & 3) * SLOT + w * 1024;
            const int ko = ((t + rb + cb) & 15) * 128;
#pragma unroll
            for (int n = 0; n < 4; ++n) dma16<false>((const char*)a.x + ko + (64 * (n >> 1) + 8 * (n & 1)) * 2048, vsa, s + n * 4096);
            mid();
#pragma unroll
            for (int n = 0; n < 4; ++n) dma16<false>((const char*)((n & 1) ? a.bim : a.bre) + ko + (n >> 1) * 32 * 2048, vsb, s + 16384 + n * 4096);
        };
        {
            const unsigned va = (unsigned)((rb * 128 + 8 * w + (lane >> 3)) * 2048) + swz;
            unsigned vb[7];
#pragma unroll
            for (int i = 0; i < 7; ++i) {
                const int sl = 2 * i + (lane >> 5), kq = 2 * w + (sl >= 7 ? 1 : 0), nb2 = sl >= 7 ? sl - 7 : sl;
                vb[i] = (unsigned)((8 * kq + ((lane >> 2) & 7)) * 7168 + cb * 448 + nb2 * 64 + (((lane & 3) ^ (2 * (kq & 1))) * 16));
            }
            int slot = 0;
            auto issue = [&](int t, auto mid) {
                const unsigned s = lb + slot * SLOTM;
                slot = slot == 2 ? 0 : slot + 1;
                int ks;
                if (rb >= TSP) { const int u = rb + t - TSP; ks = t < TSP ? t : u >= 16 ? u - 16 + TSP : u; }
                else ks = t > TSP ? t : t == TSP ? rb : t < rb ? t : t + 1;
                ks = __builtin_amdgcn_readfirstlane(ks);
                const char* pb = (const char*)a.w1 + (size_t)ks * 64 * 7168;
#pragma unroll
                for (int i = 0; i < 7; ++i) dma16<false>(pb, vb[i], s + 16384 + (w * 7 + i) * 1024);
                mid();
#pragma unroll
                for (int n = 0; n < 4; ++n) dma16<false>((const char*)a.x + ks * 128 + n * 32 * 2048, va, s + w * 1024 + n * 4096);
            };
            issue(0, none);
            { const char* sp = w == 0 ? (const char*)a.b1 + cb * 448 : w == 1 ? (const char*)a.lre + cb * 128 : w == 2 ? (const char*)a.lim + cb * 128 : (const char*)a.ldt + cb * 128;
              if (lane < (w == 0 ? 28 : 8)) dma16<false>(sp, vl, lb + LDS_SM + (w == 0 ? 0 : 384 + w * 128)); }
            issue(1, none);
            WAITV(12); BAR();
            issue(2, none);
            { const u32x2 tv = *(const LAS u32x2*)(smem + LDS_TAG);
              tag = ((u64)(unsigned)__builtin_amdgcn_readfirstlane(tv.y) << 32) | (unsigned)__builtin_amdgcn_readfirstlane(tv.x); }
            const char* const pbw = (const char*)((w & 1) ? a.bim : a.bre) + (size_t)(512 * kh + 4 * un + 2 * (w >> 1)) * 2048;
            const unsigned fo = (lane & 8) ? WS_FC + cb * 128 + (lane & 7) * 16 : WS_FY + rb * 128 + (lane & 7) * 16;
            const int lnrow = 512 * (xcd >> 1) + 256 * kh + 8 * rk;
_Pragma("unroll 1")
            for (int t = 0; t < 13; ++t) {
                WAITV(11); BAR();
                issue(t + 3, [&] { if (t != TSP + 1 && t != 12) swarm8(sd, t < 9 ? pbw + (t > TSP ? t - 1 : t) * 512 : pw2 + (t - 9) * W2C); });
                if (t == TSP + 1 && w == 0) {
                    const u32x4 v = *(const LAS u32x4*)(smem + LDS_BC + lane * 16);
                    st128(v, rs, lane < 8 ? WS_STAT + (unsigned)(lnrow + lane) * 16u : WS_CSP + (unsigned)(cb * 16 + rb) * 896u + (lane - 8) * 16, 0, 16);
                }
                if (t == TSP + 3 && w == 0) __hip_atomic_store((gu64*)(ws + ((lane & 1) ? WS_FC + (cb * 16 + rb) * 8 : WS_FY + ((lnrow >> 7) * 16 + (rk & 15)) * 8)), tag, RLX_AGENT);
                if (t == 11) dma16<true>(ws, fo, lb + LDS_E + w * 1024);
            }
            WAITV(11); BAR();
            check_flags(*parea, ws, fo, tag);
#pragma unroll
            for (int n = 0; n < 3; ++n) dma16<true>(ws + WS_CSP + cb * 14336 + (w + 4 * n) * 1024, vl, lb + LDS_GATH + (w + 4 * n) * 1024);
            if (w < 2) dma16<true>(ws + WS_CSP + cb * 14336 + (w + 12) * 1024, vl, lb + LDS_GATH + (w + 12) * 1024);
            else dma16<true>(ws + WS_STAT + rb * 2048 + (w - 2) * 1024, vl, lb + LDS_STAT + (w - 2) * 1024);
            WAITV(0); BAR();
            {
                const int col = w * 56 + (lane < 56 ? lane : 0);
                float s = 0.f;
#pragma unroll
                for (int i = 0; i < 16; ++i) s += *(const LAS float*)(smem + LDS_GATH + i * 896 + col * 4);
                const float gg = *(const LAS float*)(smem + LDS_STAT + (w * 32 + (lane & 31)) * 16 + 8);
                const float b1v = bf1(*(const LAS u16*)(smem + LDS_SM + col * 2));
                *(LAS float*)(smem + LDS_BC + 1024 + col * 4) = s;
                *(LAS float*)(smem + LDS_BC + col * 4) = b1v;
                *(LAS float*)(smem + LDS_G2 + (w * 32 + (lane & 31)) * 4) = 2.0f * gg;
            }
            issue_s(0, none);
            __builtin_amdgcn_s_waitcnt(0xC07F);
            BAR();
        }
        const char* const a2 = ws + WS_A2 + (size_t)rb * 88 * 16384 + w * 4096;
        const unsigned vr = (unsigned)((c8 * 128 + 8 * w + (lane >> 3)) * 2048) + swz;
        unsigned vt[2];
        { const int rho = (lane >> 2) & 7;
#pragma unroll
          for (int qq = 0; qq < 2; ++qq) { const int q = 2 * (w & 1) + qq; const int k = 32 * (w >> 1) + (rho < 4 ? 4 * q + rho : 16 + 4 * q + rho - 4);
              vt[qq] = (unsigned)(k * 2048 + c8 * 256 + (lane >> 5) * 64 + (((lane & 3) ^ (2 * qq)) * 16)); } }
        auto issue_gm = [&](int t, auto mid) {
            const unsigned s = lb + ((t + 2) & 3) * SLOT;
            const int km = kh * 28 + t;
            dma64c(a2 + (size_t)(32 + km) * 16384, vl, s + w * 4096);
            mid();
            const char* pb = (const char*)a.w2 + (size_t)km * 64 * 2048;
#pragma unroll
            for (int n = 0; n < 4; ++n) dma16<false>(pb + (n & 1) * 128, vt[n >> 1], s + 16384 + (w * 4 + n) * 1024);
        };
        auto issue_gs = [&](int t, auto mid) {
            const unsigned s = lb + ((t + 2) & 3) * SLOT;
            const int ks = kh * 16 + (t - 28);
            dma64c(a2 + (size_t)ks * 16384, vl, s + w * 4096);
            mid();
            const char* pb = (const char*)((ks & 1) ? a.cim : a.cre) + (ks >> 1) * 128;
#pragma unroll
            for (int n = 0; n < 4; ++n) dma16<false>(pb + n * 32 * 2048, vr, s + 16384 + (w + 4 * n) * 1024);
        };
        {
_Pragma("unroll 1")
            for (int t = 1; t < 4; ++t) issue_s(t, none);
            {
                const float lr = bf1(*(const LAS u16*)(smem + LDS_SM + 512 + lane * 2)), li = bf1(*(const LAS u16*)(smem + LDS_SM + 640 + lane * 2));
                const float dt = (float)(__bf16)expf(bf1(*(const LAS u16*)(smem + LDS_SM + 768 + lane * 2)));
                const float zr = dt * lr, zi = dt * li;
                float sn, cs; sincosf(zi, &sn, &cs);
                const float er = expf(zr), Lr = er * cs, Li = er * sn;
                float pr = Lr, pi = Li;
#pragma unroll
                for (int i = 0; i < 4; ++i) cfma(pr, pi, pr, pi, 0.f, 0.f);
                l16 = (f32x2){pr, pi};
                if (w == 0) {
                    const float den = 1.0f / (lr * lr + li * li), nr = Lr - 1.0f;
                    *(LAS f32x4*)(smem + LDS_COEF + lane * 16) = (f32x4){Lr, Li, (nr * lr + Li * li) * den, (Li * lr - nr * li) * den};
                }
                if (w == 2) {
                    float qr = 1.0f, qi = 0.f;
#pragma unroll
                    for (int i = 0; i < 8; ++i) { *(LAS f32x2*)(smem + LDS_PSEG + (i * 64 + lane) * 8) = (f32x2){qr, qi}; cfma(pr, pi, qr, qi, 0.f, 0.f); }
                }
#pragma unroll
                for (int i = 0; i < 3; ++i) cfma(pr, pi, pr, pi, 0.f, 0.f);
                float p2r = pr, p2i = pi; cfma(pr, pi, p2r, p2i, 0.f, 0.f);
                float p4r = p2r, p4i = p2i; cfma(p2r, p2i, p4r, p4i, 0.f, 0.f);
                const int e0 = (rb - 1 - w) & 3, top = (rb - 1 - w) >> 2;
                float qr = (e0 & 1) ? pr : 1.0f, qi = (e0 & 1) ? pi : 0.f;
                if (e0 & 2) cfma(p2r, p2i, qr, qi, 0.f, 0.f);
#pragma unroll
                for (int j = 3; j >= 0; --j) {
                    wt[j] = j <= top ? (f32x2){qr, qi} : (f32x2){0.f, 0.f};
                    if (j <= top) cfma(p4r, p4i, qr, qi, 0.f, 0.f);
                }
                asm volatile("" : "+v"(wt[0]), "+v"(wt[1]), "+v"(wt[2]), "+v"(wt[3]), "+v"(l16));
                __builtin_amdgcn_s_waitcnt(0xC07F);
            }
            WAITV(24); BAR();
            const unsigned fo = WS_FM + (rb * 2 + kh) * 256 + (lane & 15) * 16;
_Pragma("unroll 1")
            for (int t = 0; t < 10; ++t) { WAITV(16); BAR(); issue_s(t + 4, [&] { swarm4(sd, pw2 + (3 + (t >> 1)) * W2C + (t & 1) * 256); }); }
            WAITV(16); BAR(); issue_s(14, [&] { dma16<true>(ws, fo, lb + LDS_E + w * 1024); });
            WAITV(17); BAR(); issue_s(15, none);
            WAITV(8); BAR();
            check_flags(*parea, ws, fo, tag);
            swarm8(sd, pw2 + 8 * W2C);
            BAR();
            issue_gm(0, none);
            WAITV(8); BAR();
            issue_gm(1, none);
            BAR();
            issue_gm(2, none); issue_gm(3, none);
        }
        {
            const unsigned fo = WS_FS + (rb * 2 + kh) * 256 + (lane & 15) * 16;
            const char* const pcr = (const char*)a.cre + (8 * un + 2 * w) * 2048 + kh * 1024;
            const char* const pci = (const char*)a.cim + (8 * un + 2 * w) * 2048 + kh * 1024;
            const unsigned tag_lo = (unsigned)tag, tag_hi = (unsigned)(tag >> 32);
            WAITV(24); BAR();
            if (w == 1) {
                f32x2 e[8];
#pragma unroll
                for (int i = 0; i < 8; ++i) e[i] = *(const LAS f32x2*)(smem + LDS_E + (i * 64 + lane) * 8);
                LDS_FENCE();
                float er = 0.f, ei = 0.f;
#pragma unroll
                for (int i = 0; i < 8; ++i) {
                    *(LAS f32x2*)(smem + LDS_E + (i * 64 + lane) * 8) = (f32x2){er, ei};
                    cfma(l16.x, l16.y, er, ei, e[i].x, e[i].y);
                }
                st128((u32x4){__float_as_uint(er), tag_lo, __float_as_uint(ei), tag_hi}, rs, vl, WS_GRAN + (unsigned)(cb * 16 + rb) * 1024u, 16);
                __builtin_amdgcn_s_waitcnt(0xC07F);
            }
_Pragma("unroll 1")
            for (int t = 0; t < 8; ++t) { WAITV(16); BAR(); issue_gm(t + 4, [&] { swarm4(sd, pw2 + (9 + (t >> 1)) * W2C + (t & 1) * 256); }); }
#pragma unroll
            for (int j = 0; j < 4; ++j)
                if (w + 4 * j < rb) dma16<true>(ws + WS_GRAN + cb * 16384 + (w + 4 * j) * 1024, vl, lb + LDS_GATH + (w * 4 + j) * 1024);
            WAITV(16); BAR(); issue_gm(12, [&] { swarm4(sd, pw2 + 13 * W2C); });
            WAITV(16); BAR(); issue_gm(13, none);
            WAITV(16); BAR();
            {
                u32x4 g[4];
                unsigned bad = 0;
#pragma unroll
                for (int j = 0; j < 4; ++j) g[j] = *(const LAS u32x4*)(smem + LDS_GATH + (w * 4 + j) * 1024 + lane * 16);
#pragma unroll
                for (int j = 0; j < 4; ++j) bad |= w + 4 * j < rb ? (g[j][1] ^ tag_lo) | (g[j][3] ^ tag_hi) : 0u;
                if (!__all(bad == 0u)) {
                    const unsigned long long t0 = __builtin_amdgcn_s_memrealtime();
                    do {
                        __builtin_amdgcn_s_sleep(1);
                        bad = 0;
                        asm volatile("" ::: "memory");
#pragma unroll
                        for (int j = 0; j < 4; ++j) {
                            g[j] = __builtin_amdgcn_raw_buffer_load_b128(rs, vl + (w + 4 * j) * 1024, WS_GRAN + (unsigned)cb * 16384u, 16);
                            bad |= w + 4 * j < rb ? (g[j][1] ^ tag_lo) | (g[j][3] ^ tag_hi) : 0u;
                        }
                    } while (!__all(bad == 0u) && !deadline(t0));
                }
                WAITV(8); BAR();
                float sr = 0.f, si = 0.f;
#pragma unroll
                for (int j = 0; j < 4; ++j) {
                    float vr_ = w + 4 * j < rb ? __uint_as_float(g[j][0]) : 0.f, vi_ = w + 4 * j < rb ? __uint_as_float(g[j][2]) : 0.f;
                    cfma(wt[j].x, wt[j].y, vr_, vi_, sr, si); sr = vr_; si = vi_;
                }
                *(LAS f32x2*)(smem + LDS_BC + lane * 32 + w * 8) = (f32x2){sr, si};
                __builtin_amdgcn_s_waitcnt(0xC07F);
            }
            WAITV(0); BAR();
_Pragma("unroll 1")
            for (int t = 14; t < 17; ++t) issue_gm(t, none);
            swarm4(sd, pw2 + 13 * W2C + 256);
_Pragma("unroll 1")
            for (int t = 13; t < 21; ++t) { WAITV(16); BAR(); issue_gm(t + 4, [&] { swarm2x2(sd, ((t & 1) ? pcr : pci) + ((t - 13) >> 1) * 128); }); }
            dma16<true>(ws, fo, lb + LDS_E + w * 1024);
            WAITV(16); BAR(); issue_gm(25, [&] { swarm2x2(sd, pcr + 512); });
            WAITV(16); BAR(); issue_gm(26, none);
            { u32x4 fv;
              WAITV(16); BAR(); issue_gm(27, [&] { fv = *parea; });
              check_flags(fv, ws, fo, tag); }
_Pragma("unroll 1")
            for (int t = 24; t < 31; ++t) { WAITV(16); BAR(); issue_gs(t + 4, [&] { swarm2x2(sd, ((t & 1) ? pcr : pci) + ((t - 15) >> 1) * 128); }); }
_Pragma("unroll 1")
            for (int t = 31; t < 40; ++t) { WAITV(16); BAR(); issue_gs(t + 4, none); }
            WAITV(16); BAR();
            {
                const int rl = 16 * w + (lane >> 4);
#pragma unroll
                for (int n = 0; n < 4; ++n) {
                    const int rr = rl + 4 * n;
                    dma16<false>((const char*)a.x + (size_t)(rb * 128 + kh * 32) * 2048 + c8 * 256, (unsigned)((64 * (rr >> 5) + (rr & 31)) * 2048 + (((lane & 15) ^ (rr & 15)) * 16)), lb + 2 * SLOT + (w * 4 + n) * 1024);
                }
                dma4<false>((const char*)((w & 1) ? a.b2 : a.dsk) + c8 * 256, (unsigned)lane * 4u, lb + 2 * SLOT + 16384 + w * 256);
            }
            WAITV(13); BAR();
            WAITV(5); BAR();
            WAITV(0); BAR();
            asm volatile("s_waitcnt lgkmcnt(0)" ::"s"(sd));
        }
        if (bid == 0 && w == 0) {
            const unsigned long long t0 = __builtin_amdgcn_s_memrealtime();
            for (;;) {
                bool ok = true;
#pragma unroll
                for (int i = 0; i < 4; ++i) ok &= __hip_atomic_load((gu64*)(ws + WS_FY) + i * 64 + lane, RLX_AGENT) == tag;
                if (__all(ok) || deadline(t0)) break;
                __builtin_amdgcn_s_sleep(2);
            }
            if (lane == 0) __hip_atomic_store((gu64*)(ws + WS_EPOCH), tag, RLX_AGENT);
        }
        return;
    }

    const int wm = wid >> 1, wn = wid & 1;
    const unsigned lb = (unsigned)(size_t)smem;
    const __amdgpu_buffer_rsrc_t rs = __builtin_amdgcn_make_buffer_rsrc(ws, 0, WS_END, 0x00020000);
    STAMP(0);
    {
        if (wid == 0) {
            const u64 t = next_tag(ep);
            if (lane == 0) *(LAS u32x2*)(smem + LDS_TAG) = (u32x2){(unsigned)t, (unsigned)(t >> 32)};
        }
        f32x2 xf[2][8];
        float s[2], gd[2], vs[2];
#pragma unroll
        for (int i = 0; i < 2; ++i) {
            f32x2 s2 = {0.f, 0.f}, g2 = {0.f, 0.f};
#pragma unroll
            for (int h = 0; h < 2; ++h)
#pragma unroll
                for (int j = 0; j < 4; ++j) {
                    const f32x2 v = {bflo(xv[i][h][j]), bfhi(xv[i][h][j])}, gw = {bflo(gv[h][j]), bfhi(gv[h][j])};
                    xf[i][h * 4 + j] = v; s2 += v; g2 += v * gw;
                }
            s[i] = wave_sum(s2.x + s2.y) * (1.0f / 1024.0f); gd[i] = wave_sum(g2.x + g2.y);
        }
#pragma unroll
        for (int i = 0; i < 2; ++i) {
            f32x2 v2 = {0.f, 0.f};
#pragma unroll
            for (int j = 0; j < 8; ++j) { const f32x2 d = xf[i][j] - s[i]; v2 += d * d; }
            vs[i] = wave_sum(v2.x + v2.y);
        }
        const float gl = lane ? gd[1] : gd[0], ml = lane ? s[1] : s[0], vl = lane ? vs[1] : vs[0];
        const float rstd = 1.0f / sqrtf(vl * (1.0f / 1024.0f) + 1e-5f), g = 1.0f / (1.0f + expf(-gl));
        if (lane < 2) *(LAS f32x4*)(smem + LDS_BC + (wid * 2 + lane) * 16) = (f32x4){rstd, -rstd * ml, g, 0.f};
    }
    bf16x8 A0[4], A1[4];
    {
        f32x4 acc[7][4];
        bf16x8 B0[7], B1[7];
        LaneC c = lane_consts<PH_M1>(lb, wm, wn, lane);
        __builtin_amdgcn_s_waitcnt(0xC07F);
        STAMP(1);
        BAR();
        STAMP(2);
        load_frags<PH_M1, 0, 7>(c, A0, B0);
#pragma unroll
        for (int n = 0; n < 7; ++n)
#pragma unroll
            for (int m = 0; m < 4; ++m) acc[n][m] = (f32x4){0.f, 0.f, 0.f, 0.f};
        SCHED0();
_Pragma("unroll 1")
        for (int t = 0; t < TSP; ++t) cstep<PH_M1, 7>(c, SLOTM, acc, A0, B0, A1, B1);
        SCHED0();
        { ONCE() {
            f32x4 cs = {0.f, 0.f, 0.f, 0.f};
            load_frags<PH_M1, 1, 7>(c, A1, B1);
            mma<PH_M1, 7>(acc, A0, B0);
            advance<PH_M1>(c, TSP == 2 ? (unsigned)(-2 * SLOTM) : (unsigned)SLOTM);
            sched_half<18, 28, 5>();
            colsum_half(B0, cs, lane);
            SCHED0();
            __builtin_amdgcn_s_waitcnt(0xC07F);
            BAR();
            SCHED0();
            load_frags<PH_M1, 0, 7>(c, A0, B0);
            mma<PH_M1, 7>(acc, A1, B1);
            sched_half<18, 28>();
            colsum_half(B1, cs, lane);
            SCHED0();
            const int r = lane & 15, nbt = r == 6 ? 6 + wn : r + 8 * wn;
            if (r < 7) *(LAS f32x4*)(smem + LDS_BC + 128 + (nbt * 16 + (lane >> 4) * 4) * 4) = cs;
            SCHED0();
        } }
        int sc = (TSP + 1) % 3;
_Pragma("unroll 1")
        for (int t = TSP + 1; t < 15; ++t) { cstep<PH_M1, 7>(c, sc == 2 ? (unsigned)(-2 * SLOTM) : (unsigned)SLOTM, acc, A0, B0, A1, B1); sc = sc == 2 ? 0 : sc + 1; }
        { ONCE() cstep2<PH_M1, PH_M1, true, 7>(c, c, acc, A0, B0, A1, B1); }
        STAMP(3);
        m1_epilogue(acc, smem, rs, rb, cb, wm, wn, lane);
    }
    u64 tag;
    { const u32x2 tv = *(const LAS u32x2*)(smem + LDS_TAG);
      tag = ((u64)(unsigned)__builtin_amdgcn_readfirstlane(tv.y) << 32) | (unsigned)__builtin_amdgcn_readfirstlane(tv.x); }
    f32x4 sv[4][4];
    bf16x8 B0[4], B1[4];
    const int ch = lane & 15, q = lane >> 4, seg = wm * 4 + q;
    {
#pragma unroll
        for (int n = 0; n < 4; ++n)
#pragma unroll
            for (int m = 0; m < 4; ++m) sv[n][m] = (f32x4){0.f, 0.f, 0.f, 0.f};
        LaneC c = lane_consts<PH_S1>(lb + 2 * SLOT, wm, wn, lane);
        STAMP(4);
        BAR();
        STAMP(5);
        load_frags<PH_S1, 0, 4>(c, A0, B0);
        SCHED0();
#define S1STEP(t) cstep<PH_S1, 4>(c, ringd(t), sv, A0, B0, A1, B1)
_Pragma("unroll 1")
        for (int t = 0; t < 4; ++t) S1STEP(t);
        signal_flag((gu64*)(ws + WS_FM) + (rb * 2 + kh) * 32 + c8 * 4 + wid, tag, lane);
_Pragma("unroll 1")
        for (int t = 4; t < 15; ++t) S1STEP(t);
        f32x2 lam[2];
#pragma unroll
        for (int cbk = 0; cbk < 2; ++cbk) lam[cbk] = *(const LAS f32x2*)(smem + LDS_COEF + (wn * 32 + cbk * 16 + ch) * 16);
        SCHED0();
        { ONCE() cstep2<PH_S1, PH_S1, true, 4>(c, c, sv, A0, B0, A1, B1); }
        STAMP(6);
        asm volatile("s_nop 15");
        SCHED0();
        float er[2], ei[2];
#pragma unroll
        for (int cbk = 0; cbk < 2; ++cbk) { er[cbk] = sv[cbk][0][0]; ei[cbk] = sv[2 + cbk][0][0]; }
#pragma unroll
        for (int i = 1; i < 16; ++i)
#pragma unroll
            for (int cbk = 0; cbk < 2; ++cbk) cfma(lam[cbk].x, lam[cbk].y, er[cbk], ei[cbk], sv[cbk][i >> 2][i & 3], sv[2 + cbk][i >> 2][i & 3]);
#pragma unroll
        for (int cbk = 0; cbk < 2; ++cbk) *(LAS f32x2*)(smem + LDS_E + (seg * 64 + wn * 32 + cbk * 16 + ch) * 8) = (f32x2){er[cbk], ei[cbk]};
        STAMP(7);
    }
    {
        f32x4 acc[4][4];
#pragma unroll
        for (int n = 0; n < 4; ++n)
#pragma unroll
            for (int m = 0; m < 4; ++m) acc[n][m] = (f32x4){0.f, 0.f, 0.f, 0.f};
        LaneC cm = lane_consts<PH_GM>(lb + 2 * SLOT, wm, wn, lane);
        __builtin_amdgcn_s_waitcnt(0xC07F);
        STAMP(8);
        BAR();
        STAMP(9);
        load_frags<PH_GM, 0, 4>(cm, A0, B0);
        SCHED0();
#define GMSTEP(t) cstep<PH_GM, 4>(cm, ringd(t), acc, A0, B0, A1, B1)
_Pragma("unroll 1")
        for (int t = 0; t < 13; ++t) GMSTEP(t);
        STAMP(10);
        {
            f32x4 g2[4], k0[2]; float zr[2], zi[2];
#pragma unroll
            for (int cbk = 0; cbk < 2; ++cbk) {
                const int cl = wn * 32 + cbk * 16 + ch;
                const f32x2 c0 = *(const LAS f32x2*)(smem + LDS_E + (seg * 64 + cl) * 8), ps = *(const LAS f32x2*)(smem + LDS_PSEG + (seg * 64 + cl) * 8);
                const f32x4 sp = *(const LAS f32x4*)(smem + LDS_BC + cl * 32) + *(const LAS f32x4*)(smem + LDS_BC + cl * 32 + 16);
                zr[cbk] = sp[0] + sp[2]; zi[cbk] = sp[1] + sp[3];
                cfma(ps.x, ps.y, zr[cbk], zi[cbk], c0.x, c0.y);
                k0[cbk] = *(const LAS f32x4*)(smem + LDS_COEF + (wn * 32 + cbk * 16 + ch) * 16);
            }
#pragma unroll
            for (int m = 0; m < 4; ++m) g2[m] = *(const LAS f32x4*)(smem + LDS_G2 + (seg * 16 + m * 4) * 4);
            const unsigned so = WS_A2 + (unsigned)(rb * 88 + 2 * cb) * 16384u;
#pragma unroll
            for (int th = 0; th < 2; ++th) {
#pragma unroll
                for (int i = th * 8; i < th * 8 + 8; ++i)
#pragma unroll
                    for (int cbk = 0; cbk < 2; ++cbk) {
                        cfma(k0[cbk][0], k0[cbk][1], zr[cbk], zi[cbk], sv[cbk][i >> 2][i & 3], sv[2 + cbk][i >> 2][i & 3]);
                        sv[cbk][i >> 2][i & 3] = zr[cbk]; sv[2 + cbk][i >> 2][i & 3] = zi[cbk];
                    }
#pragma unroll
                for (int cbk = 0; cbk < 2; ++cbk) {
                    const int kb = 4 * wn + 2 * cbk + (ch >> 3);
                    const unsigned vo = (unsigned)((kb * 8 + seg) * 256 + (((ch & 7) ^ (4 * (kb & 1))) * 16));
                    u32x4 vr, vi;
#pragma unroll
                    for (int mh = 0; mh < 2; ++mh) {
                        const f32x4 re = sv[cbk][th * 2 + mh], im = sv[2 + cbk][th * 2 + mh];
                        const f32x4 hr4 = (re * k0[cbk][2] - im * k0[cbk][3]) * g2[th * 2 + mh], hi4 = (im * -k0[cbk][2] - re * k0[cbk][3]) * g2[th * 2 + mh];
                        vr[mh * 2] = pkbf(hr4[0], hr4[1]); vr[mh * 2 + 1] = pkbf(hr4[2], hr4[3]);
                        vi[mh * 2] = pkbf(hi4[0], hi4[1]); vi[mh * 2 + 1] = pkbf(hi4[2], hi4[3]);
                    }
                    st128(vr, rs, vo + th * 128, so, 16);
                    st128(vi, rs, vo + th * 128, so + 16384u, 16);
                }
            }
            SCHED0();
        }
        STAMP(11);
_Pragma("unroll 1")
        for (int t = 13; t < 16; ++t) GMSTEP(t);
        signal_flag((gu64*)(ws + WS_FS) + (rb * 2 + kh) * 32 + c8 * 4 + wid, tag, lane);
_Pragma("unroll 1")
        for (int t = 16; t < 27; ++t) GMSTEP(t);
        LaneC cs = lane_consts<PH_GS>(lb + 2 * SLOT, wm, wn, lane);
        float og[4];
#pragma unroll
        for (int m = 0; m < 4; ++m) og[m] = 1.0f - *(const LAS float*)(smem + LDS_STAT + (wm * 64 + m * 16 + ch) * 16 + 8);
        SCHED0();
        { ONCE() cstep2<PH_GM, PH_GS, false, 4>(cm, cs, acc, A0, B0, A1, B1); }
#pragma unroll
        for (int n = 0; n < 4; ++n)
#pragma unroll
            for (int m = 0; m < 4; ++m) acc[n][m] *= og[m];
        SCHED0();
        STAMP(12);
_Pragma("unroll 1")
        for (int t = 28; t < 43; ++t) cstep<PH_GS, 4>(cs, ringd(t), acc, A0, B0, A1, B1);
        { ONCE() cstep2<PH_GS, PH_GS, true, 4>(cs, cs, acc, A0, B0, A1, B1); }
        const int r = ch;
        const int t0r = rb * 128 + wm * 64 + kh * 32 + r, d0 = c8 * 128 + wn * 64 + q * 4;

        STAMP(13);
        if (kh) {
#pragma unroll
            for (int n = 0; n < 4; ++n)
#pragma unroll
                for (int mm = 0; mm < 2; ++mm) { const f32x4 t = acc[n][mm]; acc[n][mm] = acc[n][2 + mm]; acc[n][2 + mm] = t; }
        }
        const unsigned xo = WS_XB + (unsigned)(bid * 4 + wid) * 8192u, xi = WS_XB + (unsigned)((bid ^ 1) * 4 + wid) * 8192u;
#pragma unroll
        for (int n = 0; n < 4; ++n)
#pragma unroll
            for (int mm = 0; mm < 2; ++mm) st128(__builtin_bit_cast(u32x4, acc[n][2 + mm]), rs, lane * 16 + (n * 2 + mm) * 1024, xo, 16);
        const __amdgpu_buffer_rsrc_t ro = __builtin_amdgcn_make_buffer_rsrc(a.out, 0, 8u << 20, 0x00020000);
        f32x4 o[4][2];
#pragma unroll
        for (int mm = 0; mm < 2; ++mm) {
            if (mm == 1) signal_flag((gu64*)(ws + WS_XFLAG) + (bid * 4 + wid) * 8, tag, lane);
            const float g = *(const LAS float*)(smem + LDS_STAT + (wm * 64 + kh * 32 + mm * 16 + r) * 16 + 8);
#pragma unroll
            for (int n = 0; n < 4; ++n) {
                const u32x2 xw = *(const LAS u32x2*)(smem + 2 * SLOT + (wm * 32 + mm * 16 + r) * 256 + (((wn * 8 + n * 2 + (q >> 1)) ^ r) * 16) + (q & 1) * 8);
                const u32x2 dw = *(const LAS u32x2*)(smem + 2 * SLOT + 16384 + (wn * 64 + n * 16 + q * 4) * 2), bw = *(const LAS u32x2*)(smem + 2 * SLOT + 16384 + 256 + (wn * 64 + n * 16 + q * 4) * 2);
                const f32x4 own = acc[n][mm];
                o[n][mm][0] = own[0] + g * (bflo(xw.x) * bflo(dw.x)) + (1.0f - g) * bflo(bw.x);
                o[n][mm][1] = own[1] + g * (bfhi(xw.x) * bfhi(dw.x)) + (1.0f - g) * bfhi(bw.x);
                o[n][mm][2] = own[2] + g * (bflo(xw.y) * bflo(dw.y)) + (1.0f - g) * bflo(bw.y);
                o[n][mm][3] = own[3] + g * (bfhi(xw.y) * bfhi(dw.y)) + (1.0f - g) * bfhi(bw.y);
                asm volatile("" : "+v"(o[n][mm]));
            }
        }
        STAMP(14);
        {
            gu64* fl = (gu64*)(ws + WS_XFLAG) + ((bid ^ 1) * 4 + wid) * 8;
            wait_flags(fl, __hip_atomic_load(fl, RLX_AGENT), tag);
        }
        STAMP(15);
#pragma unroll
        for (int n = 0; n < 4; ++n)
#pragma unroll
            for (int mm = 0; mm < 2; ++mm) {
                const u32x4 v = __builtin_amdgcn_raw_buffer_load_b128(rs, lane * 16 + (n * 2 + mm) * 1024, xi, 16);
                o[n][mm] += __builtin_bit_cast(f32x4, v);
            }
#pragma unroll
        for (int mm = 0; mm < 2; ++mm)
#pragma unroll
            for (int n = 0; n < 4; ++n) __builtin_amdgcn_raw_buffer_store_b128(__builtin_bit_cast(u32x4, o[n][mm]), ro, ((t0r + mm * 16) * 1024 + d0 + n * 16) * 4, 0, 2);
    }
}
}
extern "C" __global__ void gm_code_end() {}

extern "C" void kernel_launch(void* const* d_in, const int* in_sizes, int n_in,
                              void* d_out, int out_size, void* d_ws, size_t ws_size,
                              hipStream_t stream) {
    using namespace gm;
    Args a;
    a.x = (const u16*)d_in[0]; a.lre = (const u16*)d_in[1]; a.lim = (const u16*)d_in[2]; a.bre = (const u16*)d_in[3]; a.bim = (const u16*)d_in[4];
    a.cre = (const u16*)d_in[5]; a.cim = (const u16*)d_in[6]; a.ldt = (const u16*)d_in[7]; a.dsk = (const u16*)d_in[8];
    a.w1 = (const u16*)d_in[10]; a.b1 = (const u16*)d_in[11]; a.w2 = (const u16*)d_in[12]; a.b2 = (const u16*)d_in[13]; a.gw = (const u16*)d_in[14];
    a.out = (float*)d_out; a.ws = (char*)d_ws;
    (void)hipFuncSetAttribute((const void*)fused_kernel, hipFuncAttributeMaxDynamicSharedMemorySize, LDS_BYTES);
    fused_kernel<<<256, 512, LDS_BYTES, stream>>>(a);
}
```

```cpp
#include <hip/hip_runtime.h>
#include <hip/hip_bf16.h>

namespace gm {
#define LAS __attribute__((address_space(3)))
#define GAS __attribute__((address_space(1)))
typedef unsigned short u16;
typedef short bf16x8 __attribute__((ext_vector_type(8)));
typedef short s16x4 __attribute__((ext_vector_type(4)));
typedef float f32x4 __attribute__((ext_vector_type(4)));
typedef float f32x2 __attribute__((ext_vector_type(2)));
typedef unsigned u32x4 __attribute__((ext_vector_type(4)));
typedef unsigned u32x2 __attribute__((ext_vector_type(2)));
typedef LAS char* ldsp;
typedef unsigned long long u64;
typedef GAS u64 gu64;

#define BAR() __builtin_amdgcn_s_barrier()
#define WAITV(n) asm volatile("s_waitcnt vmcnt(%0)" ::"i"(n) : "memory")
#define SCHED0() __builtin_amdgcn_sched_barrier(0)
#define LDS_FENCE() do { SCHED0(); __builtin_amdgcn_s_waitcnt(0xC07F); asm volatile("" ::: "memory"); SCHED0(); } while (0)
#define ONCE() int once_ = 1; asm volatile("" : "+s"(once_)); _Pragma("unroll 1") for (int i_ = 0; i_ < once_; ++i_)
#define RLX_AGENT __ATOMIC_RELAXED, __HIP_MEMORY_SCOPE_AGENT

constexpr unsigned WS_EPOCH = 0;
constexpr unsigned WS_FY = 4096;
constexpr unsigned WS_FC = 12288;
constexpr unsigned WS_FS = 16384;
constexpr unsigned WS_FM = 24576;
constexpr unsigned WS_XFLAG = 32768;
constexpr unsigned WS_GRAN = 131072;
constexpr unsigned WS_STAT = 1u << 20;
constexpr unsigned WS_CSP = 2u << 20;
constexpr unsigned WS_A2 = 8u << 20;
constexpr unsigned WS_XB = 32u << 20;
constexpr unsigned WS_END = 40u << 20;

constexpr int SLOT = 32768, SLOTM = 45056;
constexpr int LDS_PSEG = 131072;
constexpr int LDS_E = 135168;
constexpr int LDS_COEF = LDS_E + 4096;
constexpr int LDS_BC = LDS_COEF + 1024;
constexpr int LDS_STAT = LDS_BC + 2048;
constexpr int LDS_G2 = LDS_STAT + 2048;
constexpr int LDS_DUMMY = LDS_G2;
constexpr int LDS_TAG = LDS_G2 + 512;
constexpr int LDS_GATH = LDS_TAG + 64;
constexpr int LDS_SM = LDS_GATH + 16384;
constexpr int LDS_BYTES = LDS_SM + 1024;
static_assert(LDS_BYTES <= 160 * 1024, "LDS");

constexpr int TSP = 2;
enum { PH_S1 = 0, PH_M1 = 1, PH_GS = 2, PH_GM = 3 };

struct Args {
    const u16 *x, *lre, *lim, *bre, *bim, *cre, *cim, *ldt, *dsk, *w1, *b1, *w2, *b2, *gw;
    float* out;
    char* ws;
};

__device__ __forceinline__ bf16x8 lds128(ldsp p) { return *(const LAS bf16x8*)p; }
__device__ __forceinline__ s16x4 ldstr(ldsp p) { return __builtin_amdgcn_ds_read_tr16_b64_v4i16((LAS s16x4*)p); }
__device__ __forceinline__ bf16x8 cat(s16x4 a, s16x4 b) { return __builtin_shufflevector(a, b, 0, 1, 2, 3, 4, 5, 6, 7); }
template <bool C> __device__ __forceinline__ void dma16(const void* sb, unsigned vo, unsigned la) {
    if constexpr (C) asm volatile("s_mov_b32 m0, %0\n\ts_nop 0\n\tglobal_load_lds_dwordx4 %1, %2 sc1" ::"s"(la), "v"(vo), "s"(sb) : "memory");
    else asm volatile("s_mov_b32 m0, %0\n\ts_nop 0\n\tglobal_load_lds_dwordx4 %1, %2" ::"s"(la), "v"(vo), "s"(sb) : "memory");
}
__device__ __forceinline__ void dma64c(const void* sb, unsigned vo, unsigned la) {
    asm volatile("s_mov_b32 m0, %0\n\ts_nop 0\n\tglobal_load_lds_dwordx4 %1, %2 sc1\n\tglobal_load_lds_dwordx4 %1, %2 offset:1024 sc1\n\t"
                 "global_load_lds_dwordx4 %1, %2 offset:2048 sc1\n\tglobal_load_lds_dwordx4 %1, %2 offset:3072 sc1" ::"s"(la), "v"(vo), "s"(sb) : "memory");
}
template <bool C> __device__ __forceinline__ void dma4(const void* sb, unsigned vo, unsigned la) {
    if constexpr (C) asm volatile("s_mov_b32 m0, %0\n\ts_nop 0\n\tglobal_load_lds_dword %1, %2 sc1" ::"s"(la), "v"(vo), "s"(sb) : "memory");
    else asm volatile("s_mov_b32 m0, %0\n\ts_nop 0\n\tglobal_load_lds_dword %1, %2" ::"s"(la), "v"(vo), "s"(sb) : "memory");
}
__device__ __forceinline__ void swarm8(unsigned sd, const void* p) {
    asm volatile("s_load_dword %0, %1, 0x0\n\ts_load_dword %0, %1, 0x40\n\ts_load_dword %0, %1, 0x80\n\ts_load_dword %0, %1, 0xc0\n\t"
                 "s_load_dword %0, %1, 0x100\n\ts_load_dword %0, %1, 0x140\n\ts_load_dword %0, %1, 0x180\n\ts_load_dword %0, %1, 0x1c0" ::"s"(sd), "s"(p));
}
__device__ __forceinline__ void swarm4(unsigned sd, const void* p) {
    asm volatile("s_load_dword %0, %1, 0x0\n\ts_load_dword %0, %1, 0x40\n\ts_load_dword %0, %1, 0x80\n\ts_load_dword %0, %1, 0xc0" ::"s"(sd), "s"(p));
}
__device__ __forceinline__ void swarm2x2(unsigned sd, const void* p) {
    asm volatile("s_load_dword %0, %1, 0x0\n\ts_load_dword %0, %1, 0x40\n\ts_load_dword %0, %1, 0x800\n\ts_load_dword %0, %1, 0x840" ::"s"(sd), "s"(p));
}
__device__ __forceinline__ void st128(u32x4 v, __amdgpu_buffer_rsrc_t rs, unsigned voff, unsigned base, int) { __builtin_amdgcn_raw_buffer_store_b128(v, rs, voff + base, 0, 16); }
__device__ __forceinline__ void st64(u32x2 v, __amdgpu_buffer_rsrc_t rs, unsigned voff, unsigned base, int) { __builtin_amdgcn_raw_buffer_store_b64(v, rs, voff + base, 0, 16); }
__device__ __forceinline__ float bflo(unsigned w) { return __uint_as_float(w << 16); }
__device__ __forceinline__ float bfhi(unsigned w) { return __uint_as_float(w & 0xffff0000u); }
__device__ __forceinline__ float bf1(u16 b) { return __uint_as_float((unsigned)b << 16); }
__device__ __forceinline__ unsigned pkbf(float lo, float hi) {
    typedef __bf16 bf2_t __attribute__((ext_vector_type(2)));
    bf2_t v = {(__bf16)lo, (__bf16)hi};
    return __builtin_bit_cast(unsigned, v);
}
__device__ __forceinline__ void cfma(float lr, float li, float& zr, float& zi, float ur, float ui) {
    float tr, ti;
    asm("v_fma_f32 %0, -%3, %5, %6\n\tv_fma_f32 %1, %3, %4, %7\n\tv_fma_f32 %0, %2, %4, %0\n\tv_fma_f32 %1, %2, %5, %1"
        : "=&v"(tr), "=&v"(ti) : "v"(lr), "v"(li), "v"(zr), "v"(zi), "v"(ur), "v"(ui));
    zr = tr; zi = ti;
}
__device__ __forceinline__ bool deadline(unsigned long long t0) { return __builtin_amdgcn_s_memrealtime() - t0 > 400000ull; }

struct LaneC { unsigned a0, a1, b0, b1, b2; };
template <int PH>
__device__ __forceinline__ LaneC lane_consts(unsigned base, int wm, int wn, int lane) {
    const int r = lane & 15, q = lane >> 4, rr = r >> 2, p = lane & 3;
    const unsigned rl = r * 128 + ((q ^ (r >> 1)) * 16);
    LaneC c; c.b2 = 0;
    if constexpr (PH == PH_S1) { c.a0 = wm * 8192 + rl; c.a1 = c.a0 ^ 64; c.b0 = 16384 + wn * 8192 + rl; c.b1 = c.b0 ^ 64; }
    if constexpr (PH == PH_M1) {
        c.a0 = wm * 8192 + rl; c.a1 = c.a0 ^ 64;
        const unsigned t0 = 16384 + q * 7 * 512 + rr * 64 + ((q & 1) * 32) + p * 8;
        c.b0 = t0 + wn * 2048;
        c.b1 = (t0 ^ 32) + wn * 2048;
        c.b2 = t0 ^ (wn * 32);
    }
    if constexpr (PH == PH_GS) {
        c.a0 = (q * 8 + wm * 4) * 256 + (p >> 1) * 128 + (4 * (q & 1) + rr) * 16 + (p & 1) * 8; c.a1 = c.a0 ^ 64;
        c.b0 = 16384 + wn * 8192 + rl; c.b1 = c.b0 ^ 64;
    }
    if constexpr (PH == PH_GM) {
        c.a0 = wm * 8192 + lane * 16; c.a1 = c.a0;
        c.b0 = 16384 + q * 4 * 512 + wn * 1024 + rr * 64 + ((q & 1) * 32) + p * 8; c.b1 = c.b0 ^ 32;
    }
    c.a0 += base; c.a1 += base; c.b0 += base; c.b1 += base; c.b2 += base;
    return c;
}
template <int PH> __device__ __forceinline__ void advance(LaneC& c, unsigned d) {
    c.a0 += d; c.b0 += d; c.b1 += d;
    if constexpr (PH != PH_GM) c.a1 += d;
    if constexpr (PH == PH_M1) c.b2 += d;
    if constexpr (PH == PH_M1) asm volatile("" : "+v"(c.a0), "+v"(c.a1), "+v"(c.b0), "+v"(c.b1), "+v"(c.b2));
    else if constexpr (PH == PH_GM) asm volatile("" : "+v"(c.a0), "+v"(c.b0), "+v"(c.b1));
    else asm volatile("" : "+v"(c.a0), "+v"(c.a1), "+v"(c.b0), "+v"(c.b1));
}
template <int PH, int KK, int NB>
__device__ __forceinline__ void load_frags(const LaneC& c, bf16x8 (&A)[4], bf16x8 (&B)[NB]) {
    if constexpr (PH == PH_S1 || PH == PH_M1) {
        ldsp pa = (ldsp)(size_t)(KK ? c.a1 : c.a0);
#pragma unroll
        for (int m = 0; m < 4; ++m) A[m] = lds128(pa + m * 2048);
    } else if constexpr (PH == PH_GS) {
        ldsp p0 = (ldsp)(size_t)c.a0, p1 = (ldsp)(size_t)c.a1;
#pragma unroll
        for (int m = 0; m < 4; ++m) A[m] = cat(ldstr(p0 + (KK * 32 + m) * 256), ldstr(p1 + (KK * 32 + m) * 256));
    } else {
        ldsp pa = (ldsp)(size_t)c.a0;
#pragma unroll
        for (int m = 0; m < 4; ++m) A[m] = lds128(pa + (m * 2 + KK) * 1024);
    }
    if constexpr (PH == PH_S1 || PH == PH_GS) {
        ldsp pb = (ldsp)(size_t)(KK ? c.b1 : c.b0);
#pragma unroll
        for (int n = 0; n < NB; ++n) B[n] = lds128(pb + n * 2048);
    } else {
        constexpr int NS2 = PH == PH_M1 ? 7 : 4;
#pragma unroll
        for (int n = 0; n < NB; ++n) { ldsp p = (ldsp)(size_t)(n == 6 ? c.b2 : (n & 1) ? c.b1 : c.b0) + KK * 4 * NS2 * 512 + (n >> 1) * 512; B[n] = cat(ldstr(p), ldstr(p + 256)); }
    }
}
template <int PH, int NB>
__device__ __forceinline__ void mma(f32x4 (&acc)[NB][4], const bf16x8 (&A)[4], const bf16x8 (&B)[NB]) {
#pragma unroll
    for (int n = 0; n < NB; ++n)
#pragma unroll
        for (int mi = 0; mi < 4; ++mi) {
            const int m = (n & 1) ? 3 - mi : mi;
            if constexpr (PH == PH_S1) acc[n][m] = __builtin_amdgcn_mfma_f32_16x16x32_bf16(A[m], B[n], acc[n][m], 0, 0, 0);
            else acc[n][m] = __builtin_amdgcn_mfma_f32_16x16x32_bf16(B[n], A[m], acc[n][m], 0, 0, 0);
        }
}
template <int PH> constexpr int nreads() { return PH == PH_S1 ? 8 : PH == PH_M1 ? 18 : 12; }
template <int NR, int NM, int NV = 0>
__device__ __forceinline__ void sched_half() {
#pragma unroll
    for (int i = 0; i < NR; ++i) { __builtin_amdgcn_sched_group_barrier(0x8, 1, 0); __builtin_amdgcn_sched_group_barrier(0x100, 1, 0); }
    if constexpr (NV > 0) {
#pragma unroll
        for (int i = 0; i < (NV + 1) / 2; ++i) { __builtin_amdgcn_sched_group_barrier(0x8, 1, 0); __builtin_amdgcn_sched_group_barrier(0x2, 2, 0); }
    }
    __builtin_amdgcn_sched_group_barrier(0x8, NM - NR - (NV + 1) / 2, 0);
}
template <int PH, int NB>
__device__ __forceinline__ void cstep(LaneC& c, unsigned d, f32x4 (&acc)[NB][4], bf16x8 (&A0)[4], bf16x8 (&B0)[NB], bf16x8 (&A1)[4], bf16x8 (&B1)[NB]) {
    load_frags<PH, 1, NB>(c, A1, B1);
    mma<PH, NB>(acc, A0, B0);
    advance<PH>(c, d);
    sched_half<nreads<PH>(), 4 * NB, PH == PH_M1 ? 5 : 4>();
    SCHED0();
    __builtin_amdgcn_s_waitcnt(0xC07F);
    BAR();
    SCHED0();
    load_frags<PH, 0, NB>(c, A0, B0);
    mma<PH, NB>(acc, A1, B1);
    sched_half<nreads<PH>(), 4 * NB>();
}
template <int PC, int PN, bool LAST, int NB>
__device__ __forceinline__ void cstep2(const LaneC& cc, const LaneC& cn, f32x4 (&acc)[NB][4], bf16x8 (&A0)[4], bf16x8 (&B0)[NB], bf16x8 (&A1)[4], bf16x8 (&B1)[NB]) {
    SCHED0();
    load_frags<PC, 1, NB>(cc, A1, B1);
    mma<PC, NB>(acc, A0, B0);
    sched_half<nreads<PC>(), 4 * NB>();
    SCHED0();
    __builtin_amdgcn_s_waitcnt(0xC07F);
    BAR();
    SCHED0();
    if constexpr (!LAST) load_frags<PN, 0, NB>(cn, A0, B0);
    mma<PC, NB>(acc, A1, B1);
    if constexpr (!LAST) sched_half<nreads<PN>(), 4 * NB>();
    SCHED0();
}
__device__ __forceinline__ unsigned ringd(int t) { return ((t + 3) & 3) ? (unsigned)SLOT : (unsigned)(-3 * SLOT); }

__device__ __forceinline__ u64 next_tag(u64 e) { return e * 0x9E3779B97F4A7C15ull + 0xD1B54A32D192ED03ull; }
__device__ __forceinline__ void wait_flags(gu64* fl, u64 v, u64 tag) {
    if (!__all(v == tag)) {
        const unsigned long long t0 = __builtin_amdgcn_s_memrealtime();
        do { __builtin_amdgcn_s_sleep(2); v = __hip_atomic_load(fl, RLX_AGENT); } while (!__all(v == tag) && !deadline(t0));
    }
    asm volatile("" ::: "memory");
}
__device__ __forceinline__ void signal_flag(gu64* fl, u64 tag, int lane) {
    WAITV(0);
    if (lane == 0) __hip_atomic_store(fl, tag, RLX_AGENT);
}
template <int CTRL, int RMASK> __device__ __forceinline__ float dpp_add(float v) {
    return v + __builtin_bit_cast(float, __builtin_amdgcn_update_dpp(0, __builtin_bit_cast(int, v), CTRL, RMASK, 0xf, true));
}
__device__ __forceinline__ float wave_sum(float v) {
    v = dpp_add<0x111, 0xf>(v); v = dpp_add<0x112, 0xf>(v); v = dpp_add<0x114, 0xf>(v); v = dpp_add<0x118, 0xf>(v);
    v = dpp_add<0x142, 0xa>(v); v = dpp_add<0x143, 0xc>(v);
    return __builtin_bit_cast(float, __builtin_amdgcn_readlane(__builtin_bit_cast(int, v), 63));
}


__device__ __forceinline__ unsigned gelu2(f32x2 v) {
    const f32x2 t = (v * v) * -0.1029432397f + -2.3022081986f;
    const f32x2 u = v * t;
    f32x2 d; d.x = __builtin_amdgcn_exp2f(u.x); d.y = __builtin_amdgcn_exp2f(u.y);
    d = d + 1.0f;
    f32x2 r; r.x = __builtin_amdgcn_rcpf(d.x); r.y = __builtin_amdgcn_rcpf(d.y);
    const f32x2 o = v * r;
    return pkbf(o.x, o.y);
}

__device__ __forceinline__ void m1_epilogue(f32x4 (&acc)[7][4], ldsp smem, __amdgpu_buffer_rsrc_t rs, int rb, int cb, int wm, int wn, int lane) {
    f32x2 st[4];
#pragma unroll
    for (int m = 0; m < 4; ++m) st[m] = *(const LAS f32x2*)(smem + LDS_STAT + (wm * 64 + m * 16 + (lane & 15)) * 16);
    u32x2 pk[7][4];
#pragma unroll
    for (int n = 0; n < 7; ++n) {
        const int nbt = n == 6 ? 6 + wn : n + 8 * wn;
        const f32x4 b = *(const LAS f32x4*)(smem + LDS_BC + (nbt * 16 + (lane >> 4) * 4) * 4), cs = *(const LAS f32x4*)(smem + LDS_BC + 1024 + (nbt * 16 + (lane >> 4) * 4) * 4);
#pragma unroll
        for (int m = 0; m < 4; ++m) {
            const f32x4 v = acc[n][m] * st[m].x + (cs * st[m].y + b);
            pk[n][m].x = gelu2((f32x2){v[0], v[1]});
            pk[n][m].y = gelu2((f32x2){v[2], v[3]});
        }
        if ((n & 1) || n == 6) {
            const int pair = 7 * cb + (nbt >> 1);
            const unsigned base = WS_A2 + (unsigned)(rb * 88 + 32 + (pair >> 1)) * 16384u + (unsigned)(pair & 1) * 1024u + (unsigned)(wm * 4) * 2048u;
#pragma unroll
            for (int m = 0; m < 4; ++m) {
                if (n == 6) st64(pk[n][m], rs, lane * 16 + m * 2048, base + wn * 8, 16);
                else { const u32x4 v = {pk[n - (n & 1)][m].x, pk[n - (n & 1)][m].y, pk[n][m].x, pk[n][m].y}; st128(v, rs, lane * 16 + m * 2048, base, 16); }
            }
        }
    }
}

__device__ __forceinline__ void colsum_half(const bf16x8 (&B)[7], f32x4& cs, int lane) {
    int r = lane & 15;
    SCHED0();
    asm volatile("" : "+v"(r));
#pragma unroll
    for (int n = 0; n < 7; ++n) {
        const unsigned o = r == n ? 0x3F803F80u : 0u;
        cs = __builtin_amdgcn_mfma_f32_16x16x32_bf16(B[n], __builtin_bit_cast(bf16x8, (u32x4){o, o, o, o}), cs, 0, 0, 0);
    }
}

__device__ __forceinline__ void check_flags(u32x4 v, const char* ws, unsigned off, u64 tag) {
    const unsigned lo = (unsigned)tag, hi = (unsigned)(tag >> 32);
    if (!__all((((v[0] ^ lo) | (v[1] ^ hi)) | ((v[2] ^ lo) | (v[3] ^ hi))) == 0u)) {
        const unsigned long long t0 = __builtin_amdgcn_s_memrealtime();
        bool ok;
        do { __builtin_amdgcn_s_sleep(2);
             ok = __hip_atomic_load((gu64*)(ws + off), RLX_AGENT) == tag && __hip_atomic_load((gu64*)(ws + off + 8), RLX_AGENT) == tag;
        } while (!__all(ok) && !deadline(t0));
    }
    asm volatile("" ::: "memory");
}

__global__ __launch_bounds__(512, 2) void fused_kernel(Args a) {
    extern __shared__ __attribute__((aligned(16))) char smem_[];
    ldsp smem = (ldsp)smem_;
    const int wid = __builtin_amdgcn_readfirstlane(threadIdx.x >> 6), lane = threadIdx.x & 63;
    const int bid = blockIdx.x, xcd = bid & 7, rk = bid >> 3;
    const int kh = xcd & 1, rb = (xcd >> 1) * 4 + (rk >> 3), c8 = rk & 7, cb = kh * 8 + c8;
    char* const ws = a.ws;
    const char* pc; unsigned code_len;
    asm volatile("s_getpc_b64 %0\n.Lgm_pc%=:\n\ts_mov_b32 %1, gm_code_end-.Lgm_pc%=" : "=s"(pc), "=s"(code_len));
    if (wid == 3) {
        const unsigned o = (unsigned)(rk + 32 * (lane & 15)) * 128u;
        dma4<false>(pc, o < code_len && code_len < (1u << 20) ? o : 0u, (unsigned)(size_t)smem + LDS_DUMMY);
    }
    asm volatile("" ::"s"(a.x), "s"(a.lre), "s"(a.lim), "s"(a.bre), "s"(a.bim), "s"(a.cre), "s"(a.cim), "s"(a.ldt), "s"(a.dsk), "s"(a.w1), "s"(a.b1), "s"(a.w2), "s"(a.b2), "s"(a.gw), "s"(a.out), "s"(a.ws));

    u32x4 xv[2][2], gv[2]; u64 ep = 0;
    if (wid < 4) {
        const int lnrow = 512 * (xcd >> 1) + 256 * kh + 8 * rk + 2 * wid;
#pragma unroll
        for (int i = 0; i < 2; ++i)
#pragma unroll
            for (int h = 0; h < 2; ++h) xv[i][h] = *(const u32x4*)(a.x + (size_t)(lnrow + i) * 1024 + h * 512 + lane * 8);
#pragma unroll
        for (int h = 0; h < 2; ++h) gv[h] = *(const u32x4*)(a.gw + h * 512 + lane * 8);
        ep = __hip_atomic_load((gu64*)(ws + WS_EPOCH), RLX_AGENT);
    }
    int wid2 = wid;
    asm volatile("" : "+s"(wid2)::"memory");
    if (__builtin_expect(wid2 >= 4, 1)) {
        __builtin_amdgcn_s_waitcnt(0x0F70);
        const int w = wid - 4, un = (xcd >> 1) * 32 + rk;
        const unsigned lb = (unsigned)(size_t)smem;
        const unsigned swz = (unsigned)(((lane & 7) ^ ((4 * (w & 1) + (lane >> 4)) & 7)) * 16);
        const unsigned vl = lane * 16;
        unsigned sd;
        asm volatile("s_mov_b32 %0, 0" : "=s"(sd));
        const LAS u32x4* const parea = (const LAS u32x4*)(smem + LDS_E + w * 1024 + lane * 16);
        const char* const pw2 = (const char*)a.w2 + (size_t)(1792 * kh + un) * 2048 + w * 512;
        constexpr int W2C = 128 * 2048;
        const __amdgpu_buffer_rsrc_t rs = __builtin_amdgcn_make_buffer_rsrc(ws, 0, WS_END, 0x00020000);
        u64 tag;
        f32x2 l16, wt[4];
        const unsigned vsa = (unsigned)((rb * 128 + 16 * (lane >> 5) + ((lane >> 3) & 3) + 32 * (w & 1) + 4 * (w >> 1)) * 2048) + swz;
        const unsigned vsb = (unsigned)((cb * 64 + 16 * (w >> 1) + 8 * (w & 1) + (lane >> 3)) * 2048) + swz;
        auto none = [] {};
        auto issue_s = [&](int t, auto mid) {
            const unsigned s = lb + ((t + 2) & 3) * SLOT + w * 1024;
            const int ko = ((t + rb + cb) & 15) * 128;
#pragma unroll
            for (int n = 0; n < 4; ++n) dma16<false>((const char*)a.x + ko + (64 * (n >> 1) + 8 * (n & 1)) * 2048, vsa, s + n * 4096);
            mid();
#pragma unroll
            for (int n = 0; n < 4; ++n) dma16<false>((const char*)((n & 1) ? a.bim : a.bre) + ko + (n >> 1) * 32 * 2048, vsb, s + 16384 + n * 4096);
        };
        {
            const unsigned va = (unsigned)((rb * 128 + 8 * w + (lane >> 3)) * 2048) + swz;
            unsigned vb[7];
#pragma unroll
            for (int i = 0; i < 7; ++i) {
                const int sl = 2 * i + (lane >> 5), kq = 2 * w + (sl >= 7 ? 1 : 0), nb2 = sl >= 7 ? sl - 7 : sl;
                vb[i] = (unsigned)((8 * kq + ((lane >> 2) & 7)) * 7168 + cb * 448 + nb2 * 64 + (((lane & 3) ^ (2 * (kq & 1))) * 16));
            }
            int slot = 0;
            auto issue = [&](int t, auto mid) {
                const unsigned s = lb + slot * SLOTM;
                slot = slot == 2 ? 0 : slot + 1;
                int ks;
                if (rb >= TSP) { const int u = rb + t - TSP; ks = t < TSP ? t : u >= 16 ? u - 16 + TSP : u; }
                else ks = t > TSP ? t : t == TSP ? rb : t < rb ? t : t + 1;
                ks = __builtin_amdgcn_readfirstlane(ks);
                const char* pb = (const char*)a.w1 + (size_t)ks * 64 * 7168;
#pragma unroll
                for (int i = 0; i < 7; ++i) dma16<false>(pb, vb[i], s + 16384 + (w * 7 + i) * 1024);
                mid();
#pragma unroll
                for (int n = 0; n < 4; ++n) dma16<false>((const char*)a.x + ks * 128 + n * 32 * 2048, va, s + w * 1024 + n * 4096);
            };
            issue(0, none);
            { const char* sp = w == 0 ? (const char*)a.b1 + cb * 448 : w == 1 ? (const char*)a.lre + cb * 128 : w == 2 ? (const char*)a.lim + cb * 128 : (const char*)a.ldt + cb * 128;
              if (lane < (w == 0 ? 28 : 8)) dma16<false>(sp, vl, lb + LDS_SM + (w == 0 ? 0 : 384 + w * 128)); }
            issue(1, none);
            WAITV(12); BAR();
            issue(2, none);
            { const u32x2 tv = *(const LAS u32x2*)(smem + LDS_TAG);
              tag = ((u64)(unsigned)__builtin_amdgcn_readfirstlane(tv.y) << 32) | (unsigned)__builtin_amdgcn_readfirstlane(tv.x); }
            const char* const pbw = (const char*)((w & 1) ? a.bim : a.bre) + (size_t)(512 * kh + 4 * un + 2 * (w >> 1)) * 2048;
            const unsigned fo = (lane & 8) ? WS_FC + cb * 128 + (lane & 7) * 16 : WS_FY + rb * 128 + (lane & 7) * 16;
            const int lnrow = 512 * (xcd >> 1) + 256 * kh + 8 * rk;
_Pragma("unroll 1")
            for (int t = 0; t < 13; ++t) {
                WAITV(11); BAR();
                issue(t + 3, [&] { if (t >= 4 && t != 12) swarm8(sd, pbw + (t - 4) * 512); });
                if (t == TSP + 1 && w == 0) {
                    const u32x4 v = *(const LAS u32x4*)(smem + LDS_BC + lane * 16);
                    st128(v, rs, lane < 8 ? WS_STAT + (unsigned)(lnrow + lane) * 16u : WS_CSP + (unsigned)(cb * 16 + rb) * 896u + (lane - 8) * 16, 0, 16);
                }
                if (t == TSP + 3 && w == 0) __hip_atomic_store((gu64*)(ws + ((lane & 1) ? WS_FC + (cb * 16 + rb) * 8 : WS_FY + ((lnrow >> 7) * 16 + (rk & 15)) * 8)), tag, RLX_AGENT);
                if (t == 11) dma16<true>(ws, fo, lb + LDS_E + w * 1024);
            }
            WAITV(11); BAR();
            check_flags(*parea, ws, fo, tag);
#pragma unroll
            for (int n = 0; n < 3; ++n) dma16<true>(ws + WS_CSP + cb * 14336 + (w + 4 * n) * 1024, vl, lb + LDS_GATH + (w + 4 * n) * 1024);
            if (w < 2) dma16<true>(ws + WS_CSP + cb * 14336 + (w + 12) * 1024, vl, lb + LDS_GATH + (w + 12) * 1024);
            else dma16<true>(ws + WS_STAT + rb * 2048 + (w - 2) * 1024, vl, lb + LDS_STAT + (w - 2) * 1024);
            WAITV(0); BAR();
            {
                const int col = w * 56 + (lane < 56 ? lane : 0);
                float s = 0.f;
#pragma unroll
                for (int i = 0; i < 16; ++i) s += *(const LAS float*)(smem + LDS_GATH + i * 896 + col * 4);
                const float gg = *(const LAS float*)(smem + LDS_STAT + (w * 32 + (lane & 31)) * 16 + 8);
                const float b1v = bf1(*(const LAS u16*)(smem + LDS_SM + col * 2));
                *(LAS float*)(smem + LDS_BC + 1024 + col * 4) = s;
                *(LAS float*)(smem + LDS_BC + col * 4) = b1v;
                *(LAS float*)(smem + LDS_G2 + (w * 32 + (lane & 31)) * 4) = 2.0f * gg;
            }
            issue_s(0, none);
            __builtin_amdgcn_s_waitcnt(0xC07F);
            BAR();
        }
        const char* const a2 = ws + WS_A2 + (size_t)rb * 88 * 16384 + w * 4096;
        const unsigned vr = (unsigned)((c8 * 128 + 8 * w + (lane >> 3)) * 2048) + swz;
        unsigned vt[2];
        { const int rho = (lane >> 2) & 7;
#pragma unroll
          for (int qq = 0; qq < 2; ++qq) { const int q = 2 * (w & 1) + qq; const int k = 32 * (w >> 1) + (rho < 4 ? 4 * q + rho : 16 + 4 * q + rho - 4);
              vt[qq] = (unsigned)(k * 2048 + c8 * 256 + (lane >> 5) * 64 + (((lane & 3) ^ (2 * qq)) * 16)); } }
        auto issue_gm = [&](int t, auto mid) {
            const unsigned s = lb + ((t + 2) & 3) * SLOT;
            const int km = kh * 28 + t;
            dma64c(a2 + (size_t)(32 + km) * 16384, vl, s + w * 4096);
            mid();
            const char* pb = (const char*)a.w2 + (size_t)km * 64 * 2048;
#pragma unroll
            for (int n = 0; n < 4; ++n) dma16<false>(pb + (n & 1) * 128, vt[n >> 1], s + 16384 + (w * 4 + n) * 1024);
        };
        auto issue_gs = [&](int t, auto mid) {
            const unsigned s = lb + ((t + 2) & 3) * SLOT;
            const int ks = kh * 16 + (t - 28);
            dma64c(a2 + (size_t)ks * 16384, vl, s + w * 4096);
            mid();
            const char* pb = (const char*)((ks & 1) ? a.cim : a.cre) + (ks >> 1) * 128;
#pragma unroll
            for (int n = 0; n < 4; ++n) dma16<false>(pb + n * 32 * 2048, vr, s + 16384 + (w + 4 * n) * 1024);
        };
        {
_Pragma("unroll 1")
            for (int t = 1; t < 4; ++t) issue_s(t, none);
            {
                const float lr = bf1(*(const LAS u16*)(smem + LDS_SM + 512 + lane * 2)), li = bf1(*(const LAS u16*)(smem + LDS_SM + 640 + lane * 2));
                const float dt = (float)(__bf16)expf(bf1(*(const LAS u16*)(smem + LDS_SM + 768 + lane * 2)));
                const float zr = dt * lr, zi = dt * li;
                float sn, cs; sincosf(zi, &sn, &cs);
                const float er = expf(zr), Lr = er * cs, Li = er * sn;
                float pr = Lr, pi = Li;
#pragma unroll
                for (int i = 0; i < 4; ++i) cfma(pr, pi, pr, pi, 0.f, 0.f);
                l16 = (f32x2){pr, pi};
                if (w == 0) {
                    const float den = 1.0f / (lr * lr + li * li), nr = Lr - 1.0f;
                    *(LAS f32x4*)(smem + LDS_COEF + lane * 16) = (f32x4){Lr, Li, (nr * lr + Li * li) * den, (Li * lr - nr * li) * den};
                }
                if (w == 2) {
                    float qr = 1.0f, qi = 0.f;
#pragma unroll
                    for (int i = 0; i < 8; ++i) { *(LAS f32x2*)(smem + LDS_PSEG + (i * 64 + lane) * 8) = (f32x2){qr, qi}; cfma(pr, pi, qr, qi, 0.f, 0.f); }
                }
#pragma unroll
                for (int i = 0; i < 3; ++i) cfma(pr, pi, pr, pi, 0.f, 0.f);
                float p2r = pr, p2i = pi; cfma(pr, pi, p2r, p2i, 0.f, 0.f);
                float p4r = p2r, p4i = p2i; cfma(p2r, p2i, p4r, p4i, 0.f, 0.f);
                const int e0 = (rb - 1 - w) & 3, top = (rb - 1 - w) >> 2;
                float qr = (e0 & 1) ? pr : 1.0f, qi = (e0 & 1) ? pi : 0.f;
                if (e0 & 2) cfma(p2r, p2i, qr, qi, 0.f, 0.f);
#pragma unroll
                for (int j = 3; j >= 0; --j) {
                    wt[j] = j <= top ? (f32x2){qr, qi} : (f32x2){0.f, 0.f};
                    if (j <= top) cfma(p4r, p4i, qr, qi, 0.f, 0.f);
                }
                asm volatile("" : "+v"(wt[0]), "+v"(wt[1]), "+v"(wt[2]), "+v"(wt[3]), "+v"(l16));
                __builtin_amdgcn_s_waitcnt(0xC07F);
            }
            WAITV(24); BAR();
            const unsigned fo = WS_FM + (rb * 2 + kh) * 256 + (lane & 15) * 16;
_Pragma("unroll 1")
            for (int t = 0; t < 10; ++t) { WAITV(16); BAR(); issue_s(t + 4, [&] { swarm4(sd, pw2 + (t >> 1) * W2C + (t & 1) * 256); }); }
            WAITV(16); BAR(); issue_s(14, [&] { dma16<true>(ws, fo, lb + LDS_E + w * 1024); });
            WAITV(17); BAR(); issue_s(15, none);
            WAITV(8); BAR();
            check_flags(*parea, ws, fo, tag);
            swarm8(sd, pw2 + 5 * W2C);
            BAR();
            issue_gm(0, [&] { swarm4(sd, pw2 + 6 * W2C); });
            WAITV(8); BAR();
            issue_gm(1, [&] { swarm4(sd, pw2 + 6 * W2C + 256); });
            BAR();
            issue_gm(2, none); issue_gm(3, none);
        }
        {
            const unsigned fo = WS_FS + (rb * 2 + kh) * 256 + (lane & 15) * 16;
            const char* const pcr = (const char*)a.cre + (8 * un + 2 * w) * 2048 + kh * 1024;
            const char* const pci = (const char*)a.cim + (8 * un + 2 * w) * 2048 + kh * 1024;
            const unsigned tag_lo = (unsigned)tag, tag_hi = (unsigned)(tag >> 32);
            WAITV(24); BAR();
            if (w == 1) {
                f32x2 e[8];
#pragma unroll
                for (int i = 0; i < 8; ++i) e[i] = *(const LAS f32x2*)(smem + LDS_E + (i * 64 + lane) * 8);
                LDS_FENCE();
                float er = 0.f, ei = 0.f;
#pragma unroll
                for (int i = 0; i < 8; ++i) {
                    *(LAS f32x2*)(smem + LDS_E + (i * 64 + lane) * 8) = (f32x2){er, ei};
                    cfma(l16.x, l16.y, er, ei, e[i].x, e[i].y);
                }
                st128((u32x4){__float_as_uint(er), tag_lo, __float_as_uint(ei), tag_hi}, rs, vl, WS_GRAN + (unsigned)(cb * 16 + rb) * 1024u, 16);
                __builtin_amdgcn_s_waitcnt(0xC07F);
            }
_Pragma("unroll 1")
            for (int t = 0; t < 8; ++t) { WAITV(16); BAR(); issue_gm(t + 4, [&] { swarm4(sd, pw2 + (7 + (t >> 1)) * W2C + (t & 1) * 256); }); }
#pragma unroll
            for (int j = 0; j < 4; ++j)
                if (w + 4 * j < rb) dma16<true>(ws + WS_GRAN + cb * 16384 + (w + 4 * j) * 1024, vl, lb + LDS_GATH + (w * 4 + j) * 1024);
            WAITV(16); BAR(); issue_gm(12, [&] { swarm8(sd, pw2 + 11 * W2C); });
            WAITV(16); BAR(); issue_gm(13, none);
            WAITV(16); BAR();
            {
                u32x4 g[4];
                unsigned bad = 0;
#pragma unroll
                for (int j = 0; j < 4; ++j) g[j] = *(const LAS u32x4*)(smem + LDS_GATH + (w * 4 + j) * 1024 + lane * 16);
#pragma unroll
                for (int j = 0; j < 4; ++j) bad |= w + 4 * j < rb ? (g[j][1] ^ tag_lo) | (g[j][3] ^ tag_hi) : 0u;
                if (!__all(bad == 0u)) {
                    const unsigned long long t0 = __builtin_amdgcn_s_memrealtime();
                    do {
                        __builtin_amdgcn_s_sleep(1);
                        bad = 0;
                        asm volatile("" ::: "memory");
#pragma unroll
                        for (int j = 0; j < 4; ++j) {
                            g[j] = __builtin_amdgcn_raw_buffer_load_b128(rs, vl + (w + 4 * j) * 1024, WS_GRAN + (unsigned)cb * 16384u, 16);
                            bad |= w + 4 * j < rb ? (g[j][1] ^ tag_lo) | (g[j][3] ^ tag_hi) : 0u;
                        }
                    } while (!__all(bad == 0u) && !deadline(t0));
                }
                WAITV(8); BAR();
                float sr = 0.f, si = 0.f;
#pragma unroll
                for (int j = 0; j < 4; ++j) {
                    float vr_ = w + 4 * j < rb ? __uint_as_float(g[j][0]) : 0.f, vi_ = w + 4 * j < rb ? __uint_as_float(g[j][2]) : 0.f;
                    cfma(wt[j].x, wt[j].y, vr_, vi_, sr, si); sr = vr_; si = vi_;
                }
                *(LAS f32x2*)(smem + LDS_BC + lane * 32 + w * 8) = (f32x2){sr, si};
                __builtin_amdgcn_s_waitcnt(0xC07F);
            }
            WAITV(0); BAR();
_Pragma("unroll 1")
            for (int t = 14; t < 17; ++t) issue_gm(t, [&] { if (t > 14) swarm8(sd, pw2 + (t - 3) * W2C); });
_Pragma("unroll 1")
            for (int t = 13; t < 21; ++t) { WAITV(16); BAR(); issue_gm(t + 4, [&] { swarm2x2(sd, ((t & 1) ? pcr : pci) + ((t - 13) >> 1) * 128); }); }
            dma16<true>(ws, fo, lb + LDS_E + w * 1024);
            WAITV(16); BAR(); issue_gm(25, [&] { swarm2x2(sd, pcr + 512); });
            WAITV(16); BAR(); issue_gm(26, none);
            { u32x4 fv;
              WAITV(16); BAR(); issue_gm(27, [&] { fv = *parea; });
              check_flags(fv, ws, fo, tag); }
_Pragma("unroll 1")
            for (int t = 24; t < 31; ++t) { WAITV(16); BAR(); issue_gs(t + 4, [&] { swarm2x2(sd, ((t & 1) ? pcr : pci) + ((t - 15) >> 1) * 128); }); }
_Pragma("unroll 1")
            for (int t = 31; t < 40; ++t) { WAITV(16); BAR(); issue_gs(t + 4, none); }
            WAITV(16); BAR();
            {
                const int rl = 16 * w + (lane >> 4);
#pragma unroll
                for (int n = 0; n < 4; ++n) {
                    const int rr = rl + 4 * n;
                    dma16<false>((const char*)a.x + (size_t)(rb * 128 + kh * 32) * 2048 + c8 * 256, (unsigned)((64 * (rr >> 5) + (rr & 31)) * 2048 + (((lane & 15) ^ (rr & 15)) * 16)), lb + 2 * SLOT + (w * 4 + n) * 1024);
                }
                dma4<false>((const char*)((w & 1) ? a.b2 : a.dsk) + c8 * 256, (unsigned)lane * 4u, lb + 2 * SLOT + 16384 + w * 256);
            }
            WAITV(13); BAR();
            WAITV(5); BAR();
            WAITV(0); BAR();
            asm volatile("s_waitcnt lgkmcnt(0)" ::"s"(sd));
        }
        if (bid == 0 && w == 0) {
            const unsigned long long t0 = __builtin_amdgcn_s_memrealtime();
            for (;;) {
                bool ok = true;
#pragma unroll
                for (int i = 0; i < 4; ++i) ok &= __hip_atomic_load((gu64*)(ws + WS_FY) + i * 64 + lane, RLX_AGENT) == tag;
                if (__all(ok) || deadline(t0)) break;
                __builtin_amdgcn_s_sleep(2);
            }
            if (lane == 0) __hip_atomic_store((gu64*)(ws + WS_EPOCH), tag, RLX_AGENT);
        }
        return;
    }

    const int wm = wid >> 1, wn = wid & 1;
    const unsigned lb = (unsigned)(size_t)smem;
    const __amdgpu_buffer_rsrc_t rs = __builtin_amdgcn_make_buffer_rsrc(ws, 0, WS_END, 0x00020000);
    {
        if (wid == 0) {
            const u64 t = next_tag(ep);
            if (lane == 0) *(LAS u32x2*)(smem + LDS_TAG) = (u32x2){(unsigned)t, (unsigned)(t >> 32)};
        }
        f32x2 xf[2][8];
        float s[2], gd[2], vs[2];
#pragma unroll
        for (int i = 0; i < 2; ++i) {
            f32x2 s2 = {0.f, 0.f}, g2 = {0.f, 0.f};
#pragma unroll
            for (int h = 0; h < 2; ++h)
#pragma unroll
                for (int j = 0; j < 4; ++j) {
                    const f32x2 v = {bflo(xv[i][h][j]), bfhi(xv[i][h][j])}, gw = {bflo(gv[h][j]), bfhi(gv[h][j])};
                    xf[i][h * 4 + j] = v; s2 += v; g2 += v * gw;
                }
            s[i] = wave_sum(s2.x + s2.y) * (1.0f / 1024.0f); gd[i] = wave_sum(g2.x + g2.y);
        }
#pragma unroll
        for (int i = 0; i < 2; ++i) {
            f32x2 v2 = {0.f, 0.f};
#pragma unroll
            for (int j = 0; j < 8; ++j) { const f32x2 d = xf[i][j] - s[i]; v2 += d * d; }
            vs[i] = wave_sum(v2.x + v2.y);
        }
        const float gl = lane ? gd[1] : gd[0], ml = lane ? s[1] : s[0], vl = lane ? vs[1] : vs[0];
        const float rstd = 1.0f / sqrtf(vl * (1.0f / 1024.0f) + 1e-5f), g = 1.0f / (1.0f + expf(-gl));
        if (lane < 2) *(LAS f32x4*)(smem + LDS_BC + (wid * 2 + lane) * 16) = (f32x4){rstd, -rstd * ml, g, 0.f};
    }
    bf16x8 A0[4], A1[4];
    {
        f32x4 acc[7][4];
        bf16x8 B0[7], B1[7];
        LaneC c = lane_consts<PH_M1>(lb, wm, wn, lane);
        __builtin_amdgcn_s_waitcnt(0xC07F);
        BAR();
        load_frags<PH_M1, 0, 7>(c, A0, B0);
#pragma unroll
        for (int n = 0; n < 7; ++n)
#pragma unroll
            for (int m = 0; m < 4; ++m) acc[n][m] = (f32x4){0.f, 0.f, 0.f, 0.f};
        SCHED0();
_Pragma("unroll 1")
        for (int t = 0; t < TSP; ++t) cstep<PH_M1, 7>(c, SLOTM, acc, A0, B0, A1, B1);
        SCHED0();
        { ONCE() {
            f32x4 cs = {0.f, 0.f, 0.f, 0.f};
            load_frags<PH_M1, 1, 7>(c, A1, B1);
            mma<PH_M1, 7>(acc, A0, B0);
            advance<PH_M1>(c, TSP == 2 ? (unsigned)(-2 * SLOTM) : (unsigned)SLOTM);
            sched_half<18, 28, 5>();
            colsum_half(B0, cs, lane);
            SCHED0();
            __builtin_amdgcn_s_waitcnt(0xC07F);
            BAR();
            SCHED0();
            load_frags<PH_M1, 0, 7>(c, A0, B0);
            mma<PH_M1, 7>(acc, A1, B1);
            sched_half<18, 28>();
            colsum_half(B1, cs, lane);
            SCHED0();
            const int r = lane & 15, nbt = r == 6 ? 6 + wn : r + 8 * wn;
            if (r < 7) *(LAS f32x4*)(smem + LDS_BC + 128 + (nbt * 16 + (lane >> 4) * 4) * 4) = cs;
            SCHED0();
        } }
        int sc = (TSP + 1) % 3;
_Pragma("unroll 1")
        for (int t = TSP + 1; t < 15; ++t) { cstep<PH_M1, 7>(c, sc == 2 ? (unsigned)(-2 * SLOTM) : (unsigned)SLOTM, acc, A0, B0, A1, B1); sc = sc == 2 ? 0 : sc + 1; }
        { ONCE() cstep2<PH_M1, PH_M1, true, 7>(c, c, acc, A0, B0, A1, B1); }
        m1_epilogue(acc, smem, rs, rb, cb, wm, wn, lane);
    }
    u64 tag;
    { const u32x2 tv = *(const LAS u32x2*)(smem + LDS_TAG);
      tag = ((u64)(unsigned)__builtin_amdgcn_readfirstlane(tv.y) << 32) | (unsigned)__builtin_amdgcn_readfirstlane(tv.x); }
    f32x4 sv[4][4];
    bf16x8 B0[4], B1[4];
    const int ch = lane & 15, q = lane >> 4, seg = wm * 4 + q;
    {
#pragma unroll
        for (int n = 0; n < 4; ++n)
#pragma unroll
            for (int m = 0; m < 4; ++m) sv[n][m] = (f32x4){0.f, 0.f, 0.f, 0.f};
        LaneC c = lane_consts<PH_S1>(lb + 2 * SLOT, wm, wn, lane);
        BAR();
        load_frags<PH_S1, 0, 4>(c, A0, B0);
        SCHED0();
#define S1STEP(t) cstep<PH_S1, 4>(c, ringd(t), sv, A0, B0, A1, B1)
_Pragma("unroll 1")
        for (int t = 0; t < 4; ++t) S1STEP(t);
        signal_flag((gu64*)(ws + WS_FM) + (rb * 2 + kh) * 32 + c8 * 4 + wid, tag, lane);
_Pragma("unroll 1")
        for (int t = 4; t < 15; ++t) S1STEP(t);
        f32x2 lam[2];
#pragma unroll
        for (int cbk = 0; cbk < 2; ++cbk) lam[cbk] = *(const LAS f32x2*)(smem + LDS_COEF + (wn * 32 + cbk * 16 + ch) * 16);
        SCHED0();
        { ONCE() cstep2<PH_S1, PH_S1, true, 4>(c, c, sv, A0, B0, A1, B1); }
        asm volatile("s_nop 15");
        SCHED0();
        float er[2], ei[2];
#pragma unroll
        for (int cbk = 0; cbk < 2; ++cbk) { er[cbk] = sv[cbk][0][0]; ei[cbk] = sv[2 + cbk][0][0]; }
#pragma unroll
        for (int i = 1; i < 16; ++i)
#pragma unroll
            for (int cbk = 0; cbk < 2; ++cbk) cfma(lam[cbk].x, lam[cbk].y, er[cbk], ei[cbk], sv[cbk][i >> 2][i & 3], sv[2 + cbk][i >> 2][i & 3]);
#pragma unroll
        for (int cbk = 0; cbk < 2; ++cbk) *(LAS f32x2*)(smem + LDS_E + (seg * 64 + wn * 32 + cbk * 16 + ch) * 8) = (f32x2){er[cbk], ei[cbk]};
    }
    {
        f32x4 acc[4][4];
#pragma unroll
        for (int n = 0; n < 4; ++n)
#pragma unroll
            for (int m = 0; m < 4; ++m) acc[n][m] = (f32x4){0.f, 0.f, 0.f, 0.f};
        LaneC cm = lane_consts<PH_GM>(lb + 2 * SLOT, wm, wn, lane);
        __builtin_amdgcn_s_waitcnt(0xC07F);
        BAR();
        load_frags<PH_GM, 0, 4>(cm, A0, B0);
        SCHED0();
#define GMSTEP(t) cstep<PH_GM, 4>(cm, ringd(t), acc, A0, B0, A1, B1)
_Pragma("unroll 1")
        for (int t = 0; t < 13; ++t) GMSTEP(t);
        {
            f32x4 g2[4], k0[2]; float zr[2], zi[2];
#pragma unroll
            for (int cbk = 0; cbk < 2; ++cbk) {
                const int cl = wn * 32 + cbk * 16 + ch;
                const f32x2 c0 = *(const LAS f32x2*)(smem + LDS_E + (seg * 64 + cl) * 8), ps = *(const LAS f32x2*)(smem + LDS_PSEG + (seg * 64 + cl) * 8);
                const f32x4 sp = *(const LAS f32x4*)(smem + LDS_BC + cl * 32) + *(const LAS f32x4*)(smem + LDS_BC + cl * 32 + 16);
                zr[cbk] = sp[0] + sp[2]; zi[cbk] = sp[1] + sp[3];
                cfma(ps.x, ps.y, zr[cbk], zi[cbk], c0.x, c0.y);
                k0[cbk] = *(const LAS f32x4*)(smem + LDS_COEF + (wn * 32 + cbk * 16 + ch) * 16);
            }
#pragma unroll
            for (int m = 0; m < 4; ++m) g2[m] = *(const LAS f32x4*)(smem + LDS_G2 + (seg * 16 + m * 4) * 4);
            const unsigned so = WS_A2 + (unsigned)(rb * 88 + 2 * cb) * 16384u;
#pragma unroll
            for (int th = 0; th < 2; ++th) {
#pragma unroll
                for (int i = th * 8; i < th * 8 + 8; ++i)
#pragma unroll
                    for (int cbk = 0; cbk < 2; ++cbk) {
                        cfma(k0[cbk][0], k0[cbk][1], zr[cbk], zi[cbk], sv[cbk][i >> 2][i & 3], sv[2 + cbk][i >> 2][i & 3]);
                        sv[cbk][i >> 2][i & 3] = zr[cbk]; sv[2 + cbk][i >> 2][i & 3] = zi[cbk];
                    }
#pragma unroll
                for (int cbk = 0; cbk < 2; ++cbk) {
                    const int kb = 4 * wn + 2 * cbk + (ch >> 3);
                    const unsigned vo = (unsigned)((kb * 8 + seg) * 256 + (((ch & 7) ^ (4 * (kb & 1))) * 16));
                    u32x4 vr, vi;
#pragma unroll
                    for (int mh = 0; mh < 2; ++mh) {
                        const f32x4 re = sv[cbk][th * 2 + mh], im = sv[2 + cbk][th * 2 + mh];
                        const f32x4 hr4 = (re * k0[cbk][2] - im * k0[cbk][3]) * g2[th * 2 + mh], hi4 = (im * -k0[cbk][2] - re * k0[cbk][3]) * g2[th * 2 + mh];
                        vr[mh * 2] = pkbf(hr4[0], hr4[1]); vr[mh * 2 + 1] = pkbf(hr4[2], hr4[3]);
                        vi[mh * 2] = pkbf(hi4[0], hi4[1]); vi[mh * 2 + 1] = pkbf(hi4[2], hi4[3]);
                    }
                    st128(vr, rs, vo + th * 128, so, 16);
                    st128(vi, rs, vo + th * 128, so + 16384u, 16);
                }
            }
            SCHED0();
        }
_Pragma("unroll 1")
        for (int t = 13; t < 16; ++t) GMSTEP(t);
        signal_flag((gu64*)(ws + WS_FS) + (rb * 2 + kh) * 32 + c8 * 4 + wid, tag, lane);
_Pragma("unroll 1")
        for (int t = 16; t < 27; ++t) GMSTEP(t);
        LaneC cs = lane_consts<PH_GS>(lb + 2 * SLOT, wm, wn, lane);
        float og[4];
#pragma unroll
        for (int m = 0; m < 4; ++m) og[m] = 1.0f - *(const LAS float*)(smem + LDS_STAT + (wm * 64 + m * 16 + ch) * 16 + 8);
        SCHED0();
        { ONCE() cstep2<PH_GM, PH_GS, false, 4>(cm, cs, acc, A0, B0, A1, B1); }
#pragma unroll
        for (int n = 0; n < 4; ++n)
#pragma unroll
            for (int m = 0; m < 4; ++m) acc[n][m] *= og[m];
        SCHED0();
_Pragma("unroll 1")
        for (int t = 28; t < 43; ++t) cstep<PH_GS, 4>(cs, ringd(t), acc, A0, B0, A1, B1);
        { ONCE() cstep2<PH_GS, PH_GS, true, 4>(cs, cs, acc, A0, B0, A1, B1); }
        const int r = ch;
        if (kh) {
#pragma unroll
            for (int n = 0; n < 4; ++n)
#pragma unroll
                for (int mm = 0; mm < 2; ++mm) { const f32x4 t = acc[n][mm]; acc[n][mm] = acc[n][2 + mm]; acc[n][2 + mm] = t; }
        }
        const unsigned xo = WS_XB + (unsigned)(bid * 4 + wid) * 8192u, xi = WS_XB + (unsigned)((bid ^ 1) * 4 + wid) * 8192u;
#pragma unroll
        for (int n = 0; n < 4; ++n)
            st128((u32x4){pkbf(acc[n][2][0], acc[n][2][1]), pkbf(acc[n][2][2], acc[n][2][3]), pkbf(acc[n][3][0], acc[n][3][1]), pkbf(acc[n][3][2], acc[n][3][3])}, rs, lane * 16 + n * 1024, xo, 16);
        const __amdgpu_buffer_rsrc_t ro = __builtin_amdgcn_make_buffer_rsrc(a.out, 0, 8u << 20, 0x00020000);
        f32x4 o[4][2];
#pragma unroll
        for (int mm = 0; mm < 2; ++mm) {
            if (mm == 1) signal_flag((gu64*)(ws + WS_XFLAG) + (bid * 4 + wid) * 8, tag, lane);
            const float g = *(const LAS float*)(smem + LDS_STAT + (wm * 64 + kh * 32 + mm * 16 + r) * 16 + 8);
#pragma unroll
            for (int n = 0; n < 4; ++n) {
                const u32x2 xw = *(const LAS u32x2*)(smem + 2 * SLOT + (wm * 32 + mm * 16 + r) * 256 + (((wn * 8 + n * 2 + (q >> 1)) ^ r) * 16) + (q & 1) * 8);
                const u32x2 dw = *(const LAS u32x2*)(smem + 2 * SLOT + 16384 + (wn * 64 + n * 16 + q * 4) * 2), bw = *(const LAS u32x2*)(smem + 2 * SLOT + 16384 + 256 + (wn * 64 + n * 16 + q * 4) * 2);
                const f32x4 own = acc[n][mm];
                o[n][mm][0] = own[0] + g * (bflo(xw.x) * bflo(dw.x)) + (1.0f - g) * bflo(bw.x);
                o[n][mm][1] = own[1] + g * (bfhi(xw.x) * bfhi(dw.x)) + (1.0f - g) * bfhi(bw.x);
                o[n][mm][2] = own[2] + g * (bflo(xw.y) * bflo(dw.y)) + (1.0f - g) * bflo(bw.y);
                o[n][mm][3] = own[3] + g * (bfhi(xw.y) * bfhi(dw.y)) + (1.0f - g) * bfhi(bw.y);
                asm volatile("" : "+v"(o[n][mm]));
            }
        }
        {
            gu64* fl = (gu64*)(ws + WS_XFLAG) + ((bid ^ 1) * 4 + wid) * 8;
            wait_flags(fl, __hip_atomic_load(fl, RLX_AGENT), tag);
        }
#pragma unroll
        for (int n = 0; n < 4; ++n) {
            const u32x4 v = __builtin_amdgcn_raw_buffer_load_b128(rs, lane * 16 + n * 1024, xi, 16);
            o[n][0] += (f32x4){bflo(v[0]), bfhi(v[0]), bflo(v[1]), bfhi(v[1])}; o[n][1] += (f32x4){bflo(v[2]), bfhi(v[2]), bflo(v[3]), bfhi(v[3])};
        }
#pragma unroll
        for (int mm = 0; mm < 2; ++mm)
#pragma unroll
            for (int n = 0; n < 4; ++n) *(LAS f32x4*)(smem + wid * 8192 + (mm * 16 + r) * 256 + (((n * 4 + q) ^ r) * 16)) = o[n][mm];
        const unsigned ob = (unsigned)(((rb * 128 + wm * 64 + kh * 32 + q) * 1024 + c8 * 128 + wn * 64 + r * 4) * 4);
#pragma unroll
        for (int i = 0; i < 8; ++i) {
            const u32x4 v = *(const LAS u32x4*)(smem + wid * 8192 + (i * 4 + q) * 256 + ((r ^ ((i * 4 + q) & 15)) * 16));
            __builtin_amdgcn_raw_buffer_store_b128(v, ro, ob + i * 16384, 0, 16);
        }
    }
}
}
extern "C" __global__ void gm_code_end() {}

extern "C" void kernel_launch(void* const* d_in, const int* in_sizes, int n_in,
                              void* d_out, int out_size, void* d_ws, size_t ws_size,
                              hipStream_t stream) {
    using namespace gm;
    Args a;
    a.x = (const u16*)d_in[0]; a.lre = (const u16*)d_in[1]; a.lim = (const u16*)d_in[2]; a.bre = (const u16*)d_in[3]; a.bim = (const u16*)d_in[4];
    a.cre = (const u16*)d_in[5]; a.cim = (const u16*)d_in[6]; a.ldt = (const u16*)d_in[7]; a.dsk = (const u16*)d_in[8];
    a.w1 = (const u16*)d_in[10]; a.b1 = (const u16*)d_in[11]; a.w2 = (const u16*)d_in[12]; a.b2 = (const u16*)d_in[13]; a.gw = (const u16*)d_in[14];
    a.out = (float*)d_out; a.ws = (char*)d_ws;
    (void)hipFuncSetAttribute((const void*)fused_kernel, hipFuncAttributeMaxDynamicSharedMemorySize, LDS_BYTES);
    fused_kernel<<<256, 512, LDS_BYTES, stream>>>(a);
}
```

```cpp
#include <hip/hip_runtime.h>
#include <hip/hip_bf16.h>

namespace gm {
#define LAS __attribute__((address_space(3)))
#define GAS __attribute__((address_space(1)))
typedef unsigned short u16;
typedef short bf16x8 __attribute__((ext_vector_type(8)));
typedef short s16x4 __attribute__((ext_vector_type(4)));
typedef float f32x4 __attribute__((ext_vector_type(4)));
typedef float f32x2 __attribute__((ext_vector_type(2)));
typedef unsigned u32x4 __attribute__((ext_vector_type(4)));
typedef unsigned u32x2 __attribute__((ext_vector_type(2)));
typedef LAS char* ldsp;
typedef unsigned long long u64;
typedef GAS u64 gu64;

#define BAR() do { asm volatile("" ::: "memory"); __builtin_amdgcn_s_barrier(); asm volatile("" ::: "memory"); } while (0)
#define WAITV(n) asm volatile("s_waitcnt vmcnt(%0)" ::"i"(n) : "memory")
#define SCHED0() __builtin_amdgcn_sched_barrier(0)
#define LDS_FENCE() do { SCHED0(); __builtin_amdgcn_s_waitcnt(0xC07F); asm volatile("" ::: "memory"); SCHED0(); } while (0)
#define ONCE() int once_ = 1; asm volatile("" : "+s"(once_)); _Pragma("unroll 1") for (int i_ = 0; i_ < once_; ++i_)
#define RLX_AGENT __ATOMIC_RELAXED, __HIP_MEMORY_SCOPE_AGENT

constexpr unsigned WS_EPOCH = 0;
constexpr unsigned WS_FY = 4096;
constexpr unsigned WS_FC = 12288;
constexpr unsigned WS_FS = 16384;
constexpr unsigned WS_FM = 24576;
constexpr unsigned WS_XFLAG = 32768;
constexpr unsigned WS_GRAN = 131072;
constexpr unsigned WS_STAT = 1u << 20;
constexpr unsigned WS_CSP = 2u << 20;
constexpr unsigned WS_A2 = 8u << 20;
constexpr unsigned WS_XB = 32u << 20;
constexpr unsigned WS_END = 40u << 20;

constexpr int SLOT = 32768, SLOTM = 45056;
constexpr int LDS_PSEG = 131072;
constexpr int LDS_E = 135168;
constexpr int LDS_COEF = LDS_E + 4096;
constexpr int LDS_BC = LDS_COEF + 1024;
constexpr int LDS_STAT = LDS_BC + 2048;
constexpr int LDS_G2 = LDS_STAT + 2048;
constexpr int LDS_DUMMY = LDS_G2;
constexpr int LDS_TAG = LDS_G2 + 512;
constexpr int LDS_GATH = LDS_TAG + 64;
constexpr int LDS_SM = LDS_GATH + 16384;
constexpr int LDS_BYTES = LDS_SM + 1024;
static_assert(LDS_BYTES <= 160 * 1024, "LDS");

constexpr int TSP = 2;
constexpr unsigned XSENT = 0xFFFFFFFFu;
enum { PH_S1 = 0, PH_M1 = 1, PH_GS = 2, PH_GM = 3 };

struct Args {
    const u16 *x, *lre, *lim, *bre, *bim, *cre, *cim, *ldt, *dsk, *w1, *b1, *w2, *b2, *gw;
    float* out;
    char* ws;
};

__device__ __forceinline__ bf16x8 lds128(ldsp p) { return *(const LAS bf16x8*)p; }
__device__ __forceinline__ s16x4 ldstr(ldsp p) { return __builtin_amdgcn_ds_read_tr16_b64_v4i16((LAS s16x4*)p); }
__device__ __forceinline__ bf16x8 cat(s16x4 a, s16x4 b) { return __builtin_shufflevector(a, b, 0, 1, 2, 3, 4, 5, 6, 7); }
template <bool C> __device__ __forceinline__ void dma16(const void* sb, unsigned vo, unsigned la) {
    if constexpr (C) asm volatile("s_mov_b32 m0, %0\n\ts_nop 0\n\tglobal_load_lds_dwordx4 %1, %2 sc1" ::"s"(la), "v"(vo), "s"(sb) : "memory");
    else asm volatile("s_mov_b32 m0, %0\n\ts_nop 0\n\tglobal_load_lds_dwordx4 %1, %2" ::"s"(la), "v"(vo), "s"(sb) : "memory");
}
__device__ __forceinline__ void dma64c(const void* sb, unsigned vo, unsigned la) {
    asm volatile("s_mov_b32 m0, %0\n\ts_nop 0\n\tglobal_load_lds_dwordx4 %1, %2 sc1\n\tglobal_load_lds_dwordx4 %1, %2 offset:1024 sc1\n\t"
                 "global_load_lds_dwordx4 %1, %2 offset:2048 sc1\n\tglobal_load_lds_dwordx4 %1, %2 offset:3072 sc1" ::"s"(la), "v"(vo), "s"(sb) : "memory");
}
template <bool C> __device__ __forceinline__ void dma4(const void* sb, unsigned vo, unsigned la) {
    if constexpr (C) asm volatile("s_mov_b32 m0, %0\n\ts_nop 0\n\tglobal_load_lds_dword %1, %2 sc1" ::"s"(la), "v"(vo), "s"(sb) : "memory");
    else asm volatile("s_mov_b32 m0, %0\n\ts_nop 0\n\tglobal_load_lds_dword %1, %2" ::"s"(la), "v"(vo), "s"(sb) : "memory");
}
__device__ __forceinline__ void swarm8(unsigned sd, const void* p) {
    asm volatile("s_load_dword %0, %1, 0x0\n\ts_load_dword %0, %1, 0x40\n\ts_load_dword %0, %1, 0x80\n\ts_load_dword %0, %1, 0xc0\n\t"
                 "s_load_dword %0, %1, 0x100\n\ts_load_dword %0, %1, 0x140\n\ts_load_dword %0, %1, 0x180\n\ts_load_dword %0, %1, 0x1c0" ::"s"(sd), "s"(p));
}
__device__ __forceinline__ void swarm4(unsigned sd, const void* p) {
    asm volatile("s_load_dword %0, %1, 0x0\n\ts_load_dword %0, %1, 0x40\n\ts_load_dword %0, %1, 0x80\n\ts_load_dword %0, %1, 0xc0" ::"s"(sd), "s"(p));
}
__device__ __forceinline__ void swarm2x2(unsigned sd, const void* p) {
    asm volatile("s_load_dword %0, %1, 0x0\n\ts_load_dword %0, %1, 0x40\n\ts_load_dword %0, %1, 0x800\n\ts_load_dword %0, %1, 0x840" ::"s"(sd), "s"(p));
}
__device__ __forceinline__ void st128(u32x4 v, __amdgpu_buffer_rsrc_t rs, unsigned voff, unsigned base, int) { __builtin_amdgcn_raw_buffer_store_b128(v, rs, voff + base, 0, 16); }
__device__ __forceinline__ void st64(u32x2 v, __amdgpu_buffer_rsrc_t rs, unsigned voff, unsigned base, int) { __builtin_amdgcn_raw_buffer_store_b64(v, rs, voff + base, 0, 16); }
__device__ __forceinline__ float bflo(unsigned w) { return __uint_as_float(w << 16); }
__device__ __forceinline__ float bfhi(unsigned w) { return __uint_as_float(w & 0xffff0000u); }
__device__ __forceinline__ float bf1(u16 b) { return __uint_as_float((unsigned)b << 16); }
__device__ __forceinline__ unsigned pkbf(float lo, float hi) {
    typedef __bf16 bf2_t __attribute__((ext_vector_type(2)));
    bf2_t v = {(__bf16)lo, (__bf16)hi};
    return __builtin_bit_cast(unsigned, v);
}
__device__ __forceinline__ void cfma(float lr, float li, float& zr, float& zi, float ur, float ui) {
    float tr, ti;
    asm("v_fma_f32 %0, -%3, %5, %6\n\tv_fma_f32 %1, %3, %4, %7\n\tv_fma_f32 %0, %2, %4, %0\n\tv_fma_f32 %1, %2, %5, %1"
        : "=&v"(tr), "=&v"(ti) : "v"(lr), "v"(li), "v"(zr), "v"(zi), "v"(ur), "v"(ui));
    zr = tr; zi = ti;
}
__device__ __forceinline__ bool deadline(unsigned long long t0) { return __builtin_amdgcn_s_memrealtime() - t0 > 400000ull; }

struct LaneC { unsigned a0, a1, b0, b1, b2; };
template <int PH>
__device__ __forceinline__ LaneC lane_consts(unsigned base, int wm, int wn, int lane) {
    const int r = lane & 15, q = lane >> 4, rr = r >> 2, p = lane & 3;
    const unsigned rl = r * 128 + ((q ^ (r >> 1)) * 16);
    LaneC c; c.b2 = 0;
    if constexpr (PH == PH_S1) { c.a0 = wm * 8192 + rl; c.a1 = c.a0 ^ 64; c.b0 = 16384 + wn * 8192 + rl; c.b1 = c.b0 ^ 64; }
    if constexpr (PH == PH_M1) {
        c.a0 = wm * 8192 + rl; c.a1 = c.a0 ^ 64;
        const unsigned t0 = 16384 + q * 7 * 512 + rr * 64 + ((q & 1) * 32) + p * 8;
        c.b0 = t0 + wn * 2048;
        c.b1 = (t0 ^ 32) + wn * 2048;
        c.b2 = t0 ^ (wn * 32);
    }
    if constexpr (PH == PH_GS) {
        c.a0 = (q * 8 + wm * 4) * 256 + (p >> 1) * 128 + (4 * (q & 1) + rr) * 16 + (p & 1) * 8; c.a1 = c.a0 ^ 64;
        c.b0 = 16384 + wn * 8192 + rl; c.b1 = c.b0 ^ 64;
    }
    if constexpr (PH == PH_GM) {
        c.a0 = wm * 8192 + lane * 16; c.a1 = c.a0;
        c.b0 = 16384 + q * 4 * 512 + wn * 1024 + rr * 64 + ((q & 1) * 32) + p * 8; c.b1 = c.b0 ^ 32;
    }
    c.a0 += base; c.a1 += base; c.b0 += base; c.b1 += base; c.b2 += base;
    return c;
}
template <int PH> __device__ __forceinline__ void advance(LaneC& c, unsigned d) {
    c.a0 += d; c.b0 += d; c.b1 += d;
    if constexpr (PH != PH_GM) c.a1 += d;
    if constexpr (PH == PH_M1) c.b2 += d;
    if constexpr (PH == PH_M1) asm volatile("" : "+v"(c.a0), "+v"(c.a1), "+v"(c.b0), "+v"(c.b1), "+v"(c.b2));
    else if constexpr (PH == PH_GM) asm volatile("" : "+v"(c.a0), "+v"(c.b0), "+v"(c.b1));
    else asm volatile("" : "+v"(c.a0), "+v"(c.a1), "+v"(c.b0), "+v"(c.b1));
}
template <int PH, int KK, int NB>
__device__ __forceinline__ void load_frags(const LaneC& c, bf16x8 (&A)[4], bf16x8 (&B)[NB]) {
    if constexpr (PH == PH_S1 || PH == PH_M1) {
        ldsp pa = (ldsp)(size_t)(KK ? c.a1 : c.a0);
#pragma unroll
        for (int m = 0; m < 4; ++m) A[m] = lds128(pa + m * 2048);
    } else if constexpr (PH == PH_GS) {
        ldsp p0 = (ldsp)(size_t)c.a0, p1 = (ldsp)(size_t)c.a1;
#pragma unroll
        for (int m = 0; m < 4; ++m) A[m] = cat(ldstr(p0 + (KK * 32 + m) * 256), ldstr(p1 + (KK * 32 + m) * 256));
    } else {
        ldsp pa = (ldsp)(size_t)c.a0;
#pragma unroll
        for (int m = 0; m < 4; ++m) A[m] = lds128(pa + (m * 2 + KK) * 1024);
    }
    if constexpr (PH == PH_S1 || PH == PH_GS) {
        ldsp pb = (ldsp)(size_t)(KK ? c.b1 : c.b0);
#pragma unroll
        for (int n = 0; n < NB; ++n) B[n] = lds128(pb + n * 2048);
    } else {
        constexpr int NS2 = PH == PH_M1 ? 7 : 4;
#pragma unroll
        for (int n = 0; n < NB; ++n) { ldsp p = (ldsp)(size_t)(n == 6 ? c.b2 : (n & 1) ? c.b1 : c.b0) + KK * 4 * NS2 * 512 + (n >> 1) * 512; B[n] = cat(ldstr(p), ldstr(p + 256)); }
    }
}
template <int PH, int NB>
__device__ __forceinline__ void mma(f32x4 (&acc)[NB][4], const bf16x8 (&A)[4], const bf16x8 (&B)[NB]) {
#pragma unroll
    for (int n = 0; n < NB; ++n)
#pragma unroll
        for (int mi = 0; mi < 4; ++mi) {
            const int m = (n & 1) ? 3 - mi : mi;
            if constexpr (PH == PH_S1) acc[n][m] = __builtin_amdgcn_mfma_f32_16x16x32_bf16(A[m], B[n], acc[n][m], 0, 0, 0);
            else acc[n][m] = __builtin_amdgcn_mfma_f32_16x16x32_bf16(B[n], A[m], acc[n][m], 0, 0, 0);
        }
}
template <int PH> constexpr int nreads() { return PH == PH_S1 ? 8 : PH == PH_M1 ? 18 : 12; }
template <int NR, int NM, int NV = 0>
__device__ __forceinline__ void sched_half() {
#pragma unroll
    for (int i = 0; i < NR; ++i) { __builtin_amdgcn_sched_group_barrier(0x8, 1, 0); __builtin_amdgcn_sched_group_barrier(0x100, 1, 0); }
    if constexpr (NV > 0) {
#pragma unroll
        for (int i = 0; i < (NV + 1) / 2; ++i) { __builtin_amdgcn_sched_group_barrier(0x8, 1, 0); __builtin_amdgcn_sched_group_barrier(0x2, 2, 0); }
    }
    __builtin_amdgcn_sched_group_barrier(0x8, NM - NR - (NV + 1) / 2, 0);
}
template <int PH, int NB>
__device__ __forceinline__ void cstep(LaneC& c, unsigned d, f32x4 (&acc)[NB][4], bf16x8 (&A0)[4], bf16x8 (&B0)[NB], bf16x8 (&A1)[4], bf16x8 (&B1)[NB]) {
    load_frags<PH, 1, NB>(c, A1, B1);
    mma<PH, NB>(acc, A0, B0);
    advance<PH>(c, d);
    sched_half<nreads<PH>(), 4 * NB, PH == PH_M1 ? 5 : 4>();
    SCHED0();
    __builtin_amdgcn_s_waitcnt(0xC07F);
    BAR();
    SCHED0();
    load_frags<PH, 0, NB>(c, A0, B0);
    mma<PH, NB>(acc, A1, B1);
    sched_half<nreads<PH>(), 4 * NB>();
}
template <int PC, int PN, bool LAST, int NB>
__device__ __forceinline__ void cstep2(const LaneC& cc, const LaneC& cn, f32x4 (&acc)[NB][4], bf16x8 (&A0)[4], bf16x8 (&B0)[NB], bf16x8 (&A1)[4], bf16x8 (&B1)[NB]) {
    SCHED0();
    load_frags<PC, 1, NB>(cc, A1, B1);
    mma<PC, NB>(acc, A0, B0);
    sched_half<nreads<PC>(), 4 * NB>();
    SCHED0();
    __builtin_amdgcn_s_waitcnt(0xC07F);
    BAR();
    SCHED0();
    if constexpr (!LAST) load_frags<PN, 0, NB>(cn, A0, B0);
    mma<PC, NB>(acc, A1, B1);
    if constexpr (!LAST) sched_half<nreads<PN>(), 4 * NB>();
    SCHED0();
}
__device__ __forceinline__ unsigned ringd(int t) { return ((t + 3) & 3) ? (unsigned)SLOT : (unsigned)(-3 * SLOT); }

__device__ __forceinline__ u64 next_tag(u64 e) { return e * 0x9E3779B97F4A7C15ull + 0xD1B54A32D192ED03ull; }
__device__ __forceinline__ void signal_flag(gu64* fl, u64 tag, int lane) {
    WAITV(0);
    if (lane == 0) __hip_atomic_store(fl, tag, RLX_AGENT);
}
template <int CTRL, int RMASK> __device__ __forceinline__ float dpp_add(float v) {
    return v + __builtin_bit_cast(float, __builtin_amdgcn_update_dpp(0, __builtin_bit_cast(int, v), CTRL, RMASK, 0xf, true));
}
__device__ __forceinline__ float wave_sum(float v) {
    v = dpp_add<0x111, 0xf>(v); v = dpp_add<0x112, 0xf>(v); v = dpp_add<0x114, 0xf>(v); v = dpp_add<0x118, 0xf>(v);
    v = dpp_add<0x142, 0xa>(v); v = dpp_add<0x143, 0xc>(v);
    return __builtin_bit_cast(float, __builtin_amdgcn_readlane(__builtin_bit_cast(int, v), 63));
}


__device__ __forceinline__ unsigned gelu2(f32x2 v) {
    const f32x2 t = (v * v) * -0.1029432397f + -2.3022081986f;
    const f32x2 u = v * t;
    f32x2 d; d.x = __builtin_amdgcn_exp2f(u.x); d.y = __builtin_amdgcn_exp2f(u.y);
    d = d + 1.0f;
    f32x2 r; r.x = __builtin_amdgcn_rcpf(d.x); r.y = __builtin_amdgcn_rcpf(d.y);
    const f32x2 o = v * r;
    return pkbf(o.x, o.y);
}

__device__ __forceinline__ void m1_epilogue(f32x4 (&acc)[7][4], ldsp smem, __amdgpu_buffer_rsrc_t rs, int rb, int cb, int wm, int wn, int lane) {
    f32x2 st[4];
#pragma unroll
    for (int m = 0; m < 4; ++m) st[m] = *(const LAS f32x2*)(smem + LDS_STAT + (wm * 64 + m * 16 + (lane & 15)) * 16);
    u32x2 pk[7][4];
#pragma unroll
    for (int n = 0; n < 7; ++n) {
        const int nbt = n == 6 ? 6 + wn : n + 8 * wn;
        const f32x4 b = *(const LAS f32x4*)(smem + LDS_BC + (nbt * 16 + (lane >> 4) * 4) * 4), cs = *(const LAS f32x4*)(smem + LDS_BC + 1024 + (nbt * 16 + (lane >> 4) * 4) * 4);
#pragma unroll
        for (int m = 0; m < 4; ++m) {
            const f32x4 v = acc[n][m] * st[m].x + (cs * st[m].y + b);
            pk[n][m].x = gelu2((f32x2){v[0], v[1]});
            pk[n][m].y = gelu2((f32x2){v[2], v[3]});
        }
        if ((n & 1) || n == 6) {
            const int pair = 7 * cb + (nbt >> 1);
            const unsigned base = WS_A2 + (unsigned)(rb * 88 + 32 + (pair >> 1)) * 16384u + (unsigned)(pair & 1) * 1024u + (unsigned)(wm * 4) * 2048u;
#pragma unroll
            for (int m = 0; m < 4; ++m) {
                if (n == 6) st64(pk[n][m], rs, lane * 16 + m * 2048, base + wn * 8, 16);
                else { const u32x4 v = {pk[n - (n & 1)][m].x, pk[n - (n & 1)][m].y, pk[n][m].x, pk[n][m].y}; st128(v, rs, lane * 16 + m * 2048, base, 16); }
            }
        }
    }
}

__device__ __forceinline__ void colsum_half(const bf16x8 (&B)[7], f32x4& cs, int lane) {
    int r = lane & 15;
    SCHED0();
    asm volatile("" : "+v"(r));
#pragma unroll
    for (int n = 0; n < 7; ++n) {
        const unsigned o = r == n ? 0x3F803F80u : 0u;
        cs = __builtin_amdgcn_mfma_f32_16x16x32_bf16(B[n], __builtin_bit_cast(bf16x8, (u32x4){o, o, o, o}), cs, 0, 0, 0);
    }
}

__device__ __forceinline__ void check_flags(u32x4 v, const char* ws, unsigned off, u64 tag) {
    const unsigned lo = (unsigned)tag, hi = (unsigned)(tag >> 32);
    if (!__all((((v[0] ^ lo) | (v[1] ^ hi)) | ((v[2] ^ lo) | (v[3] ^ hi))) == 0u)) {
        const unsigned long long t0 = __builtin_amdgcn_s_memrealtime();
        bool ok;
        do { __builtin_amdgcn_s_sleep(2);
             ok = __hip_atomic_load((gu64*)(ws + off), RLX_AGENT) == tag && __hip_atomic_load((gu64*)(ws + off + 8), RLX_AGENT) == tag;
        } while (!__all(ok) && !deadline(t0));
    }
    asm volatile("" ::: "memory");
}

__global__ __launch_bounds__(512, 2) void fused_kernel(Args a) {
    extern __shared__ __attribute__((aligned(16))) char smem_[];
    ldsp smem = (ldsp)smem_;
    const int wid = __builtin_amdgcn_readfirstlane(threadIdx.x >> 6), lane = threadIdx.x & 63;
    const int bid = blockIdx.x, xcd = bid & 7, rk = bid >> 3;
    const int kh = xcd & 1, rb = (xcd >> 1) * 4 + (rk >> 3), c8 = rk & 7, cb = kh * 8 + c8;
    char* const ws = a.ws;
    const char* pc; unsigned code_len;
    asm volatile("s_getpc_b64 %0\n.Lgm_pc%=:\n\ts_mov_b32 %1, gm_code_end-.Lgm_pc%=" : "=s"(pc), "=s"(code_len));
    if (wid == 3) {
        const unsigned o = (unsigned)(rk + 32 * (lane & 15)) * 128u;
        dma4<false>(pc, o < code_len && code_len < (1u << 20) ? o : 0u, (unsigned)(size_t)smem + LDS_DUMMY);
    }
    asm volatile("" ::"s"(a.x), "s"(a.lre), "s"(a.lim), "s"(a.bre), "s"(a.bim), "s"(a.cre), "s"(a.cim), "s"(a.ldt), "s"(a.dsk), "s"(a.w1), "s"(a.b1), "s"(a.w2), "s"(a.b2), "s"(a.gw), "s"(a.out), "s"(a.ws));

    u32x4 xv[2][2], gv[2]; u64 ep = 0;
    if (wid < 4) {
        const int lnrow = 512 * (xcd >> 1) + 256 * kh + 8 * rk + 2 * wid;
#pragma unroll
        for (int i = 0; i < 2; ++i)
#pragma unroll
            for (int h = 0; h < 2; ++h) xv[i][h] = *(const u32x4*)(a.x + (size_t)(lnrow + i) * 1024 + h * 512 + lane * 8);
#pragma unroll
        for (int h = 0; h < 2; ++h) gv[h] = *(const u32x4*)(a.gw + h * 512 + lane * 8);
        ep = __hip_atomic_load((gu64*)(ws + WS_EPOCH), RLX_AGENT);
    }
    int wid2 = wid;
    asm volatile("" : "+s"(wid2)::"memory");
    if (__builtin_expect(wid2 >= 4, 1)) {
        __builtin_amdgcn_s_waitcnt(0x0F70);
        const int w = wid - 4, un = (xcd >> 1) * 32 + rk;
        const unsigned lb = (unsigned)(size_t)smem;
        const unsigned swz = (unsigned)(((lane & 7) ^ ((4 * (w & 1) + (lane >> 4)) & 7)) * 16);
        const unsigned vl = lane * 16;
        unsigned sd;
        asm volatile("s_mov_b32 %0, 0" : "=s"(sd));
        const LAS u32x4* const parea = (const LAS u32x4*)(smem + LDS_E + w * 1024 + lane * 16);
        const char* const pw2 = (const char*)a.w2 + (size_t)(1792 * kh + un) * 2048 + w * 512;
        constexpr int W2C = 128 * 2048;
        const __amdgpu_buffer_rsrc_t rs = __builtin_amdgcn_make_buffer_rsrc(ws, 0, WS_END, 0x00020000);
        u64 tag;
        f32x2 l16, wt[4];
        const unsigned vsa = (unsigned)((rb * 128 + 16 * (lane >> 5) + ((lane >> 3) & 3) + 32 * (w & 1) + 4 * (w >> 1)) * 2048) + swz;
        const unsigned vsb = (unsigned)((cb * 64 + 16 * (w >> 1) + 8 * (w & 1) + (lane >> 3)) * 2048) + swz;
        auto none = [] {};
        auto issue_s = [&](int t, auto mid) {
            const unsigned s = lb + ((t + 2) & 3) * SLOT + w * 1024;
            const int ko = ((t + rb + cb) & 15) * 128;
#pragma unroll
            for (int n = 0; n < 4; ++n) dma16<false>((const char*)a.x + ko + (64 * (n >> 1) + 8 * (n & 1)) * 2048, vsa, s + n * 4096);
            mid();
#pragma unroll
            for (int n = 0; n < 4; ++n) dma16<false>((const char*)((n & 1) ? a.bim : a.bre) + ko + (n >> 1) * 32 * 2048, vsb, s + 16384 + n * 4096);
        };
        {
            const unsigned va = (unsigned)((rb * 128 + 8 * w + (lane >> 3)) * 2048) + swz;
            unsigned vb[7];
#pragma unroll
            for (int i = 0; i < 7; ++i) {
                const int sl = 2 * i + (lane >> 5), kq = 2 * w + (sl >= 7 ? 1 : 0), nb2 = sl >= 7 ? sl - 7 : sl;
                vb[i] = (unsigned)((8 * kq + ((lane >> 2) & 7)) * 7168 + cb * 448 + nb2 * 64 + (((lane & 3) ^ (2 * (kq & 1))) * 16));
            }
            int slot = 0;
            auto issue = [&](int t, auto mid) {
                const unsigned s = lb + slot * SLOTM;
                slot = slot == 2 ? 0 : slot + 1;
                int ks;
                if (rb >= TSP) { const int u = rb + t - TSP; ks = t < TSP ? t : u >= 16 ? u - 16 + TSP : u; }
                else ks = t > TSP ? t : t == TSP ? rb : t < rb ? t : t + 1;
                ks = __builtin_amdgcn_readfirstlane(ks);
                const char* pb = (const char*)a.w1 + (size_t)ks * 64 * 7168;
#pragma unroll
                for (int i = 0; i < 7; ++i) dma16<false>(pb, vb[i], s + 16384 + (w * 7 + i) * 1024);
                mid();
#pragma unroll
                for (int n = 0; n < 4; ++n) dma16<false>((const char*)a.x + ks * 128 + n * 32 * 2048, va, s + w * 1024 + n * 4096);
            };
            issue(0, none);
            { const char* sp = w == 0 ? (const char*)a.b1 + cb * 448 : w == 1 ? (const char*)a.lre + cb * 128 : w == 2 ? (const char*)a.lim + cb * 128 : (const char*)a.ldt + cb * 128;
              if (lane < (w == 0 ? 28 : 8)) dma16<false>(sp, vl, lb + LDS_SM + (w == 0 ? 0 : 384 + w * 128)); }
            issue(1, none);
            WAITV(12); BAR();
            issue(2, none);
            { const u32x2 tv = *(const LAS u32x2*)(smem + LDS_TAG);
              tag = ((u64)(unsigned)__builtin_amdgcn_readfirstlane(tv.y) << 32) | (unsigned)__builtin_amdgcn_readfirstlane(tv.x); }
            const char* const pbw = (const char*)((w & 1) ? a.bim : a.bre) + (size_t)(512 * kh + 4 * un + 2 * (w >> 1)) * 2048;
            const unsigned fo = (lane & 8) ? WS_FC + cb * 128 + (lane & 7) * 16 : WS_FY + rb * 128 + (lane & 7) * 16;
            const int lnrow = 512 * (xcd >> 1) + 256 * kh + 8 * rk;
_Pragma("unroll 1")
            for (int t = 0; t < 13; ++t) {
                WAITV(11); BAR();
                issue(t + 3, [&] { if (t >= 4 && t != 12) swarm8(sd, pbw + (t - 4) * 512); });
                if (t == TSP + 1 && w == 0) {
                    const u32x4 v = *(const LAS u32x4*)(smem + LDS_BC + lane * 16);
                    st128(v, rs, lane < 8 ? WS_STAT + (unsigned)(lnrow + lane) * 16u : WS_CSP + (unsigned)(cb * 16 + rb) * 896u + (lane - 8) * 16, 0, 16);
                }
                if (t == TSP + 3 && w == 0) __hip_atomic_store((gu64*)(ws + ((lane & 1) ? WS_FC + (cb * 16 + rb) * 8 : WS_FY + ((lnrow >> 7) * 16 + (rk & 15)) * 8)), tag, RLX_AGENT);
                if (t >= 4 && t < 12 && ((t ^ w) & 1)) st128((u32x4){XSENT, XSENT, XSENT, XSENT}, rs, vl + ((t - 4) >> 1) * 1024, WS_XB + (unsigned)(bid * 4 + w) * 8192u, 16);
                if (t == 11) dma16<true>(ws, fo, lb + LDS_E + w * 1024);
            }
            WAITV(11); BAR();
            check_flags(*parea, ws, fo, tag);
#pragma unroll
            for (int n = 0; n < 3; ++n) dma16<true>(ws + WS_CSP + cb * 14336 + (w + 4 * n) * 1024, vl, lb + LDS_GATH + (w + 4 * n) * 1024);
            if (w < 2) dma16<true>(ws + WS_CSP + cb * 14336 + (w + 12) * 1024, vl, lb + LDS_GATH + (w + 12) * 1024);
            else dma16<true>(ws + WS_STAT + rb * 2048 + (w - 2) * 1024, vl, lb + LDS_STAT + (w - 2) * 1024);
            WAITV(0); BAR();
            __hip_atomic_store((gu64*)(ws + WS_XFLAG) + bid * 4 + w, tag, RLX_AGENT);
            {
                const int col = w * 56 + (lane < 56 ? lane : 0);
                float s = 0.f;
#pragma unroll
                for (int i = 0; i < 16; ++i) s += *(const LAS float*)(smem + LDS_GATH + i * 896 + col * 4);
                const float gg = *(const LAS float*)(smem + LDS_STAT + (w * 32 + (lane & 31)) * 16 + 8);
                const float b1v = bf1(*(const LAS u16*)(smem + LDS_SM + col * 2));
                *(LAS float*)(smem + LDS_BC + 1024 + col * 4) = s;
                *(LAS float*)(smem + LDS_BC + col * 4) = b1v;
                *(LAS float*)(smem + LDS_G2 + (w * 32 + (lane & 31)) * 4) = 2.0f * gg;
            }
            issue_s(0, none);
            __builtin_amdgcn_s_waitcnt(0xC07F);
            BAR();
        }
        const char* const a2 = ws + WS_A2 + (size_t)rb * 88 * 16384 + w * 4096;
        const unsigned vr = (unsigned)((c8 * 128 + 8 * w + (lane >> 3)) * 2048) + swz;
        unsigned vt[2];
        { const int rho = (lane >> 2) & 7;
#pragma unroll
          for (int qq = 0; qq < 2; ++qq) { const int q = 2 * (w & 1) + qq; const int k = 32 * (w >> 1) + (rho < 4 ? 4 * q + rho : 16 + 4 * q + rho - 4);
              vt[qq] = (unsigned)(k * 2048 + c8 * 256 + (lane >> 5) * 64 + (((lane & 3) ^ (2 * qq)) * 16)); } }
        auto issue_gm = [&](int t, auto mid) {
            const unsigned s = lb + ((t + 2) & 3) * SLOT;
            const int km = kh * 28 + t;
            dma64c(a2 + (size_t)(32 + km) * 16384, vl, s + w * 4096);
            mid();
            const char* pb = (const char*)a.w2 + (size_t)km * 64 * 2048;
#pragma unroll
            for (int n = 0; n < 4; ++n) dma16<false>(pb + (n & 1) * 128, vt[n >> 1], s + 16384 + (w * 4 + n) * 1024);
        };
        auto issue_gs = [&](int t, auto mid) {
            const unsigned s = lb + ((t + 2) & 3) * SLOT;
            const int ks = kh * 16 + (t - 28);
            dma64c(a2 + (size_t)ks * 16384, vl, s + w * 4096);
            mid();
            const char* pb = (const char*)((ks & 1) ? a.cim : a.cre) + (ks >> 1) * 128;
#pragma unroll
            for (int n = 0; n < 4; ++n) dma16<false>(pb + n * 32 * 2048, vr, s + 16384 + (w + 4 * n) * 1024);
        };
        {
_Pragma("unroll 1")
            for (int t = 1; t < 4; ++t) issue_s(t, none);
            {
                const float lr = bf1(*(const LAS u16*)(smem + LDS_SM + 512 + lane * 2)), li = bf1(*(const LAS u16*)(smem + LDS_SM + 640 + lane * 2));
                const float dt = (float)(__bf16)expf(bf1(*(const LAS u16*)(smem + LDS_SM + 768 + lane * 2)));
                const float zr = dt * lr, zi = dt * li;
                float sn, cs; sincosf(zi, &sn, &cs);
                const float er = expf(zr), Lr = er * cs, Li = er * sn;
                float pr = Lr, pi = Li;
#pragma unroll
                for (int i = 0; i < 4; ++i) cfma(pr, pi, pr, pi, 0.f, 0.f);
                l16 = (f32x2){pr, pi};
                if (w == 0) {
                    const float den = 1.0f / (lr * lr + li * li), nr = Lr - 1.0f;
                    *(LAS f32x4*)(smem + LDS_COEF + lane * 16) = (f32x4){Lr, Li, (nr * lr + Li * li) * den, (Li * lr - nr * li) * den};
                }
                if (w == 2) {
                    float qr = 1.0f, qi = 0.f;
#pragma unroll
                    for (int i = 0; i < 8; ++i) { *(LAS f32x2*)(smem + LDS_PSEG + (i * 64 + lane) * 8) = (f32x2){qr, qi}; cfma(pr, pi, qr, qi, 0.f, 0.f); }
                }
#pragma unroll
                for (int i = 0; i < 3; ++i) cfma(pr, pi, pr, pi, 0.f, 0.f);
                float p2r = pr, p2i = pi; cfma(pr, pi, p2r, p2i, 0.f, 0.f);
                float p4r = p2r, p4i = p2i; cfma(p2r, p2i, p4r, p4i, 0.f, 0.f);
                const int e0 = (rb - 1 - w) & 3, top = (rb - 1 - w) >> 2;
                float qr = (e0 & 1) ? pr : 1.0f, qi = (e0 & 1) ? pi : 0.f;
                if (e0 & 2) cfma(p2r, p2i, qr, qi, 0.f, 0.f);
#pragma unroll
                for (int j = 3; j >= 0; --j) {
                    wt[j] = j <= top ? (f32x2){qr, qi} : (f32x2){0.f, 0.f};
                    if (j <= top) cfma(p4r, p4i, qr, qi, 0.f, 0.f);
                }
                asm volatile("" : "+v"(wt[0]), "+v"(wt[1]), "+v"(wt[2]), "+v"(wt[3]), "+v"(l16));
                __builtin_amdgcn_s_waitcnt(0xC07F);
            }
            WAITV(24); BAR();
            const unsigned fo = WS_FM + (rb * 2 + kh) * 256 + (lane & 15) * 16;
_Pragma("unroll 1")
            for (int t = 0; t < 10; ++t) { WAITV(16); BAR(); issue_s(t + 4, [&] { swarm4(sd, pw2 + (t >> 1) * W2C + (t & 1) * 256); }); }
            WAITV(16); BAR(); issue_s(14, [&] { dma16<true>(ws, fo, lb + LDS_E + w * 1024); });
            WAITV(17); BAR(); issue_s(15, none);
            WAITV(8); BAR();
            check_flags(*parea, ws, fo, tag);
            swarm8(sd, pw2 + 5 * W2C);
            BAR();
            issue_gm(0, [&] { swarm4(sd, pw2 + 6 * W2C); });
            WAITV(8); BAR();
            issue_gm(1, [&] { swarm4(sd, pw2 + 6 * W2C + 256); });
            BAR();
            issue_gm(2, none); issue_gm(3, none);
        }
        {
            const unsigned fo = (lane & 16) ? WS_XFLAG + (bid ^ 1) * 32 + (lane & 1) * 16 : WS_FS + (rb * 2 + kh) * 256 + (lane & 15) * 16;
            const char* const pcr = (const char*)a.cre + (8 * un + 2 * w) * 2048 + kh * 1024;
            const char* const pci = (const char*)a.cim + (8 * un + 2 * w) * 2048 + kh * 1024;
            const unsigned tag_lo = (unsigned)tag, tag_hi = (unsigned)(tag >> 32);
            WAITV(24); BAR();
            if (w == 1) {
                f32x2 e[8];
#pragma unroll
                for (int i = 0; i < 8; ++i) e[i] = *(const LAS f32x2*)(smem + LDS_E + (i * 64 + lane) * 8);
                LDS_FENCE();
                float er = 0.f, ei = 0.f;
#pragma unroll
                for (int i = 0; i < 8; ++i) {
                    *(LAS f32x2*)(smem + LDS_E + (i * 64 + lane) * 8) = (f32x2){er, ei};
                    cfma(l16.x, l16.y, er, ei, e[i].x, e[i].y);
                }
                st128((u32x4){__float_as_uint(er), tag_lo, __float_as_uint(ei), tag_hi}, rs, vl, WS_GRAN + (unsigned)(cb * 16 + rb) * 1024u, 16);
                __builtin_amdgcn_s_waitcnt(0xC07F);
            }
_Pragma("unroll 1")
            for (int t = 0; t < 8; ++t) { WAITV(16); BAR(); issue_gm(t + 4, [&] { swarm4(sd, pw2 + (7 + (t >> 1)) * W2C + (t & 1) * 256); }); }
#pragma unroll
            for (int j = 0; j < 4; ++j)
                if (w + 4 * j < rb) dma16<true>(ws + WS_GRAN + cb * 16384 + (w + 4 * j) * 1024, vl, lb + LDS_GATH + (w * 4 + j) * 1024);
            WAITV(16); BAR(); issue_gm(12, [&] { swarm8(sd, pw2 + 11 * W2C); });
            WAITV(16); BAR(); issue_gm(13, none);
            WAITV(16); BAR();
            {
                u32x4 g[4];
                unsigned bad = 0;
#pragma unroll
                for (int j = 0; j < 4; ++j) g[j] = *(const LAS u32x4*)(smem + LDS_GATH + (w * 4 + j) * 1024 + lane * 16);
#pragma unroll
                for (int j = 0; j < 4; ++j) bad |= w + 4 * j < rb ? (g[j][1] ^ tag_lo) | (g[j][3] ^ tag_hi) : 0u;
                if (!__all(bad == 0u)) {
                    const unsigned long long t0 = __builtin_amdgcn_s_memrealtime();
                    do {
                        __builtin_amdgcn_s_sleep(1);
                        bad = 0;
                        asm volatile("" ::: "memory");
#pragma unroll
                        for (int j = 0; j < 4; ++j) {
                            g[j] = __builtin_amdgcn_raw_buffer_load_b128(rs, vl + (w + 4 * j) * 1024, WS_GRAN + (unsigned)cb * 16384u, 16);
                            bad |= w + 4 * j < rb ? (g[j][1] ^ tag_lo) | (g[j][3] ^ tag_hi) : 0u;
                        }
                    } while (!__all(bad == 0u) && !deadline(t0));
                }
                WAITV(8); BAR();
                float sr = 0.f, si = 0.f;
#pragma unroll
                for (int j = 0; j < 4; ++j) {
                    float vr_ = w + 4 * j < rb ? __uint_as_float(g[j][0]) : 0.f, vi_ = w + 4 * j < rb ? __uint_as_float(g[j][2]) : 0.f;
                    cfma(wt[j].x, wt[j].y, vr_, vi_, sr, si); sr = vr_; si = vi_;
                }
                *(LAS f32x2*)(smem + LDS_BC + lane * 32 + w * 8) = (f32x2){sr, si};
                __builtin_amdgcn_s_waitcnt(0xC07F);
            }
            WAITV(0); BAR();
_Pragma("unroll 1")
            for (int t = 14; t < 17; ++t) issue_gm(t, [&] { if (t > 14) swarm8(sd, pw2 + (t - 3) * W2C); });
_Pragma("unroll 1")
            for (int t = 13; t < 21; ++t) { WAITV(16); BAR(); issue_gm(t + 4, [&] { swarm2x2(sd, ((t & 1) ? pcr : pci) + ((t - 13) >> 1) * 128); }); }
            dma16<true>(ws, fo, lb + LDS_E + w * 1024);
            WAITV(16); BAR(); issue_gm(25, [&] { swarm2x2(sd, pcr + 512); });
            WAITV(16); BAR(); issue_gm(26, none);
            { u32x4 fv;
              WAITV(16); BAR(); issue_gm(27, [&] { fv = *parea; });
              check_flags(fv, ws, fo, tag); }
_Pragma("unroll 1")
            for (int t = 24; t < 31; ++t) { WAITV(16); BAR(); issue_gs(t + 4, [&] { swarm2x2(sd, ((t & 1) ? pcr : pci) + ((t - 15) >> 1) * 128); }); }
_Pragma("unroll 1")
            for (int t = 31; t < 40; ++t) { WAITV(16); BAR(); issue_gs(t + 4, none); }
            WAITV(16); BAR();
            {
                const int rl = 16 * w + (lane >> 4);
#pragma unroll
                for (int n = 0; n < 4; ++n) {
                    const int rr = rl + 4 * n;
                    dma16<false>((const char*)a.x + (size_t)(rb * 128 + kh * 32) * 2048 + c8 * 256, (unsigned)((64 * (rr >> 5) + (rr & 31)) * 2048 + (((lane & 15) ^ (rr & 15)) * 16)), lb + 2 * SLOT + (w * 4 + n) * 1024);
                }
                dma4<false>((const char*)((w & 1) ? a.b2 : a.dsk) + c8 * 256, (unsigned)lane * 4u, lb + 2 * SLOT + 16384 + w * 256);
            }
            WAITV(13); BAR();
            WAITV(5); BAR();
            WAITV(0); BAR();
            asm volatile("s_waitcnt lgkmcnt(0)" ::"s"(sd));
        }
        if (bid == 0 && w == 0) {
            const unsigned long long t0 = __builtin_amdgcn_s_memrealtime();
            for (;;) {
                bool ok = true;
#pragma unroll
                for (int i = 0; i < 4; ++i) ok &= __hip_atomic_load((gu64*)(ws + WS_FY) + i * 64 + lane, RLX_AGENT) == tag;
                if (__all(ok) || deadline(t0)) break;
                __builtin_amdgcn_s_sleep(2);
            }
            if (lane == 0) __hip_atomic_store((gu64*)(ws + WS_EPOCH), tag, RLX_AGENT);
        }
        return;
    }

    const int wm = wid >> 1, wn = wid & 1;
    const unsigned lb = (unsigned)(size_t)smem;
    const __amdgpu_buffer_rsrc_t rs = __builtin_amdgcn_make_buffer_rsrc(ws, 0, WS_END, 0x00020000);
    {
        if (wid == 0) {
            const u64 t = next_tag(ep);
            if (lane == 0) *(LAS u32x2*)(smem + LDS_TAG) = (u32x2){(unsigned)t, (unsigned)(t >> 32)};
        }
        f32x2 xf[2][8];
        float s[2], gd[2], vs[2];
#pragma unroll
        for (int i = 0; i < 2; ++i) {
            f32x2 s2 = {0.f, 0.f}, g2 = {0.f, 0.f};
#pragma unroll
            for (int h = 0; h < 2; ++h)
#pragma unroll
                for (int j = 0; j < 4; ++j) {
                    const f32x2 v = {bflo(xv[i][h][j]), bfhi(xv[i][h][j])}, gw = {bflo(gv[h][j]), bfhi(gv[h][j])};
                    xf[i][h * 4 + j] = v; s2 += v; g2 += v * gw;
                }
            s[i] = wave_sum(s2.x + s2.y) * (1.0f / 1024.0f); gd[i] = wave_sum(g2.x + g2.y);
        }
#pragma unroll
        for (int i = 0; i < 2; ++i) {
            f32x2 v2 = {0.f, 0.f};
#pragma unroll
            for (int j = 0; j < 8; ++j) { const f32x2 d = xf[i][j] - s[i]; v2 += d * d; }
            vs[i] = wave_sum(v2.x + v2.y);
        }
        const float gl = lane ? gd[1] : gd[0], ml = lane ? s[1] : s[0], vl = lane ? vs[1] : vs[0];
        const float rstd = __builtin_amdgcn_rsqf(vl * (1.0f / 1024.0f) + 1e-5f), g = __builtin_amdgcn_rcpf(1.0f + __builtin_amdgcn_exp2f(gl * -1.4426950409f));
        if (lane < 2) *(LAS f32x4*)(smem + LDS_BC + (wid * 2 + lane) * 16) = (f32x4){rstd, -rstd * ml, g, 0.f};
    }
    bf16x8 A0[4], A1[4];
    {
        f32x4 acc[7][4];
        bf16x8 B0[7], B1[7];
        LaneC c = lane_consts<PH_M1>(lb, wm, wn, lane);
        __builtin_amdgcn_s_waitcnt(0xC07F);
        BAR();
        load_frags<PH_M1, 0, 7>(c, A0, B0);
#pragma unroll
        for (int n = 0; n < 7; ++n)
#pragma unroll
            for (int m = 0; m < 4; ++m) acc[n][m] = (f32x4){0.f, 0.f, 0.f, 0.f};
        SCHED0();
_Pragma("unroll 1")
        for (int t = 0; t < TSP; ++t) cstep<PH_M1, 7>(c, SLOTM, acc, A0, B0, A1, B1);
        SCHED0();
        { ONCE() {
            f32x4 cs = {0.f, 0.f, 0.f, 0.f};
            load_frags<PH_M1, 1, 7>(c, A1, B1);
            mma<PH_M1, 7>(acc, A0, B0);
            advance<PH_M1>(c, TSP == 2 ? (unsigned)(-2 * SLOTM) : (unsigned)SLOTM);
            sched_half<18, 28, 5>();
            colsum_half(B0, cs, lane);
            SCHED0();
            __builtin_amdgcn_s_waitcnt(0xC07F);
            BAR();
            SCHED0();
            load_frags<PH_M1, 0, 7>(c, A0, B0);
            mma<PH_M1, 7>(acc, A1, B1);
            sched_half<18, 28>();
            colsum_half(B1, cs, lane);
            SCHED0();
            const int r = lane & 15, nbt = r == 6 ? 6 + wn : r + 8 * wn;
            if (r < 7) *(LAS f32x4*)(smem + LDS_BC + 128 + (nbt * 16 + (lane >> 4) * 4) * 4) = cs;
            SCHED0();
        } }
        int sc = (TSP + 1) % 3;
_Pragma("unroll 1")
        for (int t = TSP + 1; t < 15; ++t) { cstep<PH_M1, 7>(c, sc == 2 ? (unsigned)(-2 * SLOTM) : (unsigned)SLOTM, acc, A0, B0, A1, B1); sc = sc == 2 ? 0 : sc + 1; }
        { ONCE() cstep2<PH_M1, PH_M1, true, 7>(c, c, acc, A0, B0, A1, B1); }
        m1_epilogue(acc, smem, rs, rb, cb, wm, wn, lane);
    }
    u64 tag;
    { const u32x2 tv = *(const LAS u32x2*)(smem + LDS_TAG);
      tag = ((u64)(unsigned)__builtin_amdgcn_readfirstlane(tv.y) << 32) | (unsigned)__builtin_amdgcn_readfirstlane(tv.x); }
    f32x4 sv[4][4];
    bf16x8 B0[4], B1[4];
    const int ch = lane & 15, q = lane >> 4, seg = wm * 4 + q;
    {
#pragma unroll
        for (int n = 0; n < 4; ++n)
#pragma unroll
            for (int m = 0; m < 4; ++m) sv[n][m] = (f32x4){0.f, 0.f, 0.f, 0.f};
        LaneC c = lane_consts<PH_S1>(lb + 2 * SLOT, wm, wn, lane);
        BAR();
        load_frags<PH_S1, 0, 4>(c, A0, B0);
        SCHED0();
#define S1STEP(t) cstep<PH_S1, 4>(c, ringd(t), sv, A0, B0, A1, B1)
_Pragma("unroll 1")
        for (int t = 0; t < 4; ++t) S1STEP(t);
        signal_flag((gu64*)(ws + WS_FM) + (rb * 2 + kh) * 32 + c8 * 4 + wid, tag, lane);
_Pragma("unroll 1")
        for (int t = 4; t < 15; ++t) S1STEP(t);
        f32x2 lam[2];
#pragma unroll
        for (int cbk = 0; cbk < 2; ++cbk) lam[cbk] = *(const LAS f32x2*)(smem + LDS_COEF + (wn * 32 + cbk * 16 + ch) * 16);
        SCHED0();
        { ONCE() cstep2<PH_S1, PH_S1, true, 4>(c, c, sv, A0, B0, A1, B1); }
        asm volatile("s_nop 15");
        SCHED0();
        float er[2], ei[2];
#pragma unroll
        for (int cbk = 0; cbk < 2; ++cbk) { er[cbk] = sv[cbk][0][0]; ei[cbk] = sv[2 + cbk][0][0]; }
#pragma unroll
        for (int i = 1; i < 16; ++i)
#pragma unroll
            for (int cbk = 0; cbk < 2; ++cbk) cfma(lam[cbk].x, lam[cbk].y, er[cbk], ei[cbk], sv[cbk][i >> 2][i & 3], sv[2 + cbk][i >> 2][i & 3]);
#pragma unroll
        for (int cbk = 0; cbk < 2; ++cbk) *(LAS f32x2*)(smem + LDS_E + (seg * 64 + wn * 32 + cbk * 16 + ch) * 8) = (f32x2){er[cbk], ei[cbk]};
    }
    {
        f32x4 acc[4][4];
#pragma unroll
        for (int n = 0; n < 4; ++n)
#pragma unroll
            for (int m = 0; m < 4; ++m) acc[n][m] = (f32x4){0.f, 0.f, 0.f, 0.f};
        LaneC cm = lane_consts<PH_GM>(lb + 2 * SLOT, wm, wn, lane);
        __builtin_amdgcn_s_waitcnt(0xC07F);
        BAR();
        load_frags<PH_GM, 0, 4>(cm, A0, B0);
        SCHED0();
#define GMSTEP(t) cstep<PH_GM, 4>(cm, ringd(t), acc, A0, B0, A1, B1)
_Pragma("unroll 1")
        for (int t = 0; t < 13; ++t) GMSTEP(t);
        {
            f32x4 g2[4], k0[2]; float zr[2], zi[2];
#pragma unroll
            for (int cbk = 0; cbk < 2; ++cbk) {
                const int cl = wn * 32 + cbk * 16 + ch;
                const f32x2 c0 = *(const LAS f32x2*)(smem + LDS_E + (seg * 64 + cl) * 8), ps = *(const LAS f32x2*)(smem + LDS_PSEG + (seg * 64 + cl) * 8);
                const f32x4 sp = *(const LAS f32x4*)(smem + LDS_BC + cl * 32) + *(const LAS f32x4*)(smem + LDS_BC + cl * 32 + 16);
                zr[cbk] = sp[0] + sp[2]; zi[cbk] = sp[1] + sp[3];
                cfma(ps.x, ps.y, zr[cbk], zi[cbk], c0.x, c0.y);
                k0[cbk] = *(const LAS f32x4*)(smem + LDS_COEF + (wn * 32 + cbk * 16 + ch) * 16);
            }
#pragma unroll
            for (int m = 0; m < 4; ++m) g2[m] = *(const LAS f32x4*)(smem + LDS_G2 + (seg * 16 + m * 4) * 4);
            const unsigned so = WS_A2 + (unsigned)(rb * 88 + 2 * cb) * 16384u;
#pragma unroll
            for (int th = 0; th < 2; ++th) {
#pragma unroll
                for (int i = th * 8; i < th * 8 + 8; ++i)
#pragma unroll
                    for (int cbk = 0; cbk < 2; ++cbk) {
                        cfma(k0[cbk][0], k0[cbk][1], zr[cbk], zi[cbk], sv[cbk][i >> 2][i & 3], sv[2 + cbk][i >> 2][i & 3]);
                        sv[cbk][i >> 2][i & 3] = zr[cbk]; sv[2 + cbk][i >> 2][i & 3] = zi[cbk];
                    }
#pragma unroll
                for (int cbk = 0; cbk < 2; ++cbk) {
                    const int kb = 4 * wn + 2 * cbk + (ch >> 3);
                    const unsigned vo = (unsigned)((kb * 8 + seg) * 256 + (((ch & 7) ^ (4 * (kb & 1))) * 16));
                    u32x4 vr, vi;
#pragma unroll
                    for (int mh = 0; mh < 2; ++mh) {
                        const f32x4 re = sv[cbk][th * 2 + mh], im = sv[2 + cbk][th * 2 + mh];
                        const f32x4 hr4 = (re * k0[cbk][2] - im * k0[cbk][3]) * g2[th * 2 + mh], hi4 = (im * -k0[cbk][2] - re * k0[cbk][3]) * g2[th * 2 + mh];
                        vr[mh * 2] = pkbf(hr4[0], hr4[1]); vr[mh * 2 + 1] = pkbf(hr4[2], hr4[3]);
                        vi[mh * 2] = pkbf(hi4[0], hi4[1]); vi[mh * 2 + 1] = pkbf(hi4[2], hi4[3]);
                    }
                    st128(vr, rs, vo + th * 128, so, 16);
                    st128(vi, rs, vo + th * 128, so + 16384u, 16);
                }
            }
            SCHED0();
        }
_Pragma("unroll 1")
        for (int t = 13; t < 16; ++t) GMSTEP(t);
        signal_flag((gu64*)(ws + WS_FS) + (rb * 2 + kh) * 32 + c8 * 4 + wid, tag, lane);
_Pragma("unroll 1")
        for (int t = 16; t < 27; ++t) GMSTEP(t);
        LaneC cs = lane_consts<PH_GS>(lb + 2 * SLOT, wm, wn, lane);
        float og[4];
#pragma unroll
        for (int m = 0; m < 4; ++m) og[m] = 1.0f - *(const LAS float*)(smem + LDS_STAT + (wm * 64 + m * 16 + ch) * 16 + 8);
        SCHED0();
        { ONCE() cstep2<PH_GM, PH_GS, false, 4>(cm, cs, acc, A0, B0, A1, B1); }
#pragma unroll
        for (int n = 0; n < 4; ++n)
#pragma unroll
            for (int m = 0; m < 4; ++m) acc[n][m] *= og[m];
        SCHED0();
_Pragma("unroll 1")
        for (int t = 28; t < 43; ++t) cstep<PH_GS, 4>(cs, ringd(t), acc, A0, B0, A1, B1);
        { ONCE() cstep2<PH_GS, PH_GS, true, 4>(cs, cs, acc, A0, B0, A1, B1); }
        const int r = ch;
        const unsigned xo = WS_XB + (unsigned)(bid * 4 + wid) * 8192u, xi = WS_XB + (unsigned)((bid ^ 1) * 4 + wid) * 8192u;
        if (kh) {
#pragma unroll
            for (int n = 0; n < 4; ++n)
                st128((u32x4){pkbf(acc[n][0][0], acc[n][0][1]), pkbf(acc[n][0][2], acc[n][0][3]), pkbf(acc[n][1][0], acc[n][1][1]), pkbf(acc[n][1][2], acc[n][1][3])}, rs, lane * 16 + n * 1024, xo, 16);
            asm volatile("" ::: "memory");
#pragma unroll
            for (int n = 0; n < 4; ++n) { acc[n][0] = acc[n][2]; acc[n][1] = acc[n][3]; }
        } else {
#pragma unroll
            for (int n = 0; n < 4; ++n)
                st128((u32x4){pkbf(acc[n][2][0], acc[n][2][1]), pkbf(acc[n][2][2], acc[n][2][3]), pkbf(acc[n][3][0], acc[n][3][1]), pkbf(acc[n][3][2], acc[n][3][3])}, rs, lane * 16 + n * 1024, xo, 16);
            asm volatile("; keep" ::: "memory");
        }
        const __amdgpu_buffer_rsrc_t ro = __builtin_amdgcn_make_buffer_rsrc(a.out, 0, 8u << 20, 0x00020000);
        u32x4 pv[4];
        asm volatile("" ::: "memory");
#pragma unroll
        for (int i = 0; i < 4; ++i) pv[i] = __builtin_amdgcn_raw_buffer_load_b128(rs, lane * 16 + i * 1024, xi, 16);
        asm volatile("" ::: "memory");
        f32x4 o[4][2];
#pragma unroll
        for (int mm = 0; mm < 2; ++mm) {
            const float g = *(const LAS float*)(smem + LDS_STAT + (wm * 64 + kh * 32 + mm * 16 + r) * 16 + 8);
#pragma unroll
            for (int n = 0; n < 4; ++n) {
                const u32x2 xw = *(const LAS u32x2*)(smem + 2 * SLOT + (wm * 32 + mm * 16 + r) * 256 + (((wn * 8 + n * 2 + (q >> 1)) ^ r) * 16) + (q & 1) * 8);
                const u32x2 dw = *(const LAS u32x2*)(smem + 2 * SLOT + 16384 + (wn * 64 + n * 16 + q * 4) * 2), bw = *(const LAS u32x2*)(smem + 2 * SLOT + 16384 + 256 + (wn * 64 + n * 16 + q * 4) * 2);
                const f32x4 own = acc[n][mm];
                o[n][mm][0] = own[0] + g * (bflo(xw.x) * bflo(dw.x)) + (1.0f - g) * bflo(bw.x);
                o[n][mm][1] = own[1] + g * (bfhi(xw.x) * bfhi(dw.x)) + (1.0f - g) * bfhi(bw.x);
                o[n][mm][2] = own[2] + g * (bflo(xw.y) * bflo(dw.y)) + (1.0f - g) * bflo(bw.y);
                o[n][mm][3] = own[3] + g * (bfhi(xw.y) * bfhi(dw.y)) + (1.0f - g) * bfhi(bw.y);
                asm volatile("" : "+v"(o[n][mm]));
            }
        }
        {
            const unsigned long long t0 = __builtin_amdgcn_s_memrealtime();
            for (;;) {
                unsigned mx = 0;
#pragma unroll
                for (int i = 0; i < 4; ++i) mx = max(max(mx, max(pv[i][0], pv[i][1])), max(pv[i][2], pv[i][3]));
                if (__all(mx != XSENT) || deadline(t0)) break;
                __builtin_amdgcn_s_sleep(1);
                asm volatile("" ::: "memory");
#pragma unroll
                for (int i = 0; i < 4; ++i) pv[i] = __builtin_amdgcn_raw_buffer_load_b128(rs, lane * 16 + i * 1024, xi, 16);
                asm volatile("" ::: "memory");
            }
#pragma unroll
            for (int n = 0; n < 4; ++n) { const u32x4 v = pv[n];
                o[n][0] += (f32x4){bflo(v[0]), bfhi(v[0]), bflo(v[1]), bfhi(v[1])}; o[n][1] += (f32x4){bflo(v[2]), bfhi(v[2]), bflo(v[3]), bfhi(v[3])}; }
        }
#pragma unroll
        for (int mm = 0; mm < 2; ++mm)
#pragma unroll
            for (int n = 0; n < 4; ++n) *(LAS f32x4*)(smem + wid * 8192 + (mm * 16 + r) * 256 + (((n * 4 + q) ^ r) * 16)) = o[n][mm];
        const unsigned ob = (unsigned)(((rb * 128 + wm * 64 + kh * 32 + q) * 1024 + c8 * 128 + wn * 64 + r * 4) * 4);
#pragma unroll
        for (int i = 0; i < 8; ++i) {
            const u32x4 v = *(const LAS u32x4*)(smem + wid * 8192 + (i * 4 + q) * 256 + ((r ^ ((i * 4 + q) & 15)) * 16));
            __builtin_amdgcn_raw_buffer_store_b128(v, ro, ob + i * 16384, 0, 16);
        }
    }
}
}
extern "C" __global__ void gm_code_end() {}

extern "C" void kernel_launch(void* const* d_in, const int* in_sizes, int n_in,
                              void* d_out, int out_size, void* d_ws, size_t ws_size,
                              hipStream_t stream) {
    using namespace gm;
    Args a;
    a.x = (const u16*)d_in[0]; a.lre = (const u16*)d_in[1]; a.lim = (const u16*)d_in[2]; a.bre = (const u16*)d_in[3]; a.bim = (const u16*)d_in[4];
    a.cre = (const u16*)d_in[5]; a.cim = (const u16*)d_in[6]; a.ldt = (const u16*)d_in[7]; a.dsk = (const u16*)d_in[8];
    a.w1 = (const u16*)d_in[10]; a.b1 = (const u16*)d_in[11]; a.w2 = (const u16*)d_in[12]; a.b2 = (const u16*)d_in[13]; a.gw = (const u16*)d_in[14];
    a.out = (float*)d_out; a.ws = (char*)d_ws;
    (void)hipFuncSetAttribute((const void*)fused_kernel, hipFuncAttributeMaxDynamicSharedMemorySize, LDS_BYTES);
    fused_kernel<<<256, 512, LDS_BYTES, stream>>>(a);
}
```
